```python
import torch

def _softmax(x, axis=-1):
    return torch.softmax(x, dim=axis)
def _gelu(x):
    return torch.nn.functional.gelu(x, approximate="tanh")
def _silu(x):
    return torch.nn.functional.silu(x)
def _sigmoid(x):
    return torch.sigmoid(x)
def _layernorm(x, axis=-1, eps=1e-5):
    m = x.mean(dim=axis, keepdim=True)
    v = x.var(dim=axis, keepdim=True, unbiased=False)
    return (x - m) / torch.sqrt(v + eps)
def _scatter_add(tgt, idx, src, axis=0):
    return tgt.clone().index_add_(axis, idx, src)
def _segment_sum(src, seg, n):
    out = torch.zeros((n,) + src.shape[1:], dtype=src.dtype)
    return out.index_add_(0, seg, src)
def _pad(x, pw):
    flat = tuple(v for pair in reversed(pw) for v in pair)
    return torch.nn.functional.pad(x, flat)

kernel_name = 'recurrent_scan__spectral_fnet__glu_gated_geglu__prompt'

def setup_inputs():

    _w = (lambda a: (lambda t: t.to(torch.bfloat16) if t.is_floating_point() else t)(torch.as_tensor(a)))
    def _A_setup():
        B, S, D = 1, 2048, 640
        ns = torch.arange(S)[:, None] * torch.arange(S)[None, :]
        nd = torch.arange(D)[:, None] * torch.arange(D)[None, :]
        Ws_re = torch.cos(-2.0 * torch.pi * ns / S).to(float)
        Ws_im = torch.sin(-2.0 * torch.pi * ns / S).to(float)
        Wd_re = torch.cos(-2.0 * torch.pi * nd / D).to(float)
        Wd_im = torch.sin(-2.0 * torch.pi * nd / D).to(float)
        return {
            "x": _w((lambda *s: torch.randn(*s, dtype=torch.bfloat16) * 0.05)(B, S, D)),
            "dft_seq_re": _w(Ws_re),
            "dft_seq_im": _w(Ws_im),
            "dft_hid_re": _w(Wd_re),
            "dft_hid_im": _w(Wd_im),
        }
    _A_in = _A_setup()

    def _B_setup():
        B, S, Dm, Df = 1, 2048, 640, 1536
        return {
            "x": _w((lambda *s: torch.randn(*s, dtype=torch.bfloat16) * 0.05)(B, S, Dm)),
            "w_gate": _w((lambda *s: torch.randn(*s, dtype=torch.bfloat16) * 0.05)(Dm, Df) * (Dm ** -0.5)),
            "w_up": _w((lambda *s: torch.randn(*s, dtype=torch.bfloat16) * 0.05)(Dm, Df) * (Dm ** -0.5)),
            "w_down": _w((lambda *s: torch.randn(*s, dtype=torch.bfloat16) * 0.05)(Df, Dm) * (Df ** -0.5)),
        }
    _B_in = _B_setup()

    return {
        "A_x": _A_in["x"],
        "A_dft_seq_re": _A_in["dft_seq_re"],
        "A_dft_seq_im": _A_in["dft_seq_im"],
        "A_dft_hid_re": _A_in["dft_hid_re"],
        "A_dft_hid_im": _A_in["dft_hid_im"],
        "B_x": _B_in["x"],
        "B_w_gate": _B_in["w_gate"],
        "B_w_up": _B_in["w_up"],
        "B_w_down": _B_in["w_down"],
    }

def reference(A_x, A_dft_seq_re, A_dft_seq_im, A_dft_hid_re, A_dft_hid_im, B_x, B_w_gate, B_w_up, B_w_down):
    def _A_ref(x, dft_seq_re, dft_seq_im, dft_hid_re, dft_hid_im):
        a = torch.einsum("bsd,ed->bse", x, dft_hid_re)
        b = torch.einsum("bsd,ed->bse", x, dft_hid_im)
        y = torch.einsum("ts,bse->bte", dft_seq_re, a) - torch.einsum("ts,bse->bte", dft_seq_im, b)
        return y

    def _B_ref(x, w_gate, w_up, w_down):
        g = torch.einsum("bsd,df->bsf", x, w_gate)
        u = torch.einsum("bsd,df->bsf", x, w_up)
        h = _gelu(g) * u
        return torch.einsum("bsf,fd->bsd", h, w_down)

    x = A_x
    xn = _layernorm(x, axis=-1)
    s = _A_ref(xn, A_dft_seq_re, A_dft_seq_im, A_dft_hid_re, A_dft_hid_im)
    g = _silu(_B_ref(xn, B_w_gate, B_w_up, B_w_down))
    return x + s * g
```

```cpp
#include <hip/hip_runtime.h>
#include <hip/hip_bf16.h>
typedef unsigned short bf16_t;
__device__ __forceinline__ float bf2f(bf16_t v){ return __uint_as_float(((unsigned)v)<<16); }
__device__ __forceinline__ bf16_t f2bf(float f){ unsigned u=__float_as_uint(f); u += 0x7fffu + ((u>>16)&1u); return (bf16_t)(u>>16); }
__device__ __forceinline__ float rbf(float f){ return bf2f(f2bf(f)); }
constexpr int S=2048, D=640, F=1536;

__global__ void ln_k(const bf16_t* x, bf16_t* xn){
  int row=blockIdx.x; int t=threadIdx.x;
  float v[10]; float s=0;
  for(int i=0;i<10;++i){ v[i]=bf2f(x[row*D+t+64*i]); s+=v[i]; }
  for(int o=32;o>0;o>>=1) s+=__shfl_xor(s,o);
  float m=s/D; float q=0;
  for(int i=0;i<10;++i){ float d=v[i]-m; q+=d*d; }
  for(int o=32;o>0;o>>=1) q+=__shfl_xor(q,o);
  float var=rbf(q/D); float mb=rbf(m);
  float sd=rbf(sqrtf(rbf(var+1e-5f)));
  for(int i=0;i<10;++i){ xn[row*D+t+64*i]=f2bf(rbf(v[i]-mb)/sd); }
}
template<bool BT> __global__ void gemm_k(const bf16_t* A,const bf16_t* B,bf16_t* C,int M,int N,int K){
  __shared__ float As[16][17], Bs[16][17];
  int tx=threadIdx.x, ty=threadIdx.y; int row=blockIdx.y*16+ty, col=blockIdx.x*16+tx; float acc=0;
  for(int k0=0;k0<K;k0+=16){
    As[ty][tx]=bf2f(A[(size_t)row*K+k0+tx]);
    if(BT) Bs[tx][ty]=bf2f(B[(size_t)(blockIdx.x*16+ty)*K+k0+tx]); else Bs[ty][tx]=bf2f(B[(size_t)(k0+ty)*N+col]);
    __syncthreads();
    for(int k=0;k<16;++k) acc+=As[ty][k]*Bs[k][tx];
    __syncthreads();
  }
  C[(size_t)row*N+col]=f2bf(acc);
}
__global__ void glu_k(const bf16_t* g,const bf16_t* u,bf16_t* h,int n){
  int i=blockIdx.x*256+threadIdx.x; if(i>=n) return;
  float x=bf2f(g[i]); float t=tanhf(0.7978845608f*(x+0.044715f*x*x*x)); float ge=rbf(0.5f*x*(1.f+t));
  h[i]=f2bf(ge*bf2f(u[i]));
}
__global__ void fin_k(const bf16_t* x,const bf16_t* yr,const bf16_t* yi,const bf16_t* d,bf16_t* out,int n){
  int i=blockIdx.x*256+threadIdx.x; if(i>=n) return;
  float s=rbf(bf2f(yr[i])-bf2f(yi[i])); float dv=bf2f(d[i]); float g=rbf(dv/(1.f+__expf(-dv)));
  out[i]=f2bf(bf2f(x[i])+rbf(s*g));
}
extern "C" void kernel_launch(void* const* d_in, const int* in_sizes, int n_in, void* d_out, int out_size, void* d_ws, size_t ws_size, hipStream_t stream){
  const bf16_t* x=(const bf16_t*)d_in[0]; const bf16_t* wsr=(const bf16_t*)d_in[1]; const bf16_t* wsi=(const bf16_t*)d_in[2];
  const bf16_t* wdr=(const bf16_t*)d_in[3]; const bf16_t* wdi=(const bf16_t*)d_in[4];
  const bf16_t* wg=(const bf16_t*)d_in[6]; const bf16_t* wu=(const bf16_t*)d_in[7]; const bf16_t* wd=(const bf16_t*)d_in[8];
  bf16_t* ws=(bf16_t*)d_ws; bf16_t* xn=ws; bf16_t* a=xn+S*D; bf16_t* b=a+S*D; bf16_t* g=b+S*D; bf16_t* u=g+S*F; bf16_t* h=u+S*F; bf16_t* yr=h+S*F; bf16_t* yi=yr+S*D; bf16_t* d=yi+S*D;
  ln_k<<<S,64,0,stream>>>(x,xn);
  dim3 bl(16,16);
  gemm_k<true><<<dim3(D/16,S/16),bl,0,stream>>>(xn,wdr,a,S,D,D);
  gemm_k<true><<<dim3(D/16,S/16),bl,0,stream>>>(xn,wdi,b,S,D,D);
  gemm_k<false><<<dim3(F/16,S/16),bl,0,stream>>>(xn,wg,g,S,F,D);
  gemm_k<false><<<dim3(F/16,S/16),bl,0,stream>>>(xn,wu,u,S,F,D);
  glu_k<<<S*F/256,256,0,stream>>>(g,u,h,S*F);
  gemm_k<false><<<dim3(D/16,S/16),bl,0,stream>>>(wsr,a,yr,S,D,S);
  gemm_k<false><<<dim3(D/16,S/16),bl,0,stream>>>(wsi,b,yi,S,D,S);
  gemm_k<false><<<dim3(D/16,S/16),bl,0,stream>>>(h,wd,d,S,D,F);
  fin_k<<<S*D/256,256,0,stream>>>(x,yr,yi,d,(bf16_t*)d_out,S*D);
}
```

```cpp
#include <hip/hip_runtime.h>
#include <hip/hip_bf16.h>
typedef unsigned short bf16_t;
typedef short bf16x8 __attribute__((ext_vector_type(8)));
typedef short s16x4 __attribute__((ext_vector_type(4)));
typedef float f32x4 __attribute__((ext_vector_type(4)));
typedef float f32x2 __attribute__((ext_vector_type(2)));
typedef unsigned u32x4 __attribute__((ext_vector_type(4)));
typedef unsigned u32x2 __attribute__((ext_vector_type(2)));
typedef __bf16 bf16x2_t __attribute__((ext_vector_type(2)));
#define LAS __attribute__((address_space(3)))
#define DI __device__ __forceinline__

#define STAMP 0
#define K1_NOSTATS 0

constexpr int S = 2048, D = 640, F = 1536;

DI unsigned pk_bf16(float lo, float hi) { f32x2 v = {lo, hi}; return __builtin_bit_cast(unsigned, __builtin_convertvector(v, bf16x2_t)); }
DI float rbf(float f) { return __uint_as_float(pk_bf16(f, f) << 16); }
DI float bflo(unsigned u) { return __uint_as_float(u << 16); }
DI float bfhi(unsigned u) { return __uint_as_float(u & 0xffff0000u); }
DI float dot2(unsigned a, unsigned b, float c) { return __builtin_amdgcn_fdot2_f32_bf16(__builtin_bit_cast(bf16x2_t, a), __builtin_bit_cast(bf16x2_t, b), c, false); }
DI void glds16(unsigned ldsaddr, unsigned voff, const char* base) {
  asm volatile("s_mov_b32 m0, %0\n\ts_nop 0\n\tglobal_load_lds_dwordx4 %1, %2" :: "s"(ldsaddr), "v"(voff), "s"(base) : "memory");
}
template <int N> DI void wait_v() { asm volatile("s_waitcnt vmcnt(%0)" :: "i"(N) : "memory"); }
#define WAIT_L0() asm volatile("s_waitcnt lgkmcnt(0)" ::: "memory")
#define BAR() do { asm volatile("" ::: "memory"); __builtin_amdgcn_s_barrier(); asm volatile("" ::: "memory"); } while (0)
#define SGB(m, n) __builtin_amdgcn_sched_group_barrier(m, n, 0)
#define MFMA16(a, b, c) __builtin_amdgcn_mfma_f32_16x16x32_bf16(a, b, c, 0, 0, 0)
#define STAMP_AT(i)
#define STAMP_ARG

constexpr int K1_STG = 53248, K1_NB = 3, K1_TAB = K1_NB * K1_STG;
constexpr int K1_LDS = K1_TAB + 4096;
constexpr int T_RS = K1_TAB, T_RM = K1_TAB + 512, T_CS = K1_TAB + 1024;

template <bool TG>
DI void k1_body(const bf16_t* __restrict__ x, const bf16_t* __restrict__ wdft, const bf16_t* __restrict__ wg, const bf16_t* __restrict__ wu,
                bf16_t* __restrict__ AB, bf16_t* __restrict__ H, LAS char* smem, int tid, int w, int mt, int hcol0, int m8 STAMP_ARG) {
  const int lane = tid & 63;
  constexpr int NL = TG ? 12 : 13;
  if (w >= 4) {
    const int lw = w & 3, rr = lane >> 3, c8 = lane & 7;
    const unsigned lds0 = (unsigned)(size_t)smem;
    const unsigned voffX = (8 * lw + rr) * 1280 + ((c8 ^ ((rr & 2) | ((lw & 2) << 1))) * 16);
    const unsigned voffW = (8 * lw + rr) * 3072 + ((c8 ^ ((rr & 2) | ((lw & 1) << 2))) * 16);
    const unsigned voffD = (8 * lw + rr) * 1280 + ((c8 ^ rr) * 16);
    const char* bx = (const char*)x + (size_t)mt * 128 * 1280;
    const char* bg = (const char*)wg + hcol0 * 2;
    const char* bu = (const char*)wu + hcol0 * 2;
    const char* bd = (const char*)wdft + (size_t)(m8 & 3) * 160 * 1280;
    auto issue = [&](int kt, unsigned sb) {
      const unsigned buf = lds0 + sb + lw * 1024;
#pragma unroll
      for (int j = 0; j < 4; ++j) glds16(buf + j * 4096, voffX, bx + kt * 128 + j * (32 * 1280));
      const int wo = kt * (64 * 3072);
      if (TG) {
#pragma unroll
        for (int im = 0; im < 2; ++im)
#pragma unroll
          for (int j = 0; j < 2; ++j) {
            glds16(buf + 16384 + im * 8192 + j * 4096, voffW, bg + wo + j * (32 * 3072) + im * 128);
            glds16(buf + 32768 + im * 8192 + j * 4096, voffW, bu + wo + j * (32 * 3072) + im * 128);
          }
      } else {
#pragma unroll
        for (int j = 0; j < 2; ++j) {
          glds16(buf + 16384 + j * 4096, voffW, bg + wo + j * (32 * 3072));
          glds16(buf + 24576 + j * 4096, voffW, bu + wo + j * (32 * 3072));
        }
#pragma unroll
        for (int j = 0; j < 5; ++j) glds16(buf + 32768 + j * 4096, voffD, bd + kt * 128 + j * (32 * 1280));
      }
    };
    issue(0, 0); issue(1, K1_STG); issue(2, 2 * K1_STG);
    float sx[4] = {0, 0, 0, 0}, sq[4] = {0, 0, 0, 0}, cw[8] = {0, 0, 0, 0, 0, 0, 0, 0}, cd[5] = {0, 0, 0, 0, 0};
    unsigned ONE2 = 0x3f803f80u, SLO = 0x00003f80u, SHI = 0x3f800000u;
    asm volatile("" : "+v"(ONE2), "+v"(SLO), "+v"(SHI));
    const unsigned aW0 = rr * 128 + ((c8 ^ (rr & 2)) * 16), aW1 = aW0 ^ 64;
    unsigned sb = 0, sbn = 0;
    for (int k = 0; k < 10; ++k) {
      if (k == 0) wait_v<2 * NL>(); else if (k < 9) wait_v<NL>(); else wait_v<0>();
      BAR();
      if (k >= 1 && k <= 7) { issue(k + 2, sbn); }
      sbn = sb;
      {
        LAS char* p = smem + sb;
        u32x4 vx[4];
#pragma unroll
        for (int i = 0; i < 4; ++i) vx[i] = *(LAS u32x4*)(p + lw * 4096 + i * 1024 + lane * 16);
        constexpr int NW = TG ? 8 : 4;
        u32x4 vw[NW];
        LAS char* pw = p + 16384 + (TG ? lw * 8192 : lw * 4096);
#pragma unroll
        for (int i = 0; i < NW; ++i) vw[i] = *(LAS u32x4*)(pw + i * 1024 + ((i & 1) ? aW1 : aW0));
#pragma unroll
        for (int i = 0; i < 4; ++i)
#pragma unroll
          for (int e = 0; e < 4; ++e) { sx[i] = dot2(vx[i][e], ONE2, sx[i]); sq[i] = dot2(vx[i][e], vx[i][e], sq[i]); }
#pragma unroll
        for (int i = 0; i < NW; ++i)
#pragma unroll
          for (int e = 0; e < 4; ++e) { cw[2 * e] = dot2(vw[i][e], SLO, cw[2 * e]); cw[2 * e + 1] = dot2(vw[i][e], SHI, cw[2 * e + 1]); }
        if (!TG) {
          u32x4 vd[5];
#pragma unroll
          for (int i = 0; i < 5; ++i) vd[i] = *(LAS u32x4*)(p + 32768 + (lw * 5 + i) * 1024 + lane * 16);
#pragma unroll
          for (int i = 0; i < 5; ++i)
#pragma unroll
            for (int e = 0; e < 4; ++e) cd[i] = dot2(vd[i][e], ONE2, cd[i]);
        }
        WAIT_L0();
      }
      sb += K1_STG; if (sb == K1_NB * K1_STG) sb = 0;
    }
#pragma unroll
    for (int i = 0; i < 4; ++i) {
#pragma unroll
      for (int o = 1; o < 8; o <<= 1) { sx[i] += __shfl_xor(sx[i], o); sq[i] += __shfl_xor(sq[i], o); }
      const float mean = sx[i] * (1.0f / D);
      const float var = fmaxf(sq[i] * (1.0f / D) - mean * mean, 0.f);
      const float sd = rbf(sqrtf(rbf(rbf(var) + 1e-5f)));
      const float rs = 1.0f / sd;
      if (c8 == 0) { const int row = lw * 32 + i * 8 + rr; *(LAS float*)(smem + T_RS + row * 4) = rs; *(LAS float*)(smem + T_RM + row * 4) = rs * rbf(mean); }
    }
#pragma unroll
    for (int e = 0; e < 8; ++e) {
#pragma unroll
      for (int o = 8; o < 64; o <<= 1) cw[e] += __shfl_xor(cw[e], o);
    }
    if (rr == 0) {
      *(LAS f32x4*)(smem + T_CS + lw * 256 + c8 * 32) = (f32x4){cw[0], cw[1], cw[2], cw[3]};
      *(LAS f32x4*)(smem + T_CS + lw * 256 + c8 * 32 + 16) = (f32x4){cw[4], cw[5], cw[6], cw[7]};
    }
    if (!TG) {
#pragma unroll
      for (int i = 0; i < 5; ++i) {
#pragma unroll
        for (int o = 1; o < 8; o <<= 1) cd[i] += __shfl_xor(cd[i], o);
        if (c8 == 0) *(LAS float*)(smem + T_CS + 1024 + ((lw * 5 + i) * 8 + rr) * 4) = cd[i];
      }
    }
    WAIT_L0();
    BAR();
    return;
  }
  const int wm = w & 1, wn = w >> 1, q = lane >> 4, rho = lane & 15;
  constexpr int NC = TG ? 4 : 2;
  const unsigned vX0 = wm * 8192 + (8 * (rho >> 2) + (rho & 3)) * 128 + ((q ^ ((rho & 2) | ((rho >> 1) & 4))) * 16);
  const unsigned vX1 = vX0 ^ 64;
  unsigned vW[NC];
  {
    const int qq = rho >> 2, p = lane & 3, swz = (qq & 2) | ((q & 1) << 2);
#pragma unroll
    for (int c = 0; c < NC; ++c) {
      const int cc = TG ? c : (2 * wn + c);
      vW[c] = 16384 + (TG ? wn * 8192 : 0) + (8 * q + qq) * 128 + ((((2 * cc) ^ swz) + (p >> 1)) * 16) + 8 * (p & 1);
    }
  }
  constexpr int UOFF = TG ? 16384 : 8192;
  const unsigned vD0 = 32768 + (80 * wn + rho) * 128 + ((q ^ (rho & 7)) * 16), vD1 = vD0 ^ 64;
  f32x4 aG[NC][4], aU[NC][4], aA[4][5];
#pragma unroll
  for (int i = 0; i < 4; ++i) {
#pragma unroll
    for (int c = 0; c < NC; ++c) { aG[c][i] = (f32x4){0, 0, 0, 0}; aU[c][i] = (f32x4){0, 0, 0, 0}; asm volatile("" : "+v"(aG[c][i]), "+v"(aU[c][i])); }
    if (!TG) {
#pragma unroll
      for (int j = 0; j < 5; ++j) { aA[i][j] = (f32x4){0, 0, 0, 0}; asm volatile("" : "+v"(aA[i][j])); }
    }
  }
  bf16x8 FX[2][4], FG[2][NC], FU[2][NC], FD[2][5];
  auto trd = [&](LAS char* p) -> bf16x8 {
    const s16x4 lo = __builtin_amdgcn_ds_read_tr16_b64_v4i16((LAS s16x4*)p);
    const s16x4 hi = __builtin_amdgcn_ds_read_tr16_b64_v4i16((LAS s16x4*)(p + 512));
    return __builtin_shufflevector(lo, hi, 0, 1, 2, 3, 4, 5, 6, 7);
  };
  auto rd = [&](int s, unsigned sb, int ks) {
    LAS char* px = smem + sb + (ks ? vX1 : vX0);
    FG[s][0] = trd(smem + sb + vW[0] + ks * 4096);
#pragma unroll
    for (int i = 0; i < 4; ++i) FX[s][i] = *(LAS bf16x8*)(px + (i >> 1) * 4096 + (i & 1) * 512);
    FU[s][0] = trd(smem + sb + vW[0] + ks * 4096 + UOFF);
#pragma unroll
    for (int c = 1; c < NC; ++c) { FG[s][c] = trd(smem + sb + vW[c] + ks * 4096); FU[s][c] = trd(smem + sb + vW[c] + ks * 4096 + UOFF); }
    if (!TG) {
      LAS char* pd = smem + sb + (ks ? vD1 : vD0);
#pragma unroll
      for (int j = 0; j < 5; ++j) FD[s][j] = *(LAS bf16x8*)(pd + j * 2048);
    }
  };
  auto mma = [&](int s) {
#pragma unroll
    for (int c = 0; c < NC; ++c) {
#pragma unroll
      for (int i = 0; i < 4; ++i) aG[c][i] = MFMA16(FG[s][c], FX[s][i], aG[c][i]);
#pragma unroll
      for (int i = 0; i < 4; ++i) aU[c][i] = MFMA16(FU[s][c], FX[s][i], aU[c][i]);
    }
    if (!TG) {
#pragma unroll
      for (int j = 0; j < 5; ++j)
#pragma unroll
        for (int i = 0; i < 4; ++i) aA[i][j] = MFMA16(FX[s][i], FD[s][j], aA[i][j]);
    }
  };
  auto sched = [&]() {
    if (TG) {
#pragma unroll
      for (int i = 0; i < 20; ++i) { SGB(0x8, 1); SGB(0x100, 1); }
      SGB(0x8, 12);
    } else {
#pragma unroll
      for (int i = 0; i < 8; ++i) { SGB(0x8, 2); SGB(0x100, 1); SGB(0x8, 1); SGB(0x100, 1); }
      SGB(0x8, 2); SGB(0x100, 1);
      SGB(0x8, 10);
    }
  };
  BAR();
  STAMP_AT(1);
  unsigned sb = 0;
  rd(0, sb, 0);
#pragma unroll 1
  for (int k = 0; k < 10; ++k) {
    rd(1, sb, 1); mma(0); sched();
    __builtin_amdgcn_sched_barrier(0);
    sb += K1_STG; if (sb == K1_NB * K1_STG) sb = 0;
    WAIT_L0();
    if (k < 9) BAR();
    rd(0, sb, 0); mma(1); sched();
    __builtin_amdgcn_sched_barrier(0);
  }
  BAR();
  STAMP_AT(2);
  {
    float rs[4], rm[4];
#pragma unroll
    for (int i = 0; i < 4; ++i) {
      const int row = 64 * wm + 32 * (i >> 1) + 8 * (rho >> 2) + 4 * (i & 1) + (rho & 3);
      rs[i] = *(LAS float*)(smem + T_RS + row * 4); rm[i] = *(LAS float*)(smem + T_RM + row * 4);
    }
    const float K0 = -2.0f * 0.7978845608028654f * 1.4426950408889634f, K1 = K0 * 0.044715f;
#pragma unroll
    for (int J = 0; J < NC / 2; ++J) {
      f32x4 cg[2], cu[2];
#pragma unroll
      for (int t = 0; t < 2; ++t) {
        const int c = 2 * J + t;
        if (TG) {
          cg[t] = *(LAS f32x4*)(smem + T_CS + wn * 256 + c * 64 + q * 16);
          cu[t] = *(LAS f32x4*)(smem + T_CS + 512 + wn * 256 + c * 64 + q * 16);
        } else {
          const int o = (2 * wn + c) * 64 + q * 16;
          cg[t] = *(LAS f32x4*)(smem + T_CS + o) + *(LAS f32x4*)(smem + T_CS + 256 + o);
          cu[t] = *(LAS f32x4*)(smem + T_CS + 512 + o) + *(LAS f32x4*)(smem + T_CS + 768 + o);
        }
      }
      const int f0 = hcol0 + (TG ? wn * 64 + J * 32 : wn * 32);
#pragma unroll
      for (int i = 0; i < 4; ++i) {
        u32x4 o;
#pragma unroll
        for (int t = 0; t < 2; ++t) {
          const f32x4 g = aG[2 * J + t][i] * rs[i] - cg[t] * rm[i];
          const f32x4 u = aU[2 * J + t][i] * rs[i] - cu[t] * rm[i];
          const f32x4 z = g * (g * g * K1 + K0);
          f32x4 hv;
#pragma unroll
          for (int r = 0; r < 4; ++r) hv[r] = __builtin_amdgcn_rcpf(1.0f + __builtin_amdgcn_exp2f(z[r]));
          hv = hv * g * u;
          o[2 * t] = pk_bf16(hv[0], hv[1]); o[2 * t + 1] = pk_bf16(hv[2], hv[3]);
        }
        const int rb = 4 * wm + 2 * (i >> 1) + (rho >> 3), pos = 8 * ((rho >> 2) & 1) + 4 * (i & 1) + (rho & 3) + 16 * q;
        char* dst = (char*)H + ((size_t)((mt * 24 + (f0 >> 6)) * 8 + rb) * 2 + ((f0 >> 5) & 1)) * 1024 + pos * 16;
        *(u32x4*)dst = o;
      }
    }
    if (!TG) {
      const float sgn = (m8 >= 4) ? -1.0f : 1.0f;
      float cs[5];
#pragma unroll
      for (int j = 0; j < 5; ++j) cs[j] = *(LAS float*)(smem + T_CS + 1024 + (80 * wn + 16 * j + rho) * 4);
#pragma unroll
      for (int I = 0; I < 2; ++I) {
        f32x4 r4[2], m4[2];
#pragma unroll
        for (int t = 0; t < 2; ++t) {
          const int row = 64 * wm + 32 * I + 8 * q + 4 * t;
          r4[t] = *(LAS f32x4*)(smem + T_RS + row * 4) * sgn; m4[t] = *(LAS f32x4*)(smem + T_RM + row * 4) * sgn;
        }
        const int kt = (m8 >= 4 ? 32 : 0) + mt * 2 + wm;
        char* dst = (char*)AB + ((size_t)(((m8 & 3) * 64 + kt) * 2 + I) * 10 + 5 * wn) * 1024 + lane * 16;
#pragma unroll
        for (int j = 0; j < 5; ++j) {
          const f32x4 v0 = aA[2 * I][j] * r4[0] - m4[0] * cs[j], v1 = aA[2 * I + 1][j] * r4[1] - m4[1] * cs[j];
          const u32x4 o = {pk_bf16(v0[0], v0[1]), pk_bf16(v0[2], v0[3]), pk_bf16(v1[0], v1[1]), pk_bf16(v1[2], v1[3])};
          *(u32x4*)(dst + j * 1024) = o;
        }
      }
    }
  }
  STAMP_AT(3);
}

__global__ __launch_bounds__(512, 2)
void k1(const bf16_t* __restrict__ x, const bf16_t* __restrict__ wdr, const bf16_t* __restrict__ wdi, const bf16_t* __restrict__ wg,
        const bf16_t* __restrict__ wu, const bf16_t* __restrict__ wdn, bf16_t* __restrict__ AB, bf16_t* __restrict__ H, bf16_t* __restrict__ WD STAMP_ARG) {
  extern __shared__ __attribute__((aligned(16))) char smem_[];
  LAS char* smem = (LAS char*)smem_;
  const int tid = threadIdx.x, w = __builtin_amdgcn_readfirstlane(tid >> 6);
  const int bid = blockIdx.x, xcd = bid & 7, idx = bid >> 3;
  STAMP_AT(0);
  const int mt = (xcd & 1) * 8 + (idx & 7), j = idx >> 3, n8 = (xcd >> 1) * 2 + (j >> 1);
  if (w < 4) {
    const int task = bid * 4 + w;
    if (task < 960) {
      const int lane = tid & 63, q = lane >> 4, n2 = lane & 15;
      const int fb = task / 20, nb2 = task % 20;
      const unsigned* src = (const unsigned*)(wdn + (size_t)(fb * 32 + 4 * q) * D + nb2 * 32 + 2 * n2);
      unsigned v[8];
#pragma unroll
      for (int e = 0; e < 8; ++e) v[e] = src[((e & 3) + (e >> 2) * 16) * (D / 2)];
      u32x4 lo, hi;
#pragma unroll
      for (int e = 0; e < 4; ++e) { lo[e] = __builtin_amdgcn_perm(v[2 * e + 1], v[2 * e], 0x05040100u); hi[e] = __builtin_amdgcn_perm(v[2 * e + 1], v[2 * e], 0x07060302u); }
      const int n16 = nb2 * 2 + (n2 >> 3), nt = n16 / 10, nb = n16 % 10;
      char* dst = (char*)WD + ((size_t)((nt * 24 + (fb >> 1)) * 2 + (fb & 1)) * 10 + nb) * 1024 + (((2 * n2) & 15) + 16 * q) * 16;
      *(u32x4*)dst = lo; *(u32x4*)(dst + 16) = hi;
    }
  }
  if (j & 1) k1_body<false>(x, n8 >= 4 ? wdi : wdr, wg, wu, AB, H, smem, tid, w, mt, 1024 + 64 * n8, n8);
  else       k1_body<true>(x, wdr, wg, wu, AB, H, smem, tid, w, mt, 128 * n8, n8);
}

constexpr int A_ST = 16384, B_ST = 20480, K2_STG = A_ST + B_ST, K2_NB = 4, K2_LDS = K2_NB * K2_STG;

__global__ __launch_bounds__(512, 2)
void k2(const bf16_t* __restrict__ Wre, const bf16_t* __restrict__ Wim, const bf16_t* __restrict__ AB, const bf16_t* __restrict__ H,
        const bf16_t* __restrict__ WD, bf16_t* __restrict__ P STAMP_ARG) {
  extern __shared__ __attribute__((aligned(16))) char smem_[];
  LAS char* smem = (LAS char*)smem_;
  const int tid = threadIdx.x, lane = tid & 63, w = __builtin_amdgcn_readfirstlane(tid >> 6);
  const int bid = blockIdx.x, xcd = bid & 7, idx = bid >> 3;
  STAMP_AT(0);
  const int split = xcd >> 1, mt = (xcd & 1) * 8 + (idx >> 2), nt = idx & 3;
  const int kt0 = split == 3 ? 0 : split * 21;
  const int nkt = split == 3 ? 24 : (split == 2 ? 22 : 21);
  if (w >= 4) {
    const int lw = w & 3;
    const unsigned lds0 = (unsigned)(size_t)smem;
    unsigned voffA; const unsigned voffB = lw * 1024 + lane * 16;
    const char* baseA; const char* baseA2; const char* baseB; unsigned kstrA, jstrA;
    if (split < 3) {
      const int rr = lane >> 3;
      voffA = (8 * lw + rr) * 4096 + (((lane & 7) ^ rr) * 16);
      baseA = (const char*)Wre + (size_t)mt * 128 * 4096;
      baseA2 = (const char*)Wim + (size_t)mt * 128 * 4096 - 32 * 128;
      kstrA = 128; jstrA = 32 * 4096;
      baseB = (const char*)AB + (size_t)nt * 64 * B_ST;
    } else {
      voffA = voffB;
      baseA = (const char*)H + (size_t)mt * 24 * A_ST; baseA2 = baseA;
      kstrA = A_ST; jstrA = 4096;
      baseB = (const char*)WD + (size_t)nt * 24 * B_ST;
    }
    auto issue = [&](int k) {
      const int kt = kt0 + k; const unsigned buf = lds0 + (k % K2_NB) * K2_STG + lw * 1024;
      const char* a = (kt < 32 ? baseA : baseA2) + (size_t)kt * kstrA;
      const char* b = baseB + (size_t)kt * B_ST;
#pragma unroll
      for (int j = 0; j < 4; ++j) glds16(buf + j * 4096, voffA, a + (size_t)j * jstrA);
#pragma unroll
      for (int j = 0; j < 5; ++j) glds16(buf + A_ST + j * 4096, voffB, b + j * 4096);
    };
#pragma unroll 1
    for (int k = 0; k < K2_NB; ++k) issue(k);
    int issued = K2_NB;
#pragma unroll 1
    for (int k = 0; k < nkt; ++k) {
      const int o = issued - 1 - k;
      if (o >= 3) wait_v<27>(); else if (o == 2) wait_v<18>(); else if (o == 1) wait_v<9>(); else wait_v<0>();
      BAR();
      if (k >= 1 && issued < nkt) { issue(issued); ++issued; }
    }
    BAR();
    return;
  }
  const int wm = w & 1, wn = w >> 1, q = lane >> 4, r15 = lane & 15;
  const unsigned vA0 = split < 3 ? (wm * 8192 + r15 * 128 + ((q ^ (r15 & 7)) * 16)) : (wm * 8192 + lane * 16);
  const unsigned vA1 = split < 3 ? (vA0 ^ 64) : (vA0 + 1024);
  const unsigned vB = A_ST + wn * 5120 + lane * 16;
  f32x4 acc[4][5];
#pragma unroll
  for (int i = 0; i < 4; ++i)
#pragma unroll
    for (int j = 0; j < 5; ++j) { acc[i][j] = (f32x4){0, 0, 0, 0}; asm volatile("" : "+v"(acc[i][j])); }
  bf16x8 FA[2][4], FB[2][5];
  auto rd = [&](int s, unsigned sb, int ks) {
    LAS char* pb = smem + sb + vB + ks * 10240; LAS char* pa = smem + sb + (ks ? vA1 : vA0);
    FB[s][0] = *(LAS bf16x8*)(pb);
#pragma unroll
    for (int i = 0; i < 4; ++i) FA[s][i] = *(LAS bf16x8*)(pa + i * 2048);
#pragma unroll
    for (int j = 1; j < 5; ++j) FB[s][j] = *(LAS bf16x8*)(pb + j * 1024);
  };
  auto mma = [&](int s) {
#pragma unroll
    for (int j = 0; j < 5; ++j)
#pragma unroll
      for (int i = 0; i < 4; ++i) acc[i][j] = MFMA16(FB[s][j], FA[s][i], acc[i][j]);
  };
  auto sched = [&]() {
#pragma unroll
    for (int i = 0; i < 9; ++i) { SGB(0x8, 1); SGB(0x100, 1); }
    SGB(0x8, 11);
  };
  BAR();
  STAMP_AT(1);
  unsigned sb = 0;
  rd(0, sb, 0);
#pragma unroll 1
  for (int k = 0; k < nkt; ++k) {
    rd(1, sb, 1); mma(0); sched();
    __builtin_amdgcn_sched_barrier(0);
    sb += K2_STG; if (sb == K2_NB * K2_STG) sb = 0;
    WAIT_L0();
    BAR();
    rd(0, sb, 0); mma(1); sched();
    __builtin_amdgcn_sched_barrier(0);
  }
  STAMP_AT(2);
  char* pout = (char*)P + ((size_t)((split * 64 + mt * 4 + nt) * 4 + w) * 10) * 1024 + lane * 16;
#pragma unroll
  for (int ip = 0; ip < 2; ++ip)
#pragma unroll
    for (int j = 0; j < 5; ++j) {
      const f32x4 v0 = acc[2 * ip][j], v1 = acc[2 * ip + 1][j];
      const u32x4 o = {pk_bf16(v0[0], v0[1]), pk_bf16(v0[2], v0[3]), pk_bf16(v1[0], v1[1]), pk_bf16(v1[2], v1[3])};
      *(u32x4*)(pout + (ip * 5 + j) * 1024) = o;
    }
  STAMP_AT(3);
}

__global__ __launch_bounds__(256)
void k3(const bf16_t* __restrict__ x, const bf16_t* __restrict__ P, bf16_t* __restrict__ out) {
  const int gw = blockIdx.x * 4 + (threadIdx.x >> 6), lane = threadIdx.x & 63;
  const int c = gw % 10, tw = gw / 10, w = tw & 3, tile = tw >> 2, mt = tile >> 2, nt = tile & 3;
  const int ip = c / 5, j = c % 5, wm = w & 1, wn = w >> 1, q = lane >> 4, r15 = lane & 15;
  const char* p = (const char*)P + (size_t)gw * 1024 + lane * 16;
  u32x4 v[4];
#pragma unroll
  for (int s = 0; s < 4; ++s) v[s] = *(const u32x4*)(p + (size_t)s * (S * D * 2));
  const int col = nt * 160 + wn * 80 + 16 * j + 4 * q;
#pragma unroll
  for (int t = 0; t < 2; ++t) {
    const int row = mt * 128 + wm * 64 + (2 * ip + t) * 16 + r15;
    const u32x2 xv = *(const u32x2*)(x + (size_t)row * D + col);
    u32x2 o;
#pragma unroll
    for (int e = 0; e < 2; ++e) {
      const int d = 2 * t + e;
      const float y0 = bflo(v[0][d]) + bflo(v[1][d]) + bflo(v[2][d]), y1 = bfhi(v[0][d]) + bfhi(v[1][d]) + bfhi(v[2][d]);
      const float d0 = bflo(v[3][d]), d1 = bfhi(v[3][d]);
      const float g0 = d0 * __builtin_amdgcn_rcpf(1.0f + __builtin_amdgcn_exp2f(-1.4426950408889634f * d0));
      const float g1 = d1 * __builtin_amdgcn_rcpf(1.0f + __builtin_amdgcn_exp2f(-1.4426950408889634f * d1));
      o[e] = pk_bf16(bflo(xv[e]) + y0 * g0, bfhi(xv[e]) + y1 * g1);
    }
    *(u32x2*)(out + (size_t)row * D + col) = o;
  }
}

extern "C" void kernel_launch(void* const* d_in, const int* in_sizes, int n_in, void* d_out, int out_size, void* d_ws, size_t ws_size, hipStream_t stream) {
  const bf16_t* x = (const bf16_t*)d_in[0]; const bf16_t* wsr = (const bf16_t*)d_in[1]; const bf16_t* wsi = (const bf16_t*)d_in[2];
  const bf16_t* wdr = (const bf16_t*)d_in[3]; const bf16_t* wdi = (const bf16_t*)d_in[4];
  const bf16_t* wg = (const bf16_t*)d_in[6]; const bf16_t* wu = (const bf16_t*)d_in[7]; const bf16_t* wdn = (const bf16_t*)d_in[8];
  bf16_t* AB = (bf16_t*)d_ws; bf16_t* H = AB + 2 * S * D; bf16_t* WD = H + S * F; bf16_t* P = WD + F * D;
  (void)hipFuncSetAttribute((const void*)k1, hipFuncAttributeMaxDynamicSharedMemorySize, K1_LDS);
  (void)hipFuncSetAttribute((const void*)k2, hipFuncAttributeMaxDynamicSharedMemorySize, K2_LDS);
  k1<<<256, 512, K1_LDS, stream>>>(x, wdr, wdi, wg, wu, wdn, AB, H, WD);
  k2<<<256, 512, K2_LDS, stream>>>(wsr, wsi, AB, H, WD, P);
  k3<<<640, 256, 0, stream>>>(x, P, (bf16_t*)d_out);
}
```

```cpp
#include <hip/hip_runtime.h>
#include <hip/hip_bf16.h>
typedef unsigned short bf16_t;
typedef unsigned long long u64;
typedef short bf16x8 __attribute__((ext_vector_type(8)));
typedef short s16x4 __attribute__((ext_vector_type(4)));
typedef float f32x4 __attribute__((ext_vector_type(4)));
typedef float f32x2 __attribute__((ext_vector_type(2)));
typedef unsigned u32x4 __attribute__((ext_vector_type(4)));
typedef unsigned u32x2 __attribute__((ext_vector_type(2)));
typedef __bf16 bf16x2_t __attribute__((ext_vector_type(2)));
#define LAS __attribute__((address_space(3)))
#define GAS __attribute__((address_space(1)))
#define DI __device__ __forceinline__

#define STAMP 0
#define NPRE 2
#define ROT2 1
#define YFL 2
#define XROT 0
#define HPRIO 0
#define PFN 0

constexpr int S = 2048, D = 640, F = 1536;
constexpr unsigned WS_AB = 0, WS_H = WS_AB + 2 * S * D * 2, WS_P = WS_H + S * F * 2, WS_WD = WS_P + 6 * S * D * 2, WS_RS = WS_WD + F * D * 2,
                   WS_CS = WS_RS + S * 8, WS_CD = WS_CS + 16 * 4 * 16 * 64 * 4, WS_CTRL = WS_CD + 8192, WS_END = WS_CTRL + 5120 * 128;
constexpr int C_E = 0, C_F0 = 1, C_FAB = C_F0 + 1024, C_FH = C_FAB + 512, C_FP = C_FH + 1024;
#define FLAG(i) ((u64*)(P.ws + WS_CTRL) + (i) * 16)

DI unsigned pk_bf16(float lo, float hi) { f32x2 v = {lo, hi}; return __builtin_bit_cast(unsigned, __builtin_convertvector(v, bf16x2_t)); }
DI float rbf(float f) { return __uint_as_float(pk_bf16(f, f) << 16); }
DI float bflo(unsigned u) { return __uint_as_float(u << 16); }
DI float bfhi(unsigned u) { return __uint_as_float(u & 0xffff0000u); }
DI float dot2(unsigned a, unsigned b, float c) { return __builtin_amdgcn_fdot2_f32_bf16(__builtin_bit_cast(bf16x2_t, a), __builtin_bit_cast(bf16x2_t, b), c, false); }
#define GLDS4(POL) asm volatile("s_mov_b32 m0, %0\n\ts_nop 0\n\tglobal_load_lds_dwordx4 %2, %1" POL "\n\tglobal_load_lds_dwordx4 %3, %1 offset:1024" POL \
    "\n\tglobal_load_lds_dwordx4 %4, %1 offset:2048" POL "\n\tglobal_load_lds_dwordx4 %5, %1 offset:3072" POL \
    :: "s"(lds), "s"(base), "v"(v0), "v"(v1), "v"(v2), "v"(v3) : "memory")
#define GLDS5(POL) asm volatile("s_mov_b32 m0, %0\n\ts_nop 0\n\tglobal_load_lds_dwordx4 %2, %1" POL "\n\tglobal_load_lds_dwordx4 %3, %1 offset:1024" POL \
    "\n\tglobal_load_lds_dwordx4 %4, %1 offset:2048" POL "\n\tglobal_load_lds_dwordx4 %5, %1 offset:3072" POL \
    "\n\ts_add_u32 m0, %0, 0x1000\n\ts_nop 0\n\tglobal_load_lds_dwordx4 %6, %1" POL \
    :: "s"(lds), "s"(base), "v"(v0), "v"(v1), "v"(v2), "v"(v3), "v"(v4) : "memory", "scc")
template <bool DEV> DI void glds4(unsigned lds, const char* base, unsigned v0, unsigned v1, unsigned v2, unsigned v3) {
  if (DEV) GLDS4(" sc1"); else GLDS4("");
}
template <bool DEV> DI void glds5(unsigned lds, const char* base, unsigned v0, unsigned v1, unsigned v2, unsigned v3, unsigned v4) {
  if (DEV) GLDS5(" sc1"); else GLDS5("");
}
template <int N> DI void wait_v() { asm volatile("s_waitcnt vmcnt(%0)" :: "i"(N) : "memory"); }
#define WAIT_L0() do { __builtin_amdgcn_s_waitcnt(0xC07F); asm volatile("" ::: "memory"); } while (0)
#define BAR() do { asm volatile("" ::: "memory"); __builtin_amdgcn_s_barrier(); asm volatile("" ::: "memory"); } while (0)
#define SGB(m, n) __builtin_amdgcn_sched_group_barrier(m, n, 0)
#define PIN() __builtin_amdgcn_sched_barrier(0)
DI void mf(f32x4& c, const bf16x8& a, const bf16x8& b, bool first) {
  if (first) asm volatile("v_mfma_f32_16x16x32_bf16 %0, %1, %2, 0" : "=&v"(c) : "v"(a), "v"(b));
  else asm volatile("v_mfma_f32_16x16x32_bf16 %0, %1, %2, %0" : "+v"(c) : "v"(a), "v"(b));
}
#define MFMA_DONE() asm volatile("s_nop 15")
template <int A, int B> DI void after_mfma(f32x4 (&c)[A][B]) {
#pragma unroll
  for (int i = 0; i < A; ++i)
#pragma unroll
    for (int j = 0; j < B; ++j) asm volatile("" : "+v"(c[i][j]));
}
#define STAMP_AT(i)

template <int CTRL> DI float dpp_add(float v) { return v + __builtin_bit_cast(float, __builtin_amdgcn_update_dpp(0, __builtin_bit_cast(int, v), CTRL, 0xf, 0xf, true)); }
DI float sum8(float v) { v = dpp_add<0xB1>(v); v = dpp_add<0x4E>(v); return dpp_add<0x141>(v); }
DI float sum16(float v) { return dpp_add<0x140>(sum8(v)); }
DI float xchg16(float keep_lo, float keep_hi) {
  const auto r = __builtin_amdgcn_permlane16_swap(__float_as_uint(keep_lo), __float_as_uint(keep_hi), false, false);
  return __uint_as_float(r[0]) + __uint_as_float(r[1]);
}
DI float xchg32(float keep_lo, float keep_hi) {
  const auto r = __builtin_amdgcn_permlane32_swap(__float_as_uint(keep_lo), __float_as_uint(keep_hi), false, false);
  return __uint_as_float(r[0]) + __uint_as_float(r[1]);
}
#define TIC(v)
#define ACC(a, x, y)
#define PUT(slot, a)
DI u64 ld_flag(const u64* p) { return __hip_atomic_load((const GAS u64*)p, __ATOMIC_RELAXED, __HIP_MEMORY_SCOPE_AGENT); }
DI void st_flag(u64* p, u64 v) { __hip_atomic_store((GAS u64*)p, v, __ATOMIC_RELAXED, __HIP_MEMORY_SCOPE_AGENT); }
DI void wait_flags(const u64* p, u64 tag) {
#pragma unroll 1
  for (int it = 0; it < 60000; ++it) {
    if (__all(ld_flag(p) == tag)) break;
    __builtin_amdgcn_s_sleep(4);
  }
  asm volatile("" ::: "memory");
}
DI u64 read_tag(const char* ws) {
  u64 e;
  asm volatile("s_load_dwordx2 %0, %1, 0x0\n\ts_waitcnt lgkmcnt(0)" : "=s"(e) : "s"(ws + WS_CTRL) : "memory");
  return e * 6364136223846793005ull + 1442695040888963407ull;
}
DI void st16c(__amdgpu_buffer_rsrc_t r, unsigned off, u32x4 v) { __builtin_amdgcn_raw_buffer_store_b128(v, r, off, 0, 16); }
DI u32x4 ld16c(__amdgpu_buffer_rsrc_t r, unsigned off) { return __builtin_amdgcn_raw_buffer_load_b128(r, off, 0, 16); }

struct Params {
  const bf16_t *x, *wsr, *wsi, *wdr, *wdi, *wg, *wu, *wdn;
  bf16_t* out; char* ws;
};

constexpr int L_W = 49152, L_D = 98304, K1_TAB = 159744, LDS_BYTES = K1_TAB + 4096;
constexpr int T_RS = K1_TAB, T_RM = K1_TAB + 512, T_CS = K1_TAB + 1024, T_GO = K1_TAB + 3072;
constexpr int A_ST = 16384, B_ST = 20480, L_B = 4 * A_ST;

constexpr int NY = 16, NK2 = 22;
constexpr int F_Y = 32768, F_T = F_Y + 20480, FPITCH = 176;

template <bool TG>
DI void helper_p1(const Params& P, LAS char* smem, int lane, int lw, int mt, int ns, int split, int mt2, int nt, u64& tag) {
  constexpr int NL = TG ? 12 : 13;
  const int n8 = ns & 7, hcol0 = TG ? 128 * n8 : 1024 + 64 * n8;
  const int rr = lane >> 3, c8 = lane & 7;
  const unsigned lds0 = (unsigned)(size_t)smem;
  unsigned vX[4], vW[4], vD[5];
#pragma unroll
  for (int j = 0; j < 4; ++j) {
    vX[j] = (32 * lw + 8 * j + rr) * 1280 + ((c8 ^ (TG ? rr : ((rr & 2) | ((j & 2) << 1)))) * 16) - j * 1024;
    vW[j] = (8 * j + rr) * 3072 + ((c8 ^ ((rr & 2) | ((j & 1) << 2))) * 16) - j * 1024;
  }
#pragma unroll
  for (int j = 0; j < 5; ++j) vD[j] = (40 * lw + 8 * j + rr) * 1280 + ((c8 ^ rr) * 16) - (j & 3) * 1024;
  const char* bx = (const char*)P.x + (size_t)mt * 128 * 1280;
  const char* bw = (const char*)((lw >> 1) ? P.wu : P.wg) + hcol0 * 2 + (TG ? (lw & 1) * 128 : (lw & 1) * (32 * 3072));
  const char* bd = (const char*)(n8 >= 4 ? P.wdi : P.wdr) + (size_t)(n8 & 3) * 160 * 1280;
  const unsigned ldsX = __builtin_amdgcn_readfirstlane(lds0 + lw * 4096);
  const unsigned ldsW = __builtin_amdgcn_readfirstlane(lds0 + L_W + (TG ? (lw & 1) * 49152 + (lw >> 1) * 8192 : lw * 4096));
  const unsigned ldsD = __builtin_amdgcn_readfirstlane(lds0 + L_D + lw * 5120);
  auto issue = [&](int kt, int buf) {
    glds4<false>(ldsX + buf * 16384, bx + kt * 128, vX[0], vX[1], vX[2], vX[3]);
    const char* pw = bw + kt * (64 * 3072);
    glds4<false>(ldsW + buf * 16384, pw, vW[0], vW[1], vW[2], vW[3]);
    if (TG) glds4<false>(ldsW + buf * 16384 + 4096, pw + 32 * 3072, vW[0], vW[1], vW[2], vW[3]);
    else glds5<false>(ldsD + buf * 20480, bd + kt * 128, vD[0], vD[1], vD[2], vD[3], vD[4]);
  };
  issue(0, 0); issue(1, 1);
  tag = read_tag(P.ws);
  int bn = 2;
  u64 a0 = 0, a1 = 0, a2 = 0;
#pragma unroll 1
  for (int k = 0; k < 8; ++k) {
    TIC(t0);
    wait_v<NL>();
    TIC(t1);
    BAR();
    TIC(t2);
    issue(k + 2, bn);
    TIC(t3);
    if (k >= 1) { ACC(a0, t0, t1); ACC(a1, t1, t2); ACC(a2, t2, t3); }
    bn = bn == 2 ? 0 : bn + 1;
  }
  PUT(0, a0); PUT(1, a1); PUT(2, a2);
  wait_v<NL>();
  BAR();
  {
    const int l15 = lane & 15;
    const int fi = (lane & 32) ? nt * 240 + split * 60 + lw * 15 + min(l15, 14)
                 : (lane & 16) ? ((mt * 16 + 4 * lw + (l15 >> 2)) * 4 + (l15 & 3)) : ((l15 * 16 + ns) * 4 + lw);
    wait_flags(FLAG(C_F0 + fi), tag);
  }
  BAR();
  const __amdgpu_buffer_rsrc_t rw = __builtin_amdgcn_make_buffer_rsrc(P.ws, 0, WS_END, 0x00020000);
  const u32x2 rs2 = __builtin_amdgcn_raw_buffer_load_b64(rw, WS_RS + (mt * 128 + lw * 32 + (lane & 31)) * 8, 0, 16);
  if (TG || lw < 2) {
    f32x4 s = {0, 0, 0, 0};
#pragma unroll
    for (int i = 0; i < 4; ++i) s += __builtin_bit_cast(f32x4, ld16c(rw, WS_CS + ((ns * 4 + lw) * 16) * 256 + i * 1024 + lane * 16));
#pragma unroll
    for (int e = 0; e < 4; ++e) { s[e] += __shfl_xor(s[e], 16); s[e] += __shfl_xor(s[e], 32); }
    if (lane < 16) *(LAS f32x4*)(smem + T_CS + lw * 256 + lane * 16) = s;
  } else {
#pragma unroll
    for (int i = 0; i < 2; ++i) {
      const int n = lane + 64 * i;
      if (n < 80) {
        const int e = (n / 5) * 10 + (lw - 2) * 5 + n % 5;
        *(LAS unsigned*)(smem + T_CS + 1024 + e * 4) = __builtin_amdgcn_raw_buffer_load_b32(rw, WS_CD + (n8 * 160 + e) * 4, 0, 16);
      }
    }
  }
  if (lane < 32) { *(LAS unsigned*)(smem + T_RS + (lw * 32 + lane) * 4) = rs2[0]; *(LAS unsigned*)(smem + T_RM + (lw * 32 + lane) * 4) = rs2[1]; }
  if (PFN) {
    const char* pa = (const char*)(split < 2 ? P.wsr : P.wsi) + (size_t)(mt2 * 128 + nt * 32) * 4096 + (split & 1) * 2048;
#pragma unroll
    for (int j = 0; j < PFN; ++j) {
      int kt = 2 * (lw + 4 * j) + (lane >> 5);
      if (ROT2 && PFN == 1) kt = (kt & 3) + (kt >> 2) * 8;
      asm volatile("s_mov_b32 m0, %0\n\ts_nop 0\n\tglobal_load_lds_dword %1, %2" :: "s"(lds0 + T_GO + 16 + lw * 0), "v"((lane & 31) * 4096 + kt * 128), "s"(pa) : "memory");
    }
  }
  if (lw == 0 && lane == 0) *(volatile LAS unsigned*)(smem + T_GO) = 0;
  WAIT_L0();
  BAR();
}

DI void helper_p2(const Params& P, LAS char* smem, int lane, int lw, int split, int mt, int nt, u64 tag) {
  const unsigned lds0 = (unsigned)(size_t)smem;
  const int rr = lane >> 3;
  unsigned vY[4], vH[4];
#pragma unroll
  for (int j = 0; j < 4; ++j) {
    const int t = (2 * lw + (j >> 1)) ^ split;
    vY[j] = (16 * t + 8 * (j & 1) + rr) * 4096 + (((lane & 7) ^ rr) * 16) + (3 - j) * 1024;
    vH[j] = (2 * t + (j & 1)) * 1024 + lane * 16 + (3 - j) * 1024;
  }
  const unsigned vB = lw * 5120 + lane * 16, vB4 = vB + 4096;
  const char* baseY = (const char*)(split < 2 ? P.wsr : P.wsi) + (size_t)mt * 128 * 4096 + (split & 1) * 2048 - 3072;
  const char* baseH = P.ws + WS_H + (mt * 24 + split * 6) * A_ST - 3072;
  const char* baseAB = P.ws + WS_AB + (nt * 64 + split * 16) * B_ST;
  const char* baseWD = P.ws + WS_WD + (nt * 24 + split * 6) * B_ST;
  const int rot = ROT2 ? (nt & 1) * 8 : 0;
  const unsigned ldsA = __builtin_amdgcn_readfirstlane(lds0 + lw * 4096), ldsB = __builtin_amdgcn_readfirstlane(lds0 + L_B + lw * 5120);
  auto issueYA = [&](int k) { glds4<false>(ldsA + (k & 3) * A_ST, baseY + ((k + rot) & 15) * 128, vY[0], vY[1], vY[2], vY[3]); };
  auto issueYB = [&](int k) { glds5<true>(ldsB + (k & 3) * B_ST, baseAB + ((k + rot) & 15) * B_ST, vB, vB, vB, vB, vB4); };
  auto issueD = [&](int k) {
    glds4<true>(ldsA + (k & 3) * A_ST, baseH + (k - NY) * A_ST, vH[0], vH[1], vH[2], vH[3]);
    glds5<true>(ldsB + (k & 3) * B_ST, baseWD + (k - NY) * B_ST, vB, vB, vB, vB, vB4);
  };
  STAMP_AT(8);
  issueYA(0); issueYA(1);
  if (lw == 0) {
    wait_flags(FLAG(C_FAB + (nt * 64 + split * 16) * 2 + (lane & 31)), tag);
    if (lane == 0) *(volatile LAS unsigned*)(smem + T_GO) = 1;
  } else {
#pragma unroll 1
    for (int it = 0; it < 4000000; ++it) {
      if (*(volatile LAS unsigned*)(smem + T_GO) != 0) break;
      __builtin_amdgcn_s_sleep(1);
    }
    asm volatile("" ::: "memory");
  }
  STAMP_AT(9);
  const int h0 = split < 3 ? 3 * split : 10, hn = split < 2 ? 12 : (split == 2 ? 16 : 24);
  const u64* fh = FLAG(C_FH + mt * 64 + h0 * 4 + min(lane, hn - 1));
  u64 fhv = 0;
  u64 a0 = 0, a1 = 0, a2 = 0;
  issueYB(0); issueYB(1);
  wait_v<5>(); BAR();
  issueYA(2); issueYB(2);
  wait_v<9>(); BAR();
  issueYA(3); issueYB(3); issueYA(4); issueYB(4);
#pragma unroll 1
  for (int k = 2; k < 13; ++k) {
    TIC(t0); wait_v<18>(); TIC(t1); BAR(); TIC(t2);
    if (k == 9) asm volatile("global_load_dwordx2 %0, %1, off sc1" : "+v"(fhv) : "v"(fh) : "memory");
    issueYA(k + 3); issueYB(k + 3);
    TIC(t3);
    ACC(a0, t0, t1); ACC(a1, t1, t2); ACC(a2, t2, t3);
  }
  PUT(3, a0); PUT(4, a1); PUT(5, a2);
  asm volatile("" : "+v"(fhv));
  if (!__all(fhv == tag)) wait_flags(fh, tag);
#pragma unroll 1
  for (int k = 13; k < 19; ++k) {
    wait_v<18>(); BAR();
    issueD(k + 3);
  }
  wait_v<18>(); BAR();
  wait_v<9>(); BAR();
  wait_v<0>(); BAR();
  {
    const __amdgpu_buffer_rsrc_t rw = __builtin_amdgcn_make_buffer_rsrc(P.ws, 0, WS_END, 0x00020000);
    const int tile = mt * 4 + nt;
    STAMP_AT(10);
    wait_flags(FLAG(C_FP + (tile * 4 + (split ^ (1 + min(lane, 2)))) * 4 + lw), tag);
    u32x4 pa[3][2]; u32x2 pb[3];
#pragma unroll
    for (int s = 0; s < 3; ++s) {
      const unsigned src = WS_P + (((tile * 4 + split) * 3 + s) * 4 + lw) * 2560;
      pa[s][0] = ld16c(rw, src + lane * 16); pa[s][1] = ld16c(rw, src + 1024 + lane * 16);
      pb[s] = __builtin_amdgcn_raw_buffer_load_b64(rw, src + 2048 + lane * 8, 0, 16);
    }
#pragma unroll
    for (int j = 0; j < 5; ++j) {
      f32x4 v = {0, 0, 0, 0};
#pragma unroll
      for (int s = 0; s < 3; ++s) {
        const unsigned lo = j < 4 ? pa[s][j >> 1][(j & 1) * 2] : pb[s][0], hi = j < 4 ? pa[s][j >> 1][(j & 1) * 2 + 1] : pb[s][1];
        v += (f32x4){bflo(lo), bfhi(lo), bflo(hi), bfhi(hi)};
      }
      *(LAS f32x4*)(smem + F_Y + (lw * 5 + j) * 1024 + lane * 16) = v;
    }
    WAIT_L0();
    STAMP_AT(11);
    BAR();
  }
  if (blockIdx.x == 0 && lw == 0) {
#pragma unroll 1
    for (int i = 0; i < 4; ++i) wait_flags(FLAG(C_F0 + (i * 64 + lane) * 4), tag);
    if (lane == 0) st_flag(FLAG(C_E), tag);
  }
}

template <bool TG>
DI void mfma_p1(const Params& P, LAS char* smem, int lane, int w, int mt, int ns, u64& tag) {
  const int n8 = ns & 7, hcol0 = TG ? 128 * n8 : 1024 + 64 * n8;
  const __amdgpu_buffer_rsrc_t rw = __builtin_amdgcn_make_buffer_rsrc(P.ws, 0, WS_END, 0x00020000);
  const int row = mt * 128 + ns * 8 + 2 * w + (lane >> 5);
  const int rr = lane >> 3, c8 = lane & 7;
  const bool colw = TG || w < 2;
  u32x2 vx[5]; u32x4 vw[10]; bf16x8 vd[2];
  {
    const char* px = (const char*)P.x + (size_t)row * 1280 + (lane & 31) * 8;
#pragma unroll
    for (int i = 0; i < 5; ++i) vx[i] = *(const u32x2*)(px + i * 256);
    if (colw) {
      const bf16_t* wsel = (TG ? (w >> 1) : w) ? P.wu : P.wg;
      const char* pw = (const char*)wsel + (size_t)(40 * mt + rr) * 3072 + (hcol0 + (TG ? (w & 1) * 64 : 0)) * 2 + c8 * 16;
#pragma unroll
      for (int i = 0; i < 5; ++i) vw[i] = *(const u32x4*)(pw + i * (8 * 3072));
    } else {
      const int e = (n8 & 3) * 160 + 10 * mt + 5 * (w - 2) + min(rr, 4);
      const char* pd = (const char*)(n8 >= 4 ? P.wdi : P.wdr) + (size_t)e * 1280 + c8 * 16;
#pragma unroll
      for (int i = 0; i < 10; ++i) vw[i] = *(const u32x4*)(pd + i * 128);
    }
  }
  const int task0 = (mt * 16 + ns) * 4 + w, task = min(task0, 959), tnt = task / 240, tfb = (task % 240) / 5, tnb = task % 5;
  {
    const bf16_t* src = P.wdn + (size_t)(tfb * 32 + 4 * (lane >> 4)) * D + tnt * 160 + tnb * 32 + (lane & 15);
#pragma unroll
    for (int b = 0; b < 2; ++b)
#pragma unroll
      for (int e = 0; e < 8; ++e) vd[b][e] = (short)src[((e & 3) + (e >> 2) * 16) * D + b * 16];
  }
  tag = read_tag(P.ws);
  const int wm = w & 1, wn = w >> 1, q = lane >> 4, rho = lane & 15;
  constexpr int NC = TG ? 4 : 2;
  unsigned vX0 = TG ? wm * 8192 + rho * 128 + ((q ^ (rho & 7)) * 16)
                          : wm * 8192 + (8 * (rho >> 2) + (rho & 3)) * 128 + ((q ^ ((rho & 2) | ((rho >> 1) & 4))) * 16);
  unsigned vX1 = vX0 ^ 64;
  unsigned vW[NC];
  {
    const int qq = rho >> 2, p = lane & 3, swz = (qq & 2) | ((q & 1) << 2);
#pragma unroll
    for (int c = 0; c < NC; ++c) {
      const int cc = TG ? c : (2 * wn + c);
      vW[c] = L_W + (TG ? wn * 49152 : 0) + (8 * q + qq) * 128 + ((((2 * cc) ^ swz) + (p >> 1)) * 16) + 8 * (p & 1);
    }
  }
  unsigned vD0 = L_D + (80 * wn + rho) * 128 + ((q ^ (rho & 7)) * 16), vD1 = vD0 ^ 64;
  asm volatile("" : "+v"(vX0), "+v"(vX1));
  if (!TG) asm volatile("" : "+v"(vD0), "+v"(vD1));
#pragma unroll
  for (int c = 0; c < NC; ++c) asm volatile("" : "+v"(vW[c]));
  f32x4 aG[NC][4], aU[NC][4], aA[4][5];
  {
    unsigned ONE2 = 0x3f803f80u, SLO = 0x00003f80u, SHI = 0x3f800000u;
    asm volatile("" : "+v"(ONE2), "+v"(SLO), "+v"(SHI));
    float sx = 0, sq = 0;
#pragma unroll
    for (int i = 0; i < 5; ++i)
#pragma unroll
      for (int e = 0; e < 2; ++e) { sx = dot2(vx[i][e], ONE2, sx); sq = dot2(vx[i][e], vx[i][e], sq); }
    sx = sum16(sx); sq = sum16(sq);
    sx = xchg16(sx, sx); sq = xchg16(sq, sq);
    const float mean = sx * (1.0f / D);
    const float var = fmaxf(sq * (1.0f / D) - mean * mean, 0.f);
    const float sd = rbf(__builtin_amdgcn_sqrtf(rbf(rbf(var) + 1e-5f)));
    const u32x2 rso = {__float_as_uint(__builtin_amdgcn_rcpf(sd)), __float_as_uint(rbf(mean))};
    float cres; unsigned coff;
    if (colw) {
      float c[8] = {0, 0, 0, 0, 0, 0, 0, 0};
#pragma unroll
      for (int i = 0; i < 5; ++i)
#pragma unroll
        for (int e = 0; e < 4; ++e) { c[2 * e] = dot2(vw[i][e], SLO, c[2 * e]); c[2 * e + 1] = dot2(vw[i][e], SHI, c[2 * e + 1]); }
      float d4[4], d2[2];
#pragma unroll
      for (int e = 0; e < 4; ++e) d4[e] = xchg32(c[e], c[4 + e]);
#pragma unroll
      for (int e = 0; e < 2; ++e) d2[e] = xchg16(d4[e], d4[2 + e]);
      d2[0] = dpp_add<0x128>(d2[0]); d2[1] = dpp_add<0x128>(d2[1]);
      cres = (lane & 8) ? d2[1] : d2[0];
      coff = WS_CS + (((ns * 4 + w) * 16 + mt) * 64 + 8 * c8 + rr) * 4;
    } else {
      float c0 = 0, c1 = 0;
#pragma unroll
      for (int i = 0; i < 10; ++i) { c0 = dot2(vw[i][0], ONE2, c0); c1 = dot2(vw[i][1], ONE2, c1); c0 = dot2(vw[i][2], ONE2, c0); c1 = dot2(vw[i][3], ONE2, c1); }
      cres = sum8(c0 + c1);
      coff = (c8 == 0 && rr < 5) ? WS_CD + (n8 * 160 + 10 * mt + 5 * (w - 2) + rr) * 4 : 0xfffffff0u;
    }
    asm volatile("" : "+v"(vd[0]), "+v"(vd[1]), "+v"(cres));
    __builtin_amdgcn_raw_buffer_store_b32(__float_as_uint(cres), rw, coff, 0, 16);
    __builtin_amdgcn_raw_buffer_store_b64(rso, rw, (lane & 31) == 0 ? WS_RS + row * 8 : 0xfffffff0u, 0, 16);
    const unsigned dst = task0 < 960 ? WS_WD + ((tnt * 48 + tfb) * 10 + tnb * 2) * 1024 + lane * 16 : 0xffff0000u;
    st16c(rw, dst, __builtin_bit_cast(u32x4, vd[0])); st16c(rw, dst + 1024, __builtin_bit_cast(u32x4, vd[1]));
  }
  bf16x8 FX[2][4], FD[2][5];
  s16x4 FW[2][2 * NC][2];
  constexpr int NR = TG ? 20 : 17, NM = TG ? 32 : 36;
  auto rd1 = [&](int s, int buf, int ks, int r) {
    if (r >= 2 && r < 6) {
      const int i = r - 2;
      FX[s][i] = *(LAS bf16x8*)(smem + (ks ? vX1 : vX0) + buf * 16384 + (TG ? i * 2048 : (i >> 1) * 4096 + (i & 1) * 512));
    } else if (r < 4 + 4 * NC) {
      const int h = r < 2 ? r : r - 4, f = h >> 1;
      FW[s][f][h & 1] = __builtin_amdgcn_ds_read_tr16_b64_v4i16((LAS s16x4*)(smem + vW[f >> 1] + buf * 16384 + ks * 4096 + (f & 1) * 8192 + (h & 1) * 512));
    } else {
      const int jj = r - (4 + 4 * NC);
      FD[s][jj] = *(LAS bf16x8*)(smem + (ks ? vD1 : vD0) + buf * 20480 + jj * 2048);
    }
  };
  auto mm1 = [&](int s, int m, bool first) {
    const int f = m >> 2, i = m & 3;
    if (f < 2 * NC) {
      const bf16x8 wf = __builtin_shufflevector(FW[s][f][0], FW[s][f][1], 0, 1, 2, 3, 4, 5, 6, 7);
      mf((f & 1) ? aU[f >> 1][i] : aG[f >> 1][i], wf, FX[s][i], first);
    } else mf(aA[i][f - 2 * NC], FX[s][i], FD[s][f - 2 * NC], first);
  };
  auto seg = [&](int s, bool first, bool reads, int buf, int ks) {
    int r = 0;
#pragma unroll
    for (int m = 0; m < NM; ++m) {
      mm1(s, m, first);
      if (reads && r < NR && (TG ? (m % 5 != 4) : (m % 3 != 2))) { rd1(s ^ 1, buf, ks, r); ++r; }
      PIN();
    }
  };
  BAR();
  STAMP_AT(1);
#pragma unroll
  for (int r = 0; r < NR; ++r) rd1(0, 0, 0, r);
  PIN();
#pragma unroll
  for (int k = 0; k < 10; ++k) {
    seg(0, k == 0, true, k % 3, 1);
    if (k == 1) {
      wait_v<0>();
      if (lane == 0) st_flag(FLAG(C_F0 + (mt * 16 + ns) * 4 + w), tag);
    }
    if (k < 9) {
      WAIT_L0();
      BAR();
      PIN();
    }
    seg(1, false, k < 9, (k + 1) % 3, 0);
    WAIT_L0(); PIN();
  }
  MFMA_DONE();
  after_mfma(aG); after_mfma(aU);
  if (!TG) after_mfma(aA);
  BAR();
  STAMP_AT(2);
  if (!TG) {
    const float sgn = (n8 >= 4) ? -1.0f : 1.0f;
    float cs[5];
#pragma unroll
    for (int j = 0; j < 5; ++j) cs[j] = *(LAS float*)(smem + T_CS + 1024 + (80 * wn + 16 * j + rho) * 4);
    const int kt = (n8 >= 4 ? 32 : 0) + mt * 2 + wm;
#pragma unroll
    for (int I = 0; I < 2; ++I) {
      f32x4 r4[2], m4[2];
#pragma unroll
      for (int t = 0; t < 2; ++t) {
        const int row = 64 * wm + 32 * I + 8 * q + 4 * t;
        r4[t] = *(LAS f32x4*)(smem + T_RS + row * 4) * sgn; m4[t] = *(LAS f32x4*)(smem + T_RM + row * 4);
      }
      const unsigned dst = WS_AB + ((((n8 & 3) * 64 + kt) * 2 + I) * 10 + 5 * wn) * 1024 + lane * 16;
#pragma unroll
      for (int j = 0; j < 5; ++j) {
        const f32x4 v0 = (aA[2 * I][j] - m4[0] * cs[j]) * r4[0], v1 = (aA[2 * I + 1][j] - m4[1] * cs[j]) * r4[1];
        const u32x4 o = {pk_bf16(v0[0], v0[1]), pk_bf16(v0[2], v0[3]), pk_bf16(v1[0], v1[1]), pk_bf16(v1[2], v1[3])};
        st16c(rw, dst + j * 1024, o);
      }
    }
  }
  {
    const float K0 = -2.0f * 0.7978845608028654f * 1.4426950408889634f, K1 = K0 * 0.044715f;
    float mu[4], c0[4], c1[4], v2[4];
#pragma unroll
    for (int i = 0; i < 4; ++i) {
      const int row = TG ? 64 * wm + 16 * i + rho : 64 * wm + 32 * (i >> 1) + 8 * (rho >> 2) + 4 * (i & 1) + (rho & 3);
      const float rs = *(LAS float*)(smem + T_RS + row * 4);
      mu[i] = *(LAS float*)(smem + T_RM + row * 4);
      c0[i] = K0 * rs; c1[i] = K1 * rs * rs * rs; v2[i] = __builtin_amdgcn_rcpf(rs * rs);
    }
    u32x4 ho[NC / 2][4];
#pragma unroll
    for (int J = 0; J < NC / 2; ++J) {
      f32x4 cg[2], cu[2];
#pragma unroll
      for (int t = 0; t < 2; ++t) {
        const int c = 2 * J + t;
        const int o = TG ? (wn * 256 + c * 64 + q * 16) : ((2 * wn + c) * 64 + q * 16);
        cg[t] = *(LAS f32x4*)(smem + T_CS + o);
        cu[t] = *(LAS f32x4*)(smem + T_CS + (TG ? 512 : 256) + o);
      }
      const int f0 = hcol0 + (TG ? wn * 64 + J * 32 : wn * 32);
#pragma unroll
      for (int i = 0; i < 4; ++i) {
        u32x4 o;
#pragma unroll
        for (int t = 0; t < 2; ++t) {
          const f32x4 g = aG[2 * J + t][i] - cg[t] * mu[i];
          const f32x4 u = aU[2 * J + t][i] - cu[t] * mu[i];
          const f32x4 z = g * (g * g * c1[i] + c0[i]);
          f32x4 e;
#pragma unroll
          for (int r = 0; r < 4; ++r) e[r] = __builtin_amdgcn_exp2f(z[r]);
          e = e * v2[i] + v2[i];
#pragma unroll
          for (int r = 0; r < 4; ++r) e[r] = __builtin_amdgcn_rcpf(e[r]);
          const f32x4 hv = g * u * e;
          o[2 * t] = pk_bf16(hv[0], hv[1]); o[2 * t + 1] = pk_bf16(hv[2], hv[3]);
        }
        if (TG) st16c(rw, WS_H + (((mt * 24 + (f0 >> 6)) * 8 + 4 * wm + i) * 2 + ((f0 >> 5) & 1)) * 1024 + lane * 16, o); else ho[J][i] = o;
      }
      if (!TG) {
#pragma unroll
        for (int i = 0; i < 4; ++i) asm volatile("" : "+v"(ho[J][i]));
        wait_v<0>();
        if (lane == 0) st_flag(FLAG(C_FAB + ((n8 & 3) * 64 + (n8 >= 4 ? 32 : 0) + mt * 2 + wm) * 2 + wn), tag);
#pragma unroll
        for (int i = 0; i < 4; ++i) {
          const int rb = 4 * wm + 2 * (i >> 1) + (rho >> 3), pos = 8 * ((rho >> 2) & 1) + 4 * (i & 1) + (rho & 3) + 16 * q;
          st16c(rw, WS_H + (((mt * 24 + (f0 >> 6)) * 8 + rb) * 2 + ((f0 >> 5) & 1)) * 1024 + pos * 16, ho[J][i]);
        }
      }
    }
  }
  STAMP_AT(3);
  wait_v<0>();
  if (lane == 0) st_flag(FLAG(C_FH + mt * 64 + ns * 4 + w), tag);
  STAMP_AT(4);
}

DI void mfma_p2(const Params& P, LAS char* smem, int lane, int w, int split, int mt, int nt, u64 tag) {
  const int wm = w & 1, wn = w >> 1, q = lane >> 4, r15 = lane & 15;
  unsigned vY0 = wm * 8192 + r15 * 128 + ((q ^ (r15 & 7)) * 16), vH0 = wm * 8192 + lane * 16;
  unsigned vY1 = vY0 ^ 64, vB0 = L_B + wn * 5120 + lane * 16, vB2 = vB0 + 2 * B_ST;
  asm volatile("" : "+v"(vY0), "+v"(vY1), "+v"(vH0), "+v"(vB0), "+v"(vB2));
  const __amdgpu_buffer_rsrc_t rw = __builtin_amdgcn_make_buffer_rsrc(P.ws, 0, WS_END, 0x00020000);
  const int tile = mt * 4 + nt;
  const int orow = mt * 128 + wm * 64 + split * 16, ocol = nt * 160 + wn * 80;
  u32x4 xv[3];
#pragma unroll
  for (int i = 0; i < 3; ++i) {
    const int n = min(i * 64 + lane, 159);
    xv[i] = *(const u32x4*)(P.x + (size_t)(orow + n / 10) * D + ocol + (n % 10) * 8);
  }
#pragma unroll
  for (int i = 0; i < 3; ++i) asm volatile("" : "+v"(xv[i]));
  f32x4 acc[4][5], yown[5];
  bf16x8 FA[2][4], FB[2][5];
  auto rd1 = [&](int s, int k, int ks, int r) {
    const int buf = k & 3;
    if (r >= 1 && r < 5) FA[s][r - 1] = *(LAS bf16x8*)(smem + (k < NY ? (ks ? vY1 : vY0) : vH0) + buf * A_ST + (k < NY ? 0 : ks * 1024) + (r - 1) * 2048);
    else { const int j = r ? r - 4 : 0; FB[s][j] = *(LAS bf16x8*)(smem + (buf >= 2 ? vB2 : vB0) + (buf & 1) * B_ST + ks * 10240 + j * 1024); }
  };
  auto seg = [&](int s, bool first, bool reads, int k, int ks) {
    int r = 0;
#pragma unroll
    for (int m = 0; m < 20; ++m) {
      mf(acc[m & 3][m >> 2], FB[s][m >> 2], FA[s][m & 3], first);
      if (reads && r < 9 && m % 3 != 2) { rd1(s ^ 1, k, ks, r); ++r; }
      PIN();
    }
  };
  auto publish = [&](int ph) {
#pragma unroll
    for (int i = 1; i < 4; ++i) {
      const unsigned dst = WS_P + ph * (3 * S * D * 2) + (((tile * 4 + (split ^ i)) * 3 + i - 1) * 4 + w) * 2560;
      unsigned pk[10];
#pragma unroll
      for (int j = 0; j < 5; ++j) { pk[2 * j] = pk_bf16(acc[i][j][0], acc[i][j][1]); pk[2 * j + 1] = pk_bf16(acc[i][j][2], acc[i][j][3]); }
      st16c(rw, dst + lane * 16, (u32x4){pk[0], pk[1], pk[2], pk[3]});
      st16c(rw, dst + 1024 + lane * 16, (u32x4){pk[4], pk[5], pk[6], pk[7]});
      __builtin_amdgcn_raw_buffer_store_b64((u32x2){pk[8], pk[9]}, rw, dst + 2048 + lane * 8, 0, 16);
    }
  };
  BAR();
  STAMP_AT(5);
#pragma unroll
  for (int r = 0; r < 9; ++r) rd1(0, 0, 0, r);
  PIN();
#pragma unroll
  for (int k = 0; k < NK2; ++k) {
    seg(0, k == 0 || k == NY, true, k, 1);
    if (k == NY + YFL) {
      wait_v<0>();
      if (lane == 0) st_flag(FLAG(C_FP + (tile * 4 + split) * 4 + w), tag);
    }
    if (k + 1 < NK2) {
      WAIT_L0();
      BAR();
      PIN();
    }
    seg(1, false, k + 1 < NK2, k + 1, 0);
    WAIT_L0(); PIN();
    if (k == NY - 1) {
      MFMA_DONE();
      after_mfma(acc);
      publish(0);
#pragma unroll
      for (int j = 0; j < 5; ++j) yown[j] = acc[0][j];
      __builtin_amdgcn_sched_barrier(0);
    }
  }
  MFMA_DONE();
  after_mfma(acc);
  STAMP_AT(6);
  publish(1);
  wait_v<0>();
  if (lane == 0) st_flag(FLAG(C_FP + 1024 + (tile * 4 + split) * 4 + w), tag);
  STAMP_AT(7);
  BAR();
#pragma unroll
  for (int j = 0; j < 5; ++j) yown[j] += *(LAS f32x4*)(smem + F_Y + (w * 5 + j) * 1024 + lane * 16);
  wait_flags(FLAG(C_FP + 1024 + (tile * 4 + (split ^ (1 + min(lane, 2)))) * 4 + w), tag);
  u32x4 pa[3][2]; u32x2 pb[3];
#pragma unroll
  for (int s = 0; s < 3; ++s) {
    const unsigned src = WS_P + 3 * S * D * 2 + (((tile * 4 + split) * 3 + s) * 4 + w) * 2560;
    pa[s][0] = ld16c(rw, src + lane * 16); pa[s][1] = ld16c(rw, src + 1024 + lane * 16);
    pb[s] = __builtin_amdgcn_raw_buffer_load_b64(rw, src + 2048 + lane * 8, 0, 16);
  }
  LAS char* pt = smem + F_T + w * (16 * FPITCH);
#pragma unroll
  for (int j = 0; j < 5; ++j) {
    f32x4 d = acc[0][j];
#pragma unroll
    for (int s = 0; s < 3; ++s) {
      const unsigned lo = j < 4 ? pa[s][j >> 1][(j & 1) * 2] : pb[s][0], hi = j < 4 ? pa[s][j >> 1][(j & 1) * 2 + 1] : pb[s][1];
      d += (f32x4){bflo(lo), bfhi(lo), bflo(hi), bfhi(hi)};
    }
    f32x4 e = d * -1.4426950408889634f;
#pragma unroll
    for (int r = 0; r < 4; ++r) e[r] = __builtin_amdgcn_exp2f(e[r]);
    e = e + 1.0f;
#pragma unroll
    for (int r = 0; r < 4; ++r) e[r] = __builtin_amdgcn_rcpf(e[r]);
    e = e * d * yown[j];
    *(LAS u32x2*)(pt + r15 * FPITCH + j * 32 + q * 8) = (u32x2){pk_bf16(e[0], e[1]), pk_bf16(e[2], e[3])};
  }
  WAIT_L0();
#pragma unroll
  for (int i = 0; i < 3; ++i) {
    const int n = i * 64 + lane, nc = min(n, 159);
    const u32x4 v = *(LAS u32x4*)(pt + (nc / 10) * FPITCH + (nc % 10) * 16);
    u32x4 o;
#pragma unroll
    for (int d = 0; d < 4; ++d) o[d] = pk_bf16(bflo(v[d]) + bflo(xv[i][d]), bfhi(v[d]) + bfhi(xv[i][d]));
    if (n < 160) *(u32x4*)(P.out + (size_t)(orow + nc / 10) * D + ocol + (nc % 10) * 8) = o;
  }
}

__global__ __launch_bounds__(512, 2)
void fused(Params P) {
  extern __shared__ __attribute__((aligned(16))) char smem_[];
  LAS char* smem = (LAS char*)smem_;
  const int tid = threadIdx.x, lane = tid & 63, w = __builtin_amdgcn_readfirstlane(tid >> 6);
  const int bid = blockIdx.x, xcd = bid & 7, idx = bid >> 3;
  u64 tag;
  STAMP_AT(0);
  const int mt1 = (xcd & 1) * 8 + (idx & 7), j = idx >> 3, ns = (j & 1) * 8 + (xcd >> 1) * 2 + (j >> 1);
  const int split = xcd >> 1, mt2 = (xcd & 1) * 8 + (idx >> 2), nt = idx & 3;
  if (w >= 4) {
    __builtin_amdgcn_s_setprio(HPRIO);
    if (j & 1) helper_p1<false>(P, smem, lane, w & 3, mt1, ns, split, mt2, nt, tag); else helper_p1<true>(P, smem, lane, w & 3, mt1, ns, split, mt2, nt, tag);
    helper_p2(P, smem, lane, w & 3, split, mt2, nt, tag);
  } else {
    if (j & 1) mfma_p1<false>(P, smem, lane, w, mt1, ns, tag); else mfma_p1<true>(P, smem, lane, w, mt1, ns, tag);
    mfma_p2(P, smem, lane, w, split, mt2, nt, tag);
  }
}

extern "C" void kernel_launch(void* const* d_in, const int* in_sizes, int n_in, void* d_out, int out_size, void* d_ws, size_t ws_size, hipStream_t stream) {
  Params P;
  P.x = (const bf16_t*)d_in[0]; P.wsr = (const bf16_t*)d_in[1]; P.wsi = (const bf16_t*)d_in[2];
  P.wdr = (const bf16_t*)d_in[3]; P.wdi = (const bf16_t*)d_in[4];
  P.wg = (const bf16_t*)d_in[6]; P.wu = (const bf16_t*)d_in[7]; P.wdn = (const bf16_t*)d_in[8];
  P.out = (bf16_t*)d_out; P.ws = (char*)d_ws;
  (void)hipFuncSetAttribute((const void*)fused, hipFuncAttributeMaxDynamicSharedMemorySize, LDS_BYTES);
  fused<<<256, 512, LDS_BYTES, stream>>>(P);
}
```

```cpp
#include <hip/hip_runtime.h>
#include <hip/hip_bf16.h>
typedef unsigned short bf16_t;
typedef unsigned long long u64;
typedef short bf16x8 __attribute__((ext_vector_type(8)));
typedef short s16x4 __attribute__((ext_vector_type(4)));
typedef float f32x4 __attribute__((ext_vector_type(4)));
typedef float f32x2 __attribute__((ext_vector_type(2)));
typedef unsigned u32x4 __attribute__((ext_vector_type(4)));
typedef unsigned u32x2 __attribute__((ext_vector_type(2)));
typedef __bf16 bf16x2_t __attribute__((ext_vector_type(2)));
#define LAS __attribute__((address_space(3)))
#define GAS __attribute__((address_space(1)))
#define DI __device__ __forceinline__

#define STAMP 0
#define NPRE 2
#define ROT2 0
#define YFL 4
#define FABSL 20
#define AFL 2
#define YPL 8
#define XROT 0
#define HPRIO 0
#define PFN 4

constexpr int S = 2048, D = 640, F = 1536;
constexpr unsigned WS_AB = 0, WS_H = WS_AB + 2 * S * D * 2, WS_P = WS_H + S * F * 2, WS_WD = WS_P + 3 * S * D * 2, WS_RS = WS_WD + F * D * 2,
                   WS_CS = WS_RS + S * 8, WS_CD = WS_CS + 16 * 4 * 16 * 64 * 4, WS_CTRL = WS_CD + 8192, WS_END = WS_CTRL + 5120 * 128;
constexpr int C_E = 0, C_F0 = 1, C_FAB = C_F0 + 1024, C_FH = C_FAB + 512, C_FP = C_FH + 1024;
#define FLAG(i) ((u64*)(P.ws + WS_CTRL) + (i) * 16)

DI unsigned pk_bf16(float lo, float hi) { f32x2 v = {lo, hi}; return __builtin_bit_cast(unsigned, __builtin_convertvector(v, bf16x2_t)); }
DI float rbf(float f) { return __uint_as_float(pk_bf16(f, f) << 16); }
DI float bflo(unsigned u) { return __uint_as_float(u << 16); }
DI float bfhi(unsigned u) { return __uint_as_float(u & 0xffff0000u); }
DI float dot2(unsigned a, unsigned b, float c) { return __builtin_amdgcn_fdot2_f32_bf16(__builtin_bit_cast(bf16x2_t, a), __builtin_bit_cast(bf16x2_t, b), c, false); }
#define GLDS4(POL) asm volatile("s_mov_b32 m0, %0\n\ts_nop 0\n\tglobal_load_lds_dwordx4 %2, %1" POL "\n\tglobal_load_lds_dwordx4 %3, %1 offset:1024" POL \
    "\n\tglobal_load_lds_dwordx4 %4, %1 offset:2048" POL "\n\tglobal_load_lds_dwordx4 %5, %1 offset:3072" POL \
    :: "s"(lds), "s"(base), "v"(v0), "v"(v1), "v"(v2), "v"(v3) : "memory")
#define GLDS5(POL) asm volatile("s_mov_b32 m0, %0\n\ts_nop 0\n\tglobal_load_lds_dwordx4 %2, %1" POL "\n\tglobal_load_lds_dwordx4 %3, %1 offset:1024" POL \
    "\n\tglobal_load_lds_dwordx4 %4, %1 offset:2048" POL "\n\tglobal_load_lds_dwordx4 %5, %1 offset:3072" POL \
    "\n\ts_add_u32 m0, %0, 0x1000\n\ts_nop 0\n\tglobal_load_lds_dwordx4 %6, %1" POL \
    :: "s"(lds), "s"(base), "v"(v0), "v"(v1), "v"(v2), "v"(v3), "v"(v4) : "memory", "scc")
template <bool DEV> DI void glds4(unsigned lds, const char* base, unsigned v0, unsigned v1, unsigned v2, unsigned v3) {
  if (DEV) GLDS4(" sc1"); else GLDS4("");
}
template <bool DEV> DI void glds5(unsigned lds, const char* base, unsigned v0, unsigned v1, unsigned v2, unsigned v3, unsigned v4) {
  if (DEV) GLDS5(" sc1"); else GLDS5("");
}
template <int N> DI void wait_v() { asm volatile("s_waitcnt vmcnt(%0)" :: "i"(N) : "memory"); }
#define WAIT_L0() do { __builtin_amdgcn_s_waitcnt(0xC07F); asm volatile("" ::: "memory"); } while (0)
#define BAR() do { asm volatile("" ::: "memory"); __builtin_amdgcn_s_barrier(); asm volatile("" ::: "memory"); } while (0)
#define SGB(m, n) __builtin_amdgcn_sched_group_barrier(m, n, 0)
#define PIN() __builtin_amdgcn_sched_barrier(0)
DI void mf(f32x4& c, const bf16x8& a, const bf16x8& b, bool first) {
  if (first) asm volatile("v_mfma_f32_16x16x32_bf16 %0, %1, %2, 0" : "=&v"(c) : "v"(a), "v"(b));
  else asm volatile("v_mfma_f32_16x16x32_bf16 %0, %1, %2, %0" : "+v"(c) : "v"(a), "v"(b));
}
#define MFMA_DONE() asm volatile("s_nop 15")
template <int A, int B> DI void after_mfma(f32x4 (&c)[A][B]) {
#pragma unroll
  for (int i = 0; i < A; ++i)
#pragma unroll
    for (int j = 0; j < B; ++j) asm volatile("" : "+v"(c[i][j]));
}
#define STAMP_AT(i)

template <int CTRL> DI float dpp_add(float v) { return v + __builtin_bit_cast(float, __builtin_amdgcn_update_dpp(0, __builtin_bit_cast(int, v), CTRL, 0xf, 0xf, true)); }
DI float sum8(float v) { v = dpp_add<0xB1>(v); v = dpp_add<0x4E>(v); return dpp_add<0x141>(v); }
DI float sum16(float v) { return dpp_add<0x140>(sum8(v)); }
DI float xchg16(float keep_lo, float keep_hi) {
  const auto r = __builtin_amdgcn_permlane16_swap(__float_as_uint(keep_lo), __float_as_uint(keep_hi), false, false);
  return __uint_as_float(r[0]) + __uint_as_float(r[1]);
}
DI float xchg32(float keep_lo, float keep_hi) {
  const auto r = __builtin_amdgcn_permlane32_swap(__float_as_uint(keep_lo), __float_as_uint(keep_hi), false, false);
  return __uint_as_float(r[0]) + __uint_as_float(r[1]);
}
#define TIC(v)
#define ACC(a, x, y)
#define PUT(slot, a)
DI u64 ld_flag(const u64* p) { return __hip_atomic_load((const GAS u64*)p, __ATOMIC_RELAXED, __HIP_MEMORY_SCOPE_AGENT); }
DI void st_flag(u64* p, u64 v) { __hip_atomic_store((GAS u64*)p, v, __ATOMIC_RELAXED, __HIP_MEMORY_SCOPE_AGENT); }
DI void wait_flags(const u64* p, u64 tag) {
#pragma unroll 1
  for (int it = 0; it < 60000; ++it) {
    if (__all(ld_flag(p) == tag)) break;
    __builtin_amdgcn_s_sleep(4);
  }
  asm volatile("" ::: "memory");
}
DI void wait_flags2(const u64* p, const u64* p2, u64 tag) {
#pragma unroll 1
  for (int it = 0; it < 60000; ++it) {
    const u64 a = ld_flag(p), b = ld_flag(p2);
    if (__all(a == tag && b == tag)) break;
    __builtin_amdgcn_s_sleep(4);
  }
  asm volatile("" ::: "memory");
}
DI u64 read_tag(const char* ws) {
  u64 e;
  asm volatile("s_load_dwordx2 %0, %1, 0x0\n\ts_waitcnt lgkmcnt(0)" : "=s"(e) : "s"(ws + WS_CTRL) : "memory");
  return e * 6364136223846793005ull + 1442695040888963407ull;
}
DI void st16c(__amdgpu_buffer_rsrc_t r, unsigned off, u32x4 v) { __builtin_amdgcn_raw_buffer_store_b128(v, r, off, 0, 16); }
DI u32x4 ld16c(__amdgpu_buffer_rsrc_t r, unsigned off) { return __builtin_amdgcn_raw_buffer_load_b128(r, off, 0, 16); }

struct Params {
  const bf16_t *x, *wsr, *wsi, *wdr, *wdi, *wg, *wu, *wdn;
  bf16_t* out; char* ws;
};

constexpr int L_W = 49152, L_D = 98304, K1_TAB = 159744, LDS_BYTES = K1_TAB + 4096;
constexpr int T_RS = K1_TAB, T_RM = K1_TAB + 512, T_CS = K1_TAB + 1024, T_CD = K1_TAB + 2048, T_GO = K1_TAB + 3584, T_PF = K1_TAB + 3072;
constexpr int A_ST = 16384, B_ST = 20480, L_B = 4 * A_ST;

constexpr int NY = 16, NK2 = 28;
constexpr int F_Y = 147456, F_T = 0, FPITCH = 176;

template <bool TG>
DI void helper_p1(const Params& P, LAS char* smem, int lane, int lw, int mt, int ns, int split, int mt2, int nt, u64& tag, u64& fabv) {
  constexpr int NL = TG ? 12 : 13;
  const int n8 = ns & 7, hcol0 = TG ? 128 * n8 : 1024 + 64 * n8;
  const int rr = lane >> 3, c8 = lane & 7;
  const unsigned lds0 = (unsigned)(size_t)smem;
  unsigned vX[4], vW[4], vD[5];
#pragma unroll
  for (int j = 0; j < 4; ++j) {
    vX[j] = (32 * lw + 8 * j + rr) * 1280 + ((c8 ^ (TG ? rr : ((rr & 2) | ((j & 2) << 1)))) * 16) - j * 1024;
    vW[j] = (8 * j + rr) * 3072 + ((c8 ^ ((rr & 2) | ((j & 1) << 2))) * 16) - j * 1024;
  }
#pragma unroll
  for (int j = 0; j < 5; ++j) vD[j] = (40 * lw + 8 * j + rr) * 1280 + ((c8 ^ rr) * 16) - (j & 3) * 1024;
  const char* bx = (const char*)P.x + (size_t)mt * 128 * 1280;
  const char* bw = (const char*)((lw >> 1) ? P.wu : P.wg) + hcol0 * 2 + (TG ? (lw & 1) * 128 : (lw & 1) * (32 * 3072));
  const char* bd = (const char*)(n8 >= 4 ? P.wdi : P.wdr) + (size_t)(n8 & 3) * 160 * 1280;
  const unsigned ldsX = __builtin_amdgcn_readfirstlane(lds0 + lw * 4096);
  const unsigned ldsW = __builtin_amdgcn_readfirstlane(lds0 + L_W + (TG ? (lw & 1) * 49152 + (lw >> 1) * 8192 : lw * 4096));
  const unsigned ldsD = __builtin_amdgcn_readfirstlane(lds0 + L_D + lw * 5120);
  auto issue = [&](int kt, int buf) {
    glds4<false>(ldsX + buf * 16384, bx + kt * 128, vX[0], vX[1], vX[2], vX[3]);
    const char* pw = bw + kt * (64 * 3072);
    glds4<false>(ldsW + buf * 16384, pw, vW[0], vW[1], vW[2], vW[3]);
    if (TG) glds4<false>(ldsW + buf * 16384 + 4096, pw + 32 * 3072, vW[0], vW[1], vW[2], vW[3]);
    else glds5<false>(ldsD + buf * 20480, bd + kt * 128, vD[0], vD[1], vD[2], vD[3], vD[4]);
  };
  issue(0, 0); issue(1, 1);
  tag = read_tag(P.ws);
  const char* pfb = (const char*)(split < 2 ? P.wsr : P.wsi) + (size_t)(mt2 * 128 + nt * 32) * 4096 + (split & 1) * 2048;
  const unsigned pfv = (lane & 31) * 4096 + ((lw & 1) * 2 + (lane >> 5)) * 128;
  const u64* fs = FLAG(C_F0 + (lw == 0 ? mt * 64 + lane : (((lane >> 1) & 15) * 16 + ns) * 4 + 2 + (lane & 1)));
  u64 fsv = 0;
  int bn = 2;
#pragma unroll 1
  for (int k = 0; k < 8; ++k) {
    wait_v<NL>();
    BAR();
    if (lw < (TG ? 1 : 2)) {
      if (k == 4) asm volatile("global_load_dwordx2 %0, %1, off sc1" : "+v"(fsv) : "v"(fs) : "memory");
      if (k == 5) {
        asm volatile("" : "+v"(fsv));
        if (!__all(fsv == tag)) wait_flags(fs, tag);
        const char* src = P.ws + (lw == 0 ? WS_RS + mt * 1024 : WS_CD + n8 * 640);
        asm volatile("s_mov_b32 m0, %0\n\ts_nop 0\n\tglobal_load_lds_dwordx4 %1, %2 sc1" :: "s"(lds0 + (lw == 0 ? T_RS : T_CD)), "v"(lane * 16), "s"(src) : "memory");
      }
    }
    issue(k + 2, bn);
    bn = bn == 2 ? 0 : bn + 1;
    const int b = k >> 1;
    if (PFN && b < PFN && (k & 1) == (mt & 1) && lw >= 2)
      asm volatile("s_mov_b32 m0, %0\n\ts_nop 0\n\tglobal_load_lds_dword %1, %2" :: "s"(lds0 + T_PF), "v"(pfv), "s"(pfb + b * 512) : "memory");
  }
  wait_v<NL>();
  BAR();
  wait_v<0>();
  BAR();
  if (TG || lw < 2) {
    wait_flags(FLAG(C_F0 + ((lane & 15) * 16 + ns) * 4 + lw), tag);
    const __amdgpu_buffer_rsrc_t rw = __builtin_amdgcn_make_buffer_rsrc(P.ws, 0, WS_END, 0x00020000);
    f32x4 s = {0, 0, 0, 0};
#pragma unroll
    for (int i = 0; i < 4; ++i) s += __builtin_bit_cast(f32x4, ld16c(rw, WS_CS + ((ns * 4 + lw) * 16) * 256 + i * 1024 + lane * 16));
#pragma unroll
    for (int e = 0; e < 4; ++e) { s[e] = xchg16(s[e], s[e]); s[e] = xchg32(s[e], s[e]); }
    if (lane < 16) *(LAS f32x4*)(smem + T_CS + lw * 256 + lane * 16) = s;
  }
  if (!TG) __builtin_amdgcn_s_sleep(FABSL);
  fabv = ld_flag(FLAG(C_FAB + (nt * 64 + split * 16) * 2 + (lane & 31)));
  asm volatile("" : "+v"(fabv));
  if (lw == 0 && lane == 0) *(volatile LAS unsigned*)(smem + T_GO) = 0;
  WAIT_L0();
  BAR();
}

DI void helper_p2(const Params& P, LAS char* smem, int lane, int lw, int split, int mt, int nt, u64 fabv, u64 tag) {
  const unsigned lds0 = (unsigned)(size_t)smem;
  const int rr = lane >> 3;
  unsigned vY[4];
#pragma unroll
  for (int j = 0; j < 4; ++j) vY[j] = (32 * lw + 8 * j + rr) * 4096 + (((lane & 7) ^ rr) * 16) - j * 1024;
  const unsigned vL = lane * 16, vL4 = vL + 4096;
  const char* baseY = (const char*)(split < 2 ? P.wsr : P.wsi) + (size_t)mt * 128 * 4096 + (split & 1) * 2048;
  const char* baseAB = P.ws + WS_AB + (nt * 64 + split * 16) * B_ST + lw * 5120;
  const char* baseH = P.ws + WS_H + ((mt * 24 + (lw >> 1)) * 16 + (split & 1) * 8 + (lw & 1) * 4) * 1024;
  const char* baseWD = P.ws + WS_WD + ((nt * 48 + lw) * 10 + (split >> 1) * 5) * 1024;
  const int rot = ROT2 ? (nt & 1) * 8 : 0;
  const unsigned ldsA = __builtin_amdgcn_readfirstlane(lds0 + lw * 4096), ldsB = __builtin_amdgcn_readfirstlane(lds0 + L_B + lw * 5120);
  auto issueYA = [&](int k) { glds4<false>(ldsA + (k & 3) * A_ST, baseY + ((k + rot) & 15) * 128, vY[0], vY[1], vY[2], vY[3]); };
  auto issueYB = [&](int k) { glds5<true>(ldsB + (k & 3) * B_ST, baseAB + ((k + rot) & 15) * B_ST, vL, vL, vL, vL, vL4); };
  auto issueD = [&](int k) {
    glds4<true>(ldsA + (k & 3) * A_ST, baseH + (k - NY) * (2 * A_ST), vL, vL, vL, vL);
    glds5<true>(ldsB + (k & 3) * B_ST, baseWD + (k - NY) * (2 * B_ST), vL, vL, vL, vL, vL4);
  };
  STAMP_AT(8);
  issueYA(0); issueYA(1);
  const bool ab_seen = __all(fabv == tag);
  if (lw == 0) {
    if (!ab_seen) wait_flags(FLAG(C_FAB + (nt * 64 + split * 16) * 2 + (lane & 31)), tag);
    if (lane == 0) *(volatile LAS unsigned*)(smem + T_GO) = 1;
  } else if (!ab_seen) {
#pragma unroll 1
    for (int it = 0; it < 4000000; ++it) {
      if (*(volatile LAS unsigned*)(smem + T_GO) != 0) break;
      __builtin_amdgcn_s_sleep(1);
    }
  }
  asm volatile("" ::: "memory");
  STAMP_AT(9);
  const u64* fh = FLAG(C_FH + mt * 64 + (lane & 31) * 2 + (split & 1));
  const int l36 = min(lane, 35);
  const int wt = (nt * 240 + ((l36 / 3) * 4 + lw) * 5 + (split >> 1) * 2 + l36 % 3) >> 1;
  const u64* fw = FLAG(C_FH + (wt >> 5) * 64 + (wt & 31));
  u64 fhv = 0, fwv = 0;
  u64 a0 = 0, a1 = 0, a2 = 0;
  issueYB(0); issueYB(1);
  wait_v<5>(); BAR();
  issueYA(2); issueYB(2);
  wait_v<9>(); BAR();
  issueYA(3); issueYB(3); issueYA(4); issueYB(4);
#pragma unroll 1
  for (int k = 2; k < 13; ++k) {
    TIC(t0); wait_v<18>(); TIC(t1); BAR(); TIC(t2);
    if (k == 10) asm volatile("global_load_dwordx2 %0, %2, off sc1\n\tglobal_load_dwordx2 %1, %3, off sc1" : "+v"(fhv), "+v"(fwv) : "v"(fh), "v"(fw) : "memory");
    issueYA(k + 3); issueYB(k + 3);
    TIC(t3);
    ACC(a0, t0, t1); ACC(a1, t1, t2); ACC(a2, t2, t3);
  }
  PUT(3, a0); PUT(4, a1); PUT(5, a2);
#pragma unroll 1
  for (int k = 13; k < NK2 - 3; ++k) {
    wait_v<18>(); BAR();
    if (k == 13) {
      asm volatile("" : "+v"(fhv), "+v"(fwv));
      if (!__all(fhv == tag && fwv == tag)) wait_flags2(fh, fw, tag);
    }
    issueD(k + 3);
  }
  wait_v<18>(); BAR();
  wait_v<9>(); BAR();
  wait_v<0>(); BAR();
  STAMP_AT(10);
  if (blockIdx.x == 0 && lw == 0) {
#pragma unroll 1
    for (int i = 0; i < 4; ++i) wait_flags(FLAG(C_F0 + (i * 64 + lane) * 4), tag);
    if (lane == 0) st_flag(FLAG(C_E), tag);
  }
}

struct Carry { u32x4 v[12]; };

template <bool TG>
DI void mfma_p1(const Params& P, LAS char* smem, int lane, int w, int mt, int ns, u64& tag, Carry& cy) {
  const int n8 = ns & 7, hcol0 = TG ? 128 * n8 : 1024 + 64 * n8;
  const __amdgpu_buffer_rsrc_t rw = __builtin_amdgcn_make_buffer_rsrc(P.ws, 0, WS_END, 0x00020000);
  const int row = mt * 128 + ns * 8 + 2 * w + (lane >> 5);
  const int rr = lane >> 3, c8 = lane & 7;
  const bool colw = TG || w < 2;
  u32x2 vx[5]; u32x4 vw[10];
  {
    const char* px = (const char*)P.x + (size_t)row * 1280 + (lane & 31) * 8;
#pragma unroll
    for (int i = 0; i < 5; ++i) vx[i] = *(const u32x2*)(px + i * 256);
    if (colw) {
      const bf16_t* wsel = (TG ? (w >> 1) : w) ? P.wu : P.wg;
      const char* pw = (const char*)wsel + (size_t)(40 * mt + rr) * 3072 + (hcol0 + (TG ? (w & 1) * 64 : 0)) * 2 + c8 * 16;
#pragma unroll
      for (int i = 0; i < 5; ++i) vw[i] = *(const u32x4*)(pw + i * (8 * 3072));
    } else {
      const int e = (n8 & 3) * 160 + 10 * mt + 5 * (w - 2) + min(rr, 4);
      const char* pd = (const char*)(n8 >= 4 ? P.wdi : P.wdr) + (size_t)e * 1280 + c8 * 16;
#pragma unroll
      for (int i = 0; i < 10; ++i) vw[i] = *(const u32x4*)(pd + i * 128);
    }
  }
  tag = read_tag(P.ws);
  const int wm = w & 1, wn = w >> 1;
  int q = lane >> 4, rho = lane & 15;
  constexpr int NC = TG ? 4 : 2;
  unsigned vX0 = TG ? wm * 8192 + rho * 128 + ((q ^ (rho & 7)) * 16)
                          : wm * 8192 + (8 * (rho >> 2) + (rho & 3)) * 128 + ((q ^ ((rho & 2) | ((rho >> 1) & 4))) * 16);
  unsigned vX1 = vX0 ^ 64;
  unsigned vW[NC];
  {
    const int qq = rho >> 2, p = lane & 3, swz = (qq & 2) | ((q & 1) << 2);
#pragma unroll
    for (int c = 0; c < NC; ++c) {
      const int cc = TG ? c : (2 * wn + c);
      vW[c] = L_W + (TG ? wn * 49152 : 0) + (8 * q + qq) * 128 + ((((2 * cc) ^ swz) + (p >> 1)) * 16) + 8 * (p & 1);
    }
  }
  unsigned vD0 = L_D + (80 * wn + rho) * 128 + ((q ^ (rho & 7)) * 16), vD1 = vD0 ^ 64;
  asm volatile("" : "+v"(vX0), "+v"(vX1));
  if (!TG) asm volatile("" : "+v"(vD0), "+v"(vD1));
#pragma unroll
  for (int c = 0; c < NC; ++c) asm volatile("" : "+v"(vW[c]));
  f32x4 aG[NC][4], aU[NC][4], aA[4][5];
  {
    unsigned ONE2 = 0x3f803f80u, SLO = 0x00003f80u, SHI = 0x3f800000u;
    asm volatile("" : "+v"(ONE2), "+v"(SLO), "+v"(SHI));
    float sx = 0, sq = 0;
#pragma unroll
    for (int i = 0; i < 5; ++i)
#pragma unroll
      for (int e = 0; e < 2; ++e) { sx = dot2(vx[i][e], ONE2, sx); sq = dot2(vx[i][e], vx[i][e], sq); }
    sx = sum16(sx); sq = sum16(sq);
    sx = xchg16(sx, sx); sq = xchg16(sq, sq);
    const float mean = sx * (1.0f / D);
    const float var = fmaxf(sq * (1.0f / D) - mean * mean, 0.f);
    const float sd = rbf(__builtin_amdgcn_sqrtf(rbf(rbf(var) + 1e-5f)));
    const float rso = (lane & 1) ? rbf(mean) : __builtin_amdgcn_rcpf(sd);
    float cres; unsigned coff;
    if (colw) {
      float c[8] = {0, 0, 0, 0, 0, 0, 0, 0};
#pragma unroll
      for (int i = 0; i < 5; ++i)
#pragma unroll
        for (int e = 0; e < 4; ++e) { c[2 * e] = dot2(vw[i][e], SLO, c[2 * e]); c[2 * e + 1] = dot2(vw[i][e], SHI, c[2 * e + 1]); }
      float d4[4], d2[2];
#pragma unroll
      for (int e = 0; e < 4; ++e) d4[e] = xchg32(c[e], c[4 + e]);
#pragma unroll
      for (int e = 0; e < 2; ++e) d2[e] = xchg16(d4[e], d4[2 + e]);
      d2[0] = dpp_add<0x128>(d2[0]); d2[1] = dpp_add<0x128>(d2[1]);
      cres = (lane & 8) ? d2[1] : d2[0];
      coff = WS_CS + (((ns * 4 + w) * 16 + mt) * 64 + 8 * c8 + rr) * 4;
    } else {
      float c0 = 0, c1 = 0;
#pragma unroll
      for (int i = 0; i < 10; ++i) { c0 = dot2(vw[i][0], ONE2, c0); c1 = dot2(vw[i][1], ONE2, c1); c0 = dot2(vw[i][2], ONE2, c0); c1 = dot2(vw[i][3], ONE2, c1); }
      cres = sum8(c0 + c1);
      coff = (c8 == 0 && rr < 5) ? WS_CD + (n8 * 160 + 10 * mt + 5 * (w - 2) + rr) * 4 : 0xfffffff0u;
    }
    asm volatile("" : "+v"(cres));
    __builtin_amdgcn_raw_buffer_store_b32(__float_as_uint(cres), rw, coff, 0, 16);
    __builtin_amdgcn_raw_buffer_store_b32(__float_as_uint(rso), rw, (lane & 30) == 0 ? WS_RS + mt * 1024 + (lane & 1) * 512 + (row & 127) * 4 : 0xfffffff0u, 0, 16);
  }
  bf16x8 FX[2][4], FD[2][5];
  s16x4 FW[2][2 * NC][2];
  constexpr int NR = TG ? 20 : 17, NM = TG ? 32 : 36;
  auto rd1 = [&](int s, int buf, int ks, int r) {
    if (r >= 2 && r < 6) {
      const int i = r - 2;
      FX[s][i] = *(LAS bf16x8*)(smem + (ks ? vX1 : vX0) + buf * 16384 + (TG ? i * 2048 : (i >> 1) * 4096 + (i & 1) * 512));
    } else if (r < 4 + 4 * NC) {
      const int h = r < 2 ? r : r - 4, f = h >> 1;
      FW[s][f][h & 1] = __builtin_amdgcn_ds_read_tr16_b64_v4i16((LAS s16x4*)(smem + vW[f >> 1] + buf * 16384 + ks * 4096 + (f & 1) * 8192 + (h & 1) * 512));
    } else {
      const int jj = r - (4 + 4 * NC);
      FD[s][jj] = *(LAS bf16x8*)(smem + (ks ? vD1 : vD0) + buf * 20480 + jj * 2048);
    }
  };
  auto mm1 = [&](int s, int m, bool first) {
    const int f = m >> 2, i = m & 3;
    if (f < 2 * NC) {
      const bf16x8 wf = __builtin_shufflevector(FW[s][f][0], FW[s][f][1], 0, 1, 2, 3, 4, 5, 6, 7);
      mf((f & 1) ? aU[f >> 1][i] : aG[f >> 1][i], wf, FX[s][i], first);
    } else mf(aA[i][f - 2 * NC], FX[s][i], FD[s][f - 2 * NC], first);
  };
  auto seg = [&](int s, bool first, int m0, int m1, int rk, int buf, int ks) {
    int r = 0;
#pragma unroll
    for (int m = m0; m < m1; ++m) {
      mm1(s, m, first);
      if (rk == 2 && r == 0) r = 2;
      if (rk == 2 && r == 6) r = 4 + 4 * NC;
      const int rend = rk == 0 ? 0 : rk == 3 ? 4 + 4 * NC : NR;
      if (r < rend && (TG ? (m % 5 != 4) : (rk == 3 || m % 3 != 2))) { rd1(s ^ 1, buf, ks, r); ++r; }
      PIN();
    }
  };
  BAR();
  STAMP_AT(1);
#pragma unroll
  for (int r = 0; r < NR; ++r) rd1(0, 0, 0, r);
  PIN();
  constexpr int KJ = TG ? 10 : 7;
#pragma unroll
  for (int k = 0; k < 10; ++k) {
    const int m0 = k < KJ ? 0 : 8 * NC;
    seg(0, k == 0, m0, NM, k < KJ ? 1 : 2, k % 3, 1);
    if (k == 1) {
      wait_v<0>();
      if (lane == 0) st_flag(FLAG(C_F0 + (mt * 16 + ns) * 4 + w), tag);
    }
    if (k < 9) {
      WAIT_L0();
      BAR();
      PIN();
    }
    if (k < 9) seg(1, false, m0, NM, k + 1 < KJ ? 1 : 2, (k + 1) % 3, 0);
    else seg(1, false, m0, NM, TG ? 0 : 3, KJ % 3, 0);
    WAIT_L0(); PIN();
  }
  if (!TG) {
    MFMA_DONE();
    after_mfma(aA);
    asm volatile("" : "+v"(lane)); q = lane >> 4; rho = lane & 15;
    STAMP_AT(4);
    const float sgn = (n8 >= 4) ? -1.0f : 1.0f;
    float cs[5];
#pragma unroll
    for (int j = 0; j < 5; ++j) cs[j] = *(LAS float*)(smem + T_CD + (80 * wn + 16 * j + rho) * 4);
    const int kt = (n8 >= 4 ? 32 : 0) + mt * 2 + wm;
#pragma unroll
    for (int I = 0; I < 2; ++I) {
      f32x4 r4[2], m4[2];
#pragma unroll
      for (int t = 0; t < 2; ++t) {
        const int row = 64 * wm + 32 * I + 8 * q + 4 * t;
        r4[t] = *(LAS f32x4*)(smem + T_RS + row * 4) * sgn; m4[t] = *(LAS f32x4*)(smem + T_RM + row * 4);
      }
      const unsigned dst = WS_AB + ((((n8 & 3) * 64 + kt) * 2 + I) * 10 + 5 * wn) * 1024 + lane * 16;
#pragma unroll
      for (int j = 0; j < 5; ++j) {
        const f32x4 v0 = (aA[2 * I][j] - m4[0] * cs[j]) * r4[0], v1 = (aA[2 * I + 1][j] - m4[1] * cs[j]) * r4[1];
        const u32x4 o = {pk_bf16(v0[0], v0[1]), pk_bf16(v0[2], v0[3]), pk_bf16(v1[0], v1[1]), pk_bf16(v1[2], v1[3])};
        st16c(rw, dst + j * 1024, o);
      }
    }
    STAMP_AT(7);
    WAIT_L0(); PIN();
#pragma unroll
    for (int k = KJ; k < 10; ++k) {
      seg(0, false, 0, 8 * NC, 3, k % 3, 1);
      WAIT_L0(); PIN();
      if (k == KJ + AFL) {
        wait_v<0>();
        if (lane == 0) st_flag(FLAG(C_FAB + ((n8 & 3) * 64 + kt) * 2 + wn), tag);
        PIN();
      }
      seg(1, false, 0, 8 * NC, k < 9 ? 3 : 0, (k + 1) % 3, 0);
      WAIT_L0(); PIN();
    }
  }
  MFMA_DONE();
  after_mfma(aG); after_mfma(aU);
  asm volatile("" : "+v"(lane)); q = lane >> 4; rho = lane & 15;
  unsigned wv[2][8];
  if (TG) {
#pragma unroll
    for (int u = 0; u < 2; ++u) {
      const int task = min(((mt * 8 + ns) * 4 + w) * 2 + u, 959);
      const unsigned* src = (const unsigned*)(P.wdn + (size_t)(((task % 240) / 5) * 32 + 4 * q) * D + (task / 240) * 160 + (task % 5) * 32 + 2 * rho);
#pragma unroll
      for (int e = 0; e < 8; ++e) wv[u][e] = src[((e & 3) + (e >> 2) * 16) * (D / 2)];
    }
  }
  BAR();
  STAMP_AT(2);
  {
    const float K0 = -2.0f * 0.7978845608028654f * 1.4426950408889634f, K1 = K0 * 0.044715f;
    float mu[4], c0[4], c1[4], v2[4];
#pragma unroll
    for (int i = 0; i < 4; ++i) {
      const int row = TG ? 64 * wm + 16 * i + rho : 64 * wm + 32 * (i >> 1) + 8 * (rho >> 2) + 4 * (i & 1) + (rho & 3);
      const float rs = *(LAS float*)(smem + T_RS + row * 4);
      mu[i] = *(LAS float*)(smem + T_RM + row * 4);
      c0[i] = K0 * rs; c1[i] = K1 * rs * rs * rs; v2[i] = __builtin_amdgcn_rcpf(rs * rs);
    }
#pragma unroll
    for (int J = 0; J < NC / 2; ++J) {
      f32x4 cg[2], cu[2];
#pragma unroll
      for (int t = 0; t < 2; ++t) {
        const int c = 2 * J + t;
        const int o = TG ? (wn * 256 + c * 64 + q * 16) : ((2 * wn + c) * 64 + q * 16);
        cg[t] = *(LAS f32x4*)(smem + T_CS + o);
        cu[t] = *(LAS f32x4*)(smem + T_CS + (TG ? 512 : 256) + o);
      }
#pragma unroll
      for (int i = 0; i < 4; ++i) {
        u32x4 o;
#pragma unroll
        for (int t = 0; t < 2; ++t) {
          const f32x4 g = aG[2 * J + t][i] - cg[t] * mu[i];
          const f32x4 u = aU[2 * J + t][i] - cu[t] * mu[i];
          const f32x4 z = g * (g * g * c1[i] + c0[i]);
          f32x4 e;
#pragma unroll
          for (int r = 0; r < 4; ++r) e[r] = __builtin_amdgcn_exp2f(z[r]);
          e = e * v2[i] + v2[i];
#pragma unroll
          for (int r = 0; r < 4; ++r) e[r] = __builtin_amdgcn_rcpf(e[r]);
          const f32x4 hv = g * u * e;
          o[2 * t] = pk_bf16(hv[0], hv[1]); o[2 * t + 1] = pk_bf16(hv[2], hv[3]);
        }
        cy.v[J * 4 + i] = o;
        asm volatile("" : "+v"(cy.v[J * 4 + i]));
      }
    }
  }
  if (TG) {
    const bool up = rho & 8;
#pragma unroll
    for (int u = 0; u < 2; ++u)
#pragma unroll
      for (int e = 0; e < 4; ++e) {
        const unsigned lo = __builtin_amdgcn_perm(wv[u][2 * e + 1], wv[u][2 * e], 0x05040100u), hi = __builtin_amdgcn_perm(wv[u][2 * e + 1], wv[u][2 * e], 0x07060302u);
        const unsigned t = __builtin_amdgcn_update_dpp(0, up ? lo : hi, 0x128, 0xf, 0xf, true);
        cy.v[8 + 2 * u][e] = up ? t : lo;
        cy.v[9 + 2 * u][e] = up ? hi : t;
      }
  } else {
#pragma unroll
    for (int n = 4; n < 12; ++n) cy.v[n] = (u32x4){0, 0, 0, 0};
  }
  STAMP_AT(3);
}

DI void mfma_p2(const Params& P, LAS char* smem, int lane, int w, int split, int mt, int nt, int mt1, int ns, const Carry& cy, u64 tag) {
  asm volatile("" : "+v"(lane));
  const int wm = w & 1, wn = w >> 1, q = lane >> 4, r15 = lane & 15;
  unsigned vY0 = wm * 8192 + r15 * 128 + ((q ^ (r15 & 7)) * 16), vY1 = vY0 ^ 64;
  unsigned vB0 = L_B + wn * 5120 + lane * 16, vB2 = vB0 + 2 * B_ST;
  unsigned vH = w * 2048 + lane * 16, vW0 = L_B + lane * 16, vW2 = vW0 + 2 * B_ST;
  asm volatile("" : "+v"(vY0), "+v"(vY1), "+v"(vB0), "+v"(vB2), "+v"(vH), "+v"(vW0), "+v"(vW2));
  const __amdgpu_buffer_rsrc_t rw = __builtin_amdgcn_make_buffer_rsrc(P.ws, 0, WS_END, 0x00020000);
  const int tile = mt * 4 + nt;
  f32x4 acc[4][5], dac[1][5];
  bf16x8 FA[2][4], FB[2][5], GA[2][2], GB[2][10];
  auto rdy = [&](int s, int k, int ks, int r) {
    const int buf = k & 3;
    if (r >= 1 && r < 5) FA[s][r - 1] = *(LAS bf16x8*)(smem + (ks ? vY1 : vY0) + buf * A_ST + (r - 1) * 2048);
    else { const int j = r ? r - 4 : 0; FB[s][j] = *(LAS bf16x8*)(smem + (buf >= 2 ? vB2 : vB0) + (buf & 1) * B_ST + ks * 10240 + j * 1024); }
  };
  auto rdd = [&](int s, int k, int hf, int r) {
    const int buf = k & 3, u = r / 6, j = r % 6;
    if (j == 0) GA[s][u] = *(LAS bf16x8*)(smem + vH + buf * A_ST + hf * 8192 + u * 1024);
    else GB[s][u * 5 + j - 1] = *(LAS bf16x8*)(smem + (buf >= 2 ? vW2 : vW0) + (buf & 1) * B_ST + hf * 10240 + u * 5120 + (j - 1) * 1024);
  };
  auto segy = [&](int s, bool first, int k, int ks) {
    int r = 0;
#pragma unroll
    for (int m = 0; m < 20; ++m) {
      mf(acc[m & 3][m >> 2], FB[s][m >> 2], FA[s][m & 3], first);
      if (k < NY) { if (r < 9 && m % 3 != 2) { rdy(s ^ 1, k, ks, r); ++r; } }
      else if (r < 12) { rdd(s ^ 1, k, ks, r); ++r; }
      PIN();
    }
  };
  auto segd = [&](int s, bool first, bool reads, int k, int hf) {
#pragma unroll
    for (int m = 0; m < 10; ++m) {
      mf(dac[0][m % 5], GB[s][m], GA[s][m / 5], first && m < 5);
      if (reads && m < 6) { rdd(s ^ 1, k, hf, 2 * m); rdd(s ^ 1, k, hf, 2 * m + 1); }
      PIN();
    }
  };
  const bool tg = ns < 8;
  const int f0 = tg ? 128 * ns + wn * 64 : 1024 + 64 * (ns & 7) + wn * 32;
  const unsigned hb = WS_H + (((mt1 * 24 + (f0 >> 6)) * 8 + 4 * wm) * 2 + ((f0 >> 5) & 1)) * 1024;
  const unsigned hv = tg ? lane * 16 : (r15 >> 3) * 2048 + (8 * ((r15 >> 2) & 1) + (r15 & 3) + 16 * q) * 16;
  const unsigned wdv = (2 * (r15 & 7) + (r15 >> 3) + 16 * q) * 16;
  const int wt = (mt1 * 8 + ns) * 4 + w;
  auto late_store = [&](int n) {
    if (n < 4) __builtin_amdgcn_raw_buffer_store_b128(cy.v[n], rw, hv, hb + (tg ? n * 2048 : (n >> 1) * 4096 + (n & 1) * 64), 16);
    else if (n < 8) { if (tg) __builtin_amdgcn_raw_buffer_store_b128(cy.v[n], rw, hv, hb + (n - 4) * 2048 + 1024, 16); }
    else if (tg && wt < 480) {
      const int task = wt * 2 + ((n - 8) >> 1);
      __builtin_amdgcn_raw_buffer_store_b128(cy.v[n], rw, wdv, WS_WD + (((task / 240) * 48 + (task % 240) / 5) * 10 + (task % 5) * 2 + (n & 1)) * 1024, 16);
    }
  };
  BAR();
  STAMP_AT(5);
#pragma unroll
  for (int r = 0; r < 9; ++r) rdy(0, 0, 0, r);
  PIN();
#pragma unroll
  for (int k = 0; k < NY; ++k) {
    segy(0, k == 0, k, 1);
    if (k < 6) { late_store(2 * k); late_store(2 * k + 1); }
    if (k == 7) {
      wait_v<0>();
      if (lane == 0) st_flag(FLAG(C_FH + mt1 * 64 + ns * 4 + w), tag);
    }
    WAIT_L0();
    BAR();
    PIN();
    segy(1, false, k + 1, 0);
    WAIT_L0(); PIN();
  }
  MFMA_DONE();
  after_mfma(acc);
#pragma unroll
  for (int i = 0; i < 4; ++i) {
    unsigned pk[10];
#pragma unroll
    for (int j = 0; j < 5; ++j) { pk[2 * j] = pk_bf16(acc[i][j][0], acc[i][j][1]); pk[2 * j + 1] = pk_bf16(acc[i][j][2], acc[i][j][3]); }
    if (w == split) {
      LAS char* dst = smem + F_Y + i * 2560;
      *(LAS u32x4*)(dst + lane * 16) = (u32x4){pk[0], pk[1], pk[2], pk[3]};
      *(LAS u32x4*)(dst + 1024 + lane * 16) = (u32x4){pk[4], pk[5], pk[6], pk[7]};
      *(LAS u32x2*)(dst + 2048 + lane * 8) = (u32x2){pk[8], pk[9]};
    } else {
      const unsigned dst = WS_P + (((tile * 4 + w) * 3 + (split ^ w) - 1) * 4 + i) * 2560;
      st16c(rw, dst + lane * 16, (u32x4){pk[0], pk[1], pk[2], pk[3]});
      st16c(rw, dst + 1024 + lane * 16, (u32x4){pk[4], pk[5], pk[6], pk[7]});
      __builtin_amdgcn_raw_buffer_store_b64((u32x2){pk[8], pk[9]}, rw, dst + 2048 + lane * 8, 0, 16);
    }
  }
  PIN();
  const int orow = mt * 128 + (split & 1) * 64 + w * 16, ocol = nt * 160 + (split >> 1) * 80;
  const u64* fp = FLAG(C_FP + (tile * 4 + split) * 4 + min(lane, 2));
  u64 fpv = 0;
  u32x4 xv[3], pa[3][2]; u32x2 pb[3];
#pragma unroll
  for (int k = NY; k < NK2; ++k) {
    segd(0, k == NY, true, k, 1);
    if (k == NY + YFL && w != split) {
      wait_v<0>();
      if (lane == 0) st_flag(FLAG(C_FP + (tile * 4 + w) * 4 + (split ^ w) - 1), tag);
    }
    if (k == NY + YPL) fpv = ld_flag(fp);
    if (k == NY + YPL + 1) {
      if (!__all(fpv == tag)) wait_flags(fp, tag);
      asm volatile("" ::: "memory");
#pragma unroll
      for (int s = 0; s < 3; ++s) {
        const unsigned src = WS_P + (((tile * 4 + split) * 3 + s) * 4 + w) * 2560;
        pa[s][0] = ld16c(rw, src + lane * 16); pa[s][1] = ld16c(rw, src + 1024 + lane * 16);
        pb[s] = __builtin_amdgcn_raw_buffer_load_b64(rw, src + 2048 + lane * 8, 0, 16);
      }
#pragma unroll
      for (int i = 0; i < 3; ++i) {
        const int n = min(i * 64 + lane, 159);
        xv[i] = *(const u32x4*)(P.x + (size_t)(orow + n / 10) * D + ocol + (n % 10) * 8);
      }
    }
    if (k + 1 < NK2) {
      WAIT_L0();
      BAR();
      PIN();
    }
    segd(1, false, k + 1 < NK2, k + 1, 0);
    WAIT_L0(); PIN();
  }
  MFMA_DONE();
  after_mfma(dac);
  STAMP_AT(6);
  LAS char* po = smem + F_Y + w * 2560;
  const u32x4 oa0 = *(LAS u32x4*)(po + lane * 16), oa1 = *(LAS u32x4*)(po + 1024 + lane * 16);
  const u32x2 ob = *(LAS u32x2*)(po + 2048 + lane * 8);
  LAS char* pt = smem + F_T + w * (16 * FPITCH);
#pragma unroll
  for (int j = 0; j < 5; ++j) {
    const unsigned l0 = j < 2 ? oa0[(j & 1) * 2] : j < 4 ? oa1[(j & 1) * 2] : ob[0], h0 = j < 2 ? oa0[(j & 1) * 2 + 1] : j < 4 ? oa1[(j & 1) * 2 + 1] : ob[1];
    f32x4 y = {bflo(l0), bfhi(l0), bflo(h0), bfhi(h0)};
#pragma unroll
    for (int s = 0; s < 3; ++s) {
      const unsigned lo = j < 4 ? pa[s][j >> 1][(j & 1) * 2] : pb[s][0], hi = j < 4 ? pa[s][j >> 1][(j & 1) * 2 + 1] : pb[s][1];
      y += (f32x4){bflo(lo), bfhi(lo), bflo(hi), bfhi(hi)};
    }
    const f32x4 d = dac[0][j];
    f32x4 e = d * -1.4426950408889634f;
#pragma unroll
    for (int r = 0; r < 4; ++r) e[r] = __builtin_amdgcn_exp2f(e[r]);
    e = e + 1.0f;
#pragma unroll
    for (int r = 0; r < 4; ++r) e[r] = __builtin_amdgcn_rcpf(e[r]);
    e = e * d * y;
    *(LAS u32x2*)(pt + r15 * FPITCH + j * 32 + q * 8) = (u32x2){pk_bf16(e[0], e[1]), pk_bf16(e[2], e[3])};
  }
  WAIT_L0();
#pragma unroll
  for (int i = 0; i < 3; ++i) {
    const int n = i * 64 + lane, nc = min(n, 159);
    const u32x4 v = *(LAS u32x4*)(pt + (nc / 10) * FPITCH + (nc % 10) * 16);
    u32x4 o;
#pragma unroll
    for (int d = 0; d < 4; ++d) o[d] = pk_bf16(bflo(v[d]) + bflo(xv[i][d]), bfhi(v[d]) + bfhi(xv[i][d]));
    if (n < 160) *(u32x4*)(P.out + (size_t)(orow + nc / 10) * D + ocol + (nc % 10) * 8) = o;
  }
}

__global__ __launch_bounds__(512, 2)
void fused(Params P) {
  extern __shared__ __attribute__((aligned(16))) char smem_[];
  LAS char* smem = (LAS char*)smem_;
  const int tid = threadIdx.x, lane = tid & 63, w = __builtin_amdgcn_readfirstlane(tid >> 6);
  const int bid = blockIdx.x, xcd = bid & 7, idx = bid >> 3;
  u64 tag;
  STAMP_AT(0);
  const int mt1 = (xcd & 1) * 8 + (idx & 7), j = idx >> 3, ns = (j & 1) * 8 + (xcd >> 1) * 2 + (j >> 1);
  const int split = xcd >> 1, mt2 = (xcd & 1) * 8 + (idx >> 2), nt = idx & 3;
  if (w >= 4) {
    __builtin_amdgcn_s_setprio(HPRIO);
    u64 fabv;
    if (j & 1) helper_p1<false>(P, smem, lane, w & 3, mt1, ns, split, mt2, nt, tag, fabv); else helper_p1<true>(P, smem, lane, w & 3, mt1, ns, split, mt2, nt, tag, fabv);
    helper_p2(P, smem, lane, w & 3, split, mt2, nt, fabv, tag);
  } else {
    Carry cy;
    if (j & 1) mfma_p1<false>(P, smem, lane, w, mt1, ns, tag, cy); else mfma_p1<true>(P, smem, lane, w, mt1, ns, tag, cy);
    mfma_p2(P, smem, lane, w, split, mt2, nt, mt1, ns, cy, tag);
    __builtin_amdgcn_s_waitcnt(0);
  }
}

extern "C" void kernel_launch(void* const* d_in, const int* in_sizes, int n_in, void* d_out, int out_size, void* d_ws, size_t ws_size, hipStream_t stream) {
  Params P;
  P.x = (const bf16_t*)d_in[0]; P.wsr = (const bf16_t*)d_in[1]; P.wsi = (const bf16_t*)d_in[2];
  P.wdr = (const bf16_t*)d_in[3]; P.wdi = (const bf16_t*)d_in[4];
  P.wg = (const bf16_t*)d_in[6]; P.wu = (const bf16_t*)d_in[7]; P.wdn = (const bf16_t*)d_in[8];
  P.out = (bf16_t*)d_out; P.ws = (char*)d_ws;
  (void)hipFuncSetAttribute((const void*)fused, hipFuncAttributeMaxDynamicSharedMemorySize, LDS_BYTES);
  fused<<<256, 512, LDS_BYTES, stream>>>(P);
}
```

```cpp
#include <hip/hip_runtime.h>
#include <hip/hip_bf16.h>
typedef unsigned short bf16_t;
typedef unsigned long long u64;
typedef short bf16x8 __attribute__((ext_vector_type(8)));
typedef short s16x4 __attribute__((ext_vector_type(4)));
typedef float f32x4 __attribute__((ext_vector_type(4)));
typedef float f32x2 __attribute__((ext_vector_type(2)));
typedef unsigned u32x4 __attribute__((ext_vector_type(4)));
typedef unsigned u32x2 __attribute__((ext_vector_type(2)));
typedef __bf16 bf16x2_t __attribute__((ext_vector_type(2)));
#define LAS __attribute__((address_space(3)))
#define GAS __attribute__((address_space(1)))
#define DI __device__ __forceinline__

#define STAMP 0
#define NPRE 2
#define ROT2 0
#define YFL 4
#define FABSL 1
#define AFL 2
#define TK0 0
#define TPS 2
#define YPL 8
#define XROT 0
#define HPRIO 0
#define PFN 4

constexpr int S = 2048, D = 640, F = 1536;
constexpr unsigned WS_AB = 0, WS_H = WS_AB + 2 * S * D * 2, WS_P = WS_H + S * F * 2, WS_WD = WS_P + 3 * S * D * 2, WS_RS = WS_WD + F * D * 2,
                   WS_CS = WS_RS + S * 8, WS_CD = WS_CS + 16 * 4 * 16 * 64 * 4, WS_CTRL = WS_CD + 8192, WS_END = WS_CTRL + 5120 * 128;
constexpr int C_E = 0, C_F0 = 1, C_FAB = C_F0 + 1024, C_FH = C_FAB + 512, C_FP = C_FH + 1024;
#define FLAG(i) ((u64*)(P.ws + WS_CTRL) + (i) * 16)

DI unsigned pk_bf16(float lo, float hi) { f32x2 v = {lo, hi}; return __builtin_bit_cast(unsigned, __builtin_convertvector(v, bf16x2_t)); }
DI float rbf(float f) { return __uint_as_float(pk_bf16(f, f) << 16); }
DI float bflo(unsigned u) { return __uint_as_float(u << 16); }
DI float bfhi(unsigned u) { return __uint_as_float(u & 0xffff0000u); }
DI float dot2(unsigned a, unsigned b, float c) { return __builtin_amdgcn_fdot2_f32_bf16(__builtin_bit_cast(bf16x2_t, a), __builtin_bit_cast(bf16x2_t, b), c, false); }
#define GLDS4(POL) asm volatile("s_mov_b32 m0, %0\n\ts_nop 0\n\tglobal_load_lds_dwordx4 %2, %1" POL "\n\tglobal_load_lds_dwordx4 %3, %1 offset:1024" POL \
    "\n\tglobal_load_lds_dwordx4 %4, %1 offset:2048" POL "\n\tglobal_load_lds_dwordx4 %5, %1 offset:3072" POL \
    :: "s"(lds), "s"(base), "v"(v0), "v"(v1), "v"(v2), "v"(v3) : "memory")
#define GLDS5(POL) asm volatile("s_mov_b32 m0, %0\n\ts_nop 0\n\tglobal_load_lds_dwordx4 %2, %1" POL "\n\tglobal_load_lds_dwordx4 %3, %1 offset:1024" POL \
    "\n\tglobal_load_lds_dwordx4 %4, %1 offset:2048" POL "\n\tglobal_load_lds_dwordx4 %5, %1 offset:3072" POL \
    "\n\ts_add_u32 m0, %0, 0x1000\n\ts_nop 0\n\tglobal_load_lds_dwordx4 %6, %1" POL \
    :: "s"(lds), "s"(base), "v"(v0), "v"(v1), "v"(v2), "v"(v3), "v"(v4) : "memory", "scc")
template <bool DEV> DI void glds4(unsigned lds, const char* base, unsigned v0, unsigned v1, unsigned v2, unsigned v3) {
  if (DEV) GLDS4(" sc1"); else GLDS4("");
}
template <bool DEV> DI void glds5(unsigned lds, const char* base, unsigned v0, unsigned v1, unsigned v2, unsigned v3, unsigned v4) {
  if (DEV) GLDS5(" sc1"); else GLDS5("");
}
template <int N> DI void wait_v() { asm volatile("s_waitcnt vmcnt(%0)" :: "i"(N) : "memory"); }
#define WAIT_L0() do { __builtin_amdgcn_s_waitcnt(0xC07F); asm volatile("" ::: "memory"); } while (0)
#define BAR() do { asm volatile("" ::: "memory"); __builtin_amdgcn_s_barrier(); asm volatile("" ::: "memory"); } while (0)
#define SGB(m, n) __builtin_amdgcn_sched_group_barrier(m, n, 0)
#define PIN() __builtin_amdgcn_sched_barrier(0)
DI void mf(f32x4& c, const bf16x8& a, const bf16x8& b, bool first) {
  if (first) asm volatile("v_mfma_f32_16x16x32_bf16 %0, %1, %2, 0" : "=&v"(c) : "v"(a), "v"(b));
  else asm volatile("v_mfma_f32_16x16x32_bf16 %0, %1, %2, %0" : "+v"(c) : "v"(a), "v"(b));
}
#define MFMA_DONE() asm volatile("s_nop 15")
template <int A, int B> DI void after_mfma(f32x4 (&c)[A][B]) {
#pragma unroll
  for (int i = 0; i < A; ++i)
#pragma unroll
    for (int j = 0; j < B; ++j) asm volatile("" : "+v"(c[i][j]));
}
#define STAMP_AT(i)

template <int CTRL> DI float dpp_add(float v) { return v + __builtin_bit_cast(float, __builtin_amdgcn_update_dpp(0, __builtin_bit_cast(int, v), CTRL, 0xf, 0xf, true)); }
DI float sum8(float v) { v = dpp_add<0xB1>(v); v = dpp_add<0x4E>(v); return dpp_add<0x141>(v); }
DI float sum16(float v) { return dpp_add<0x140>(sum8(v)); }
DI float xchg16(float keep_lo, float keep_hi) {
  const auto r = __builtin_amdgcn_permlane16_swap(__float_as_uint(keep_lo), __float_as_uint(keep_hi), false, false);
  return __uint_as_float(r[0]) + __uint_as_float(r[1]);
}
DI float xchg32(float keep_lo, float keep_hi) {
  const auto r = __builtin_amdgcn_permlane32_swap(__float_as_uint(keep_lo), __float_as_uint(keep_hi), false, false);
  return __uint_as_float(r[0]) + __uint_as_float(r[1]);
}
#define TIC(v)
#define ACC(a, x, y)
#define PUT(slot, a)
DI u64 ld_flag(const u64* p) { return __hip_atomic_load((const GAS u64*)p, __ATOMIC_RELAXED, __HIP_MEMORY_SCOPE_AGENT); }
DI void st_flag(u64* p, u64 v) { __hip_atomic_store((GAS u64*)p, v, __ATOMIC_RELAXED, __HIP_MEMORY_SCOPE_AGENT); }
DI void wait_flags(const u64* p, u64 tag) {
#pragma unroll 1
  for (int it = 0; it < 60000; ++it) {
    if (__all(ld_flag(p) == tag)) break;
    __builtin_amdgcn_s_sleep(4);
  }
  asm volatile("" ::: "memory");
}
DI void wait_flags2(const u64* p, const u64* p2, u64 tag) {
#pragma unroll 1
  for (int it = 0; it < 60000; ++it) {
    const u64 a = ld_flag(p), b = ld_flag(p2);
    if (__all(a == tag && b == tag)) break;
    __builtin_amdgcn_s_sleep(4);
  }
  asm volatile("" ::: "memory");
}
DI u64 read_tag(const char* ws) {
  u64 e;
  asm volatile("s_load_dwordx2 %0, %1, 0x0\n\ts_waitcnt lgkmcnt(0)" : "=s"(e) : "s"(ws + WS_CTRL) : "memory");
  return e * 6364136223846793005ull + 1442695040888963407ull;
}
DI void st16c(__amdgpu_buffer_rsrc_t r, unsigned off, u32x4 v) { __builtin_amdgcn_raw_buffer_store_b128(v, r, off, 0, 16); }
DI u32x4 ld16c(__amdgpu_buffer_rsrc_t r, unsigned off) { return __builtin_amdgcn_raw_buffer_load_b128(r, off, 0, 16); }

struct Params {
  const bf16_t *x, *wsr, *wsi, *wdr, *wdi, *wg, *wu, *wdn;
  bf16_t* out; char* ws;
};

constexpr int L_W = 49152, L_D = 98304, K1_TAB = 159744, LDS_BYTES = K1_TAB + 4096;
constexpr int T_RS = K1_TAB, T_RM = K1_TAB + 512, T_CS = K1_TAB + 1024, T_CD = K1_TAB + 2048, T_GO = K1_TAB + 3584, T_PF = K1_TAB + 3072;
constexpr int A_ST = 16384, B_ST = 20480, L_B = 4 * A_ST;

constexpr int NY = 16, NK2 = 28;
constexpr int F_Y = 147456, F_T = 0, FPITCH = 176;

template <bool TG>
DI void helper_p1(const Params& P, LAS char* smem, int lane, int lw, int mt, int ns, int split, int mt2, int nt, u64& tag, u64& fabv) {
  constexpr int NL = TG ? 12 : 13;
  const int n8 = ns & 7, hcol0 = TG ? 128 * n8 : 1024 + 64 * n8;
  const int rr = lane >> 3, c8 = lane & 7;
  const unsigned lds0 = (unsigned)(size_t)smem;
  unsigned vX[4], vW[4], vD[5];
#pragma unroll
  for (int j = 0; j < 4; ++j) {
    vX[j] = (32 * lw + 8 * j + rr) * 1280 + ((c8 ^ (TG ? rr : ((rr & 2) | ((j & 2) << 1)))) * 16) - j * 1024;
    vW[j] = (8 * j + rr) * 3072 + ((c8 ^ ((rr & 2) | ((j & 1) << 2))) * 16) - j * 1024;
  }
#pragma unroll
  for (int j = 0; j < 5; ++j) vD[j] = (40 * lw + 8 * j + rr) * 1280 + ((c8 ^ rr) * 16) - (j & 3) * 1024;
  const char* bx = (const char*)P.x + (size_t)mt * 128 * 1280;
  const char* bw = (const char*)((lw >> 1) ? P.wu : P.wg) + hcol0 * 2 + (TG ? (lw & 1) * 128 : (lw & 1) * (32 * 3072));
  const char* bd = (const char*)(n8 >= 4 ? P.wdi : P.wdr) + (size_t)(n8 & 3) * 160 * 1280;
  const unsigned ldsX = __builtin_amdgcn_readfirstlane(lds0 + lw * 4096);
  const unsigned ldsW = __builtin_amdgcn_readfirstlane(lds0 + L_W + (TG ? (lw & 1) * 49152 + (lw >> 1) * 8192 : lw * 4096));
  const unsigned ldsD = __builtin_amdgcn_readfirstlane(lds0 + L_D + lw * 5120);
  auto issue = [&](int kt, int buf) {
    glds4<false>(ldsX + buf * 16384, bx + kt * 128, vX[0], vX[1], vX[2], vX[3]);
    const char* pw = bw + kt * (64 * 3072);
    glds4<false>(ldsW + buf * 16384, pw, vW[0], vW[1], vW[2], vW[3]);
    if (TG) glds4<false>(ldsW + buf * 16384 + 4096, pw + 32 * 3072, vW[0], vW[1], vW[2], vW[3]);
    else glds5<false>(ldsD + buf * 20480, bd + kt * 128, vD[0], vD[1], vD[2], vD[3], vD[4]);
  };
  issue(0, 0); issue(1, 1);
  tag = read_tag(P.ws);
  const char* pfb = (const char*)(split < 2 ? P.wsr : P.wsi) + (size_t)(mt2 * 128 + nt * 32) * 4096 + (split & 1) * 2048;
  const unsigned pfv = (lane & 31) * 4096 + ((lw & 1) * 2 + (lane >> 5)) * 128;
  const u64* fs = FLAG(C_F0 + (lw == 0 ? mt * 64 + lane : (((lane >> 1) & 15) * 16 + ns) * 4 + 2 + (lane & 1)));
  u64 fsv = 0;
  int bn = 2;
#pragma unroll 1
  for (int k = 0; k < 8; ++k) {
    wait_v<NL>();
    BAR();
    if (lw < (TG ? 1 : 2)) {
      if (k == 4) asm volatile("global_load_dwordx2 %0, %1, off sc1" : "+v"(fsv) : "v"(fs) : "memory");
      if (k == 5) {
        asm volatile("" : "+v"(fsv));
        if (!__all(fsv == tag)) wait_flags(fs, tag);
        const char* src = P.ws + (lw == 0 ? WS_RS + mt * 1024 : WS_CD + n8 * 640);
        asm volatile("s_mov_b32 m0, %0\n\ts_nop 0\n\tglobal_load_lds_dwordx4 %1, %2 sc1" :: "s"(lds0 + (lw == 0 ? T_RS : T_CD)), "v"(lane * 16), "s"(src) : "memory");
      }
    }
    issue(k + 2, bn);
    bn = bn == 2 ? 0 : bn + 1;
    const int b = k >> 1;
    if (PFN && b < PFN && (k & 1) == (mt & 1) && lw >= 2)
      asm volatile("s_mov_b32 m0, %0\n\ts_nop 0\n\tglobal_load_lds_dword %1, %2" :: "s"(lds0 + T_PF), "v"(pfv), "s"(pfb + b * 512) : "memory");
  }
  wait_v<NL>();
  BAR();
  wait_v<0>();
  BAR();
  if (TG || lw < 2) {
    wait_flags(FLAG(C_F0 + ((lane & 15) * 16 + ns) * 4 + lw), tag);
    const __amdgpu_buffer_rsrc_t rw = __builtin_amdgcn_make_buffer_rsrc(P.ws, 0, WS_END, 0x00020000);
    f32x4 s = {0, 0, 0, 0};
#pragma unroll
    for (int i = 0; i < 4; ++i) s += __builtin_bit_cast(f32x4, ld16c(rw, WS_CS + ((ns * 4 + lw) * 16) * 256 + i * 1024 + lane * 16));
#pragma unroll
    for (int e = 0; e < 4; ++e) { s[e] = xchg16(s[e], s[e]); s[e] = xchg32(s[e], s[e]); }
    if (lane < 16) *(LAS f32x4*)(smem + T_CS + lw * 256 + lane * 16) = s;
  }
  if (!TG) __builtin_amdgcn_s_sleep(FABSL);
  fabv = ld_flag(FLAG(C_FAB + (nt * 64 + split * 16) * 2 + (lane & 31)));
  asm volatile("" : "+v"(fabv));
  if (lw == 0 && lane == 0) *(volatile LAS unsigned*)(smem + T_GO) = 0;
  WAIT_L0();
  BAR();
}

DI void helper_p2(const Params& P, LAS char* smem, int lane, int lw, int split, int mt, int nt, u64 fabv, u64 tag) {
  const unsigned lds0 = (unsigned)(size_t)smem;
  const int rr = lane >> 3;
  unsigned vY[4];
#pragma unroll
  for (int j = 0; j < 4; ++j) vY[j] = (32 * lw + 8 * j + rr) * 4096 + (((lane & 7) ^ rr) * 16) - j * 1024;
  const unsigned vL = lane * 16, vL4 = vL + 4096;
  const char* baseY = (const char*)(split < 2 ? P.wsr : P.wsi) + (size_t)mt * 128 * 4096 + (split & 1) * 2048;
  const char* baseAB = P.ws + WS_AB + (nt * 64 + split * 16) * B_ST + lw * 5120;
  const char* baseH = P.ws + WS_H + ((mt * 24 + (lw >> 1)) * 16 + (split & 1) * 8 + (lw & 1) * 4) * 1024;
  const char* baseWD = P.ws + WS_WD + ((nt * 48 + lw) * 10 + (split >> 1) * 5) * 1024;
  const int rot = ROT2 ? (nt & 1) * 8 : 0;
  const unsigned ldsA = __builtin_amdgcn_readfirstlane(lds0 + lw * 4096), ldsB = __builtin_amdgcn_readfirstlane(lds0 + L_B + lw * 5120);
  auto issueYA = [&](int k) { glds4<false>(ldsA + (k & 3) * A_ST, baseY + ((k + rot) & 15) * 128, vY[0], vY[1], vY[2], vY[3]); };
  auto issueYB = [&](int k) { glds5<true>(ldsB + (k & 3) * B_ST, baseAB + ((k + rot) & 15) * B_ST, vL, vL, vL, vL, vL4); };
  auto issueD = [&](int k) {
    glds4<true>(ldsA + (k & 3) * A_ST, baseH + (k - NY) * (2 * A_ST), vL, vL, vL, vL);
    glds5<true>(ldsB + (k & 3) * B_ST, baseWD + (k - NY) * (2 * B_ST), vL, vL, vL, vL, vL4);
  };
  STAMP_AT(8);
  issueYA(0); issueYA(1);
  const bool ab_seen = __all(fabv == tag);
  if (lw == 0) {
    if (!ab_seen) wait_flags(FLAG(C_FAB + (nt * 64 + split * 16) * 2 + (lane & 31)), tag);
    if (lane == 0) *(volatile LAS unsigned*)(smem + T_GO) = 1;
  } else if (!ab_seen) {
#pragma unroll 1
    for (int it = 0; it < 4000000; ++it) {
      if (*(volatile LAS unsigned*)(smem + T_GO) != 0) break;
      __builtin_amdgcn_s_sleep(1);
    }
  }
  asm volatile("" ::: "memory");
  STAMP_AT(9);
  const u64* fh = FLAG(C_FH + mt * 64 + (lane & 31) * 2 + (split & 1));
  const int l36 = min(lane, 35);
  const int wt = (nt * 240 + ((l36 / 3) * 4 + lw) * 5 + (split >> 1) * 2 + l36 % 3) >> 1;
  const u64* fw = FLAG(C_FH + (wt >> 5) * 64 + (wt & 31));
  u64 fhv = 0, fwv = 0;
  u64 a0 = 0, a1 = 0, a2 = 0;
  issueYB(0); issueYB(1);
  wait_v<5>(); BAR();
  issueYA(2); issueYB(2);
  wait_v<9>(); BAR();
  issueYA(3); issueYB(3); issueYA(4); issueYB(4);
#pragma unroll 1
  for (int k = 2; k < 13; ++k) {
    TIC(t0); wait_v<18>(); TIC(t1); BAR(); TIC(t2);
    if (k == 12) asm volatile("global_load_dwordx2 %0, %2, off sc1\n\tglobal_load_dwordx2 %1, %3, off sc1" : "+v"(fhv), "+v"(fwv) : "v"(fh), "v"(fw) : "memory");
    issueYA(k + 3); issueYB(k + 3);
    TIC(t3);
    ACC(a0, t0, t1); ACC(a1, t1, t2); ACC(a2, t2, t3);
  }
  PUT(3, a0); PUT(4, a1); PUT(5, a2);
#pragma unroll 1
  for (int k = 13; k < NK2 - 3; ++k) {
    if (k == 13) wait_v<9>(); else wait_v<18>();
    BAR();
    if (k == 13) {
      asm volatile("" : "+v"(fhv), "+v"(fwv));
      if (!__all(fhv == tag && fwv == tag)) wait_flags2(fh, fw, tag);
    }
    issueD(k + 3);
  }
  wait_v<18>(); BAR();
  wait_v<9>(); BAR();
  wait_v<0>(); BAR();
  STAMP_AT(10);
  if (blockIdx.x == 0 && lw == 0) {
#pragma unroll 1
    for (int i = 0; i < 4; ++i) wait_flags(FLAG(C_F0 + (i * 64 + lane) * 4), tag);
    if (lane == 0) st_flag(FLAG(C_E), tag);
  }
}

struct Carry { u32x4 v[12]; };

template <bool TG>
DI void mfma_p1(const Params& P, LAS char* smem, int lane, int w, int mt, int ns, u64& tag, Carry& cy) {
  const int n8 = ns & 7, hcol0 = TG ? 128 * n8 : 1024 + 64 * n8;
  const __amdgpu_buffer_rsrc_t rw = __builtin_amdgcn_make_buffer_rsrc(P.ws, 0, WS_END, 0x00020000);
  const int row = mt * 128 + ns * 8 + 2 * w + (lane >> 5);
  const int rr = lane >> 3, c8 = lane & 7;
  const bool colw = TG || w < 2;
  u32x2 vx[5]; u32x4 vw[10];
  {
    const char* px = (const char*)P.x + (size_t)row * 1280 + (lane & 31) * 8;
#pragma unroll
    for (int i = 0; i < 5; ++i) vx[i] = *(const u32x2*)(px + i * 256);
    if (colw) {
      const bf16_t* wsel = (TG ? (w >> 1) : w) ? P.wu : P.wg;
      const char* pw = (const char*)wsel + (size_t)(40 * mt + rr) * 3072 + (hcol0 + (TG ? (w & 1) * 64 : 0)) * 2 + c8 * 16;
#pragma unroll
      for (int i = 0; i < 5; ++i) vw[i] = *(const u32x4*)(pw + i * (8 * 3072));
    } else {
      const int e = (n8 & 3) * 160 + 10 * mt + 5 * (w - 2) + min(rr, 4);
      const char* pd = (const char*)(n8 >= 4 ? P.wdi : P.wdr) + (size_t)e * 1280 + c8 * 16;
#pragma unroll
      for (int i = 0; i < 10; ++i) vw[i] = *(const u32x4*)(pd + i * 128);
    }
  }
  tag = read_tag(P.ws);
  const int wm = w & 1, wn = w >> 1;
  int q = lane >> 4, rho = lane & 15;
  constexpr int NC = TG ? 4 : 2;
  unsigned vX0 = TG ? wm * 8192 + rho * 128 + ((q ^ (rho & 7)) * 16)
                          : wm * 8192 + (8 * (rho >> 2) + (rho & 3)) * 128 + ((q ^ ((rho & 2) | ((rho >> 1) & 4))) * 16);
  unsigned vX1 = vX0 ^ 64;
  unsigned vW[NC];
  {
    const int qq = rho >> 2, p = lane & 3, swz = (qq & 2) | ((q & 1) << 2);
#pragma unroll
    for (int c = 0; c < NC; ++c) {
      const int cc = TG ? c : (2 * wn + c);
      vW[c] = L_W + (TG ? wn * 49152 : 0) + (8 * q + qq) * 128 + ((((2 * cc) ^ swz) + (p >> 1)) * 16) + 8 * (p & 1);
    }
  }
  unsigned vD0 = L_D + (80 * wn + rho) * 128 + ((q ^ (rho & 7)) * 16), vD1 = vD0 ^ 64;
  asm volatile("" : "+v"(vX0), "+v"(vX1));
  if (!TG) asm volatile("" : "+v"(vD0), "+v"(vD1));
#pragma unroll
  for (int c = 0; c < NC; ++c) asm volatile("" : "+v"(vW[c]));
  f32x4 aG[NC][4], aU[NC][4], aA[4][5];
  {
    unsigned ONE2 = 0x3f803f80u, SLO = 0x00003f80u, SHI = 0x3f800000u;
    asm volatile("" : "+v"(ONE2), "+v"(SLO), "+v"(SHI));
    float sx = 0, sq = 0;
#pragma unroll
    for (int i = 0; i < 5; ++i)
#pragma unroll
      for (int e = 0; e < 2; ++e) { sx = dot2(vx[i][e], ONE2, sx); sq = dot2(vx[i][e], vx[i][e], sq); }
    sx = sum16(sx); sq = sum16(sq);
    sx = xchg16(sx, sx); sq = xchg16(sq, sq);
    const float mean = sx * (1.0f / D);
    const float var = fmaxf(sq * (1.0f / D) - mean * mean, 0.f);
    const float sd = rbf(__builtin_amdgcn_sqrtf(rbf(rbf(var) + 1e-5f)));
    const float rso = (lane & 1) ? rbf(mean) : __builtin_amdgcn_rcpf(sd);
    float cres; unsigned coff;
    if (colw) {
      float c[8] = {0, 0, 0, 0, 0, 0, 0, 0};
#pragma unroll
      for (int i = 0; i < 5; ++i)
#pragma unroll
        for (int e = 0; e < 4; ++e) { c[2 * e] = dot2(vw[i][e], SLO, c[2 * e]); c[2 * e + 1] = dot2(vw[i][e], SHI, c[2 * e + 1]); }
      float d4[4], d2[2];
#pragma unroll
      for (int e = 0; e < 4; ++e) d4[e] = xchg32(c[e], c[4 + e]);
#pragma unroll
      for (int e = 0; e < 2; ++e) d2[e] = xchg16(d4[e], d4[2 + e]);
      d2[0] = dpp_add<0x128>(d2[0]); d2[1] = dpp_add<0x128>(d2[1]);
      cres = (lane & 8) ? d2[1] : d2[0];
      coff = WS_CS + (((ns * 4 + w) * 16 + mt) * 64 + 8 * c8 + rr) * 4;
    } else {
      float c0 = 0, c1 = 0;
#pragma unroll
      for (int i = 0; i < 10; ++i) { c0 = dot2(vw[i][0], ONE2, c0); c1 = dot2(vw[i][1], ONE2, c1); c0 = dot2(vw[i][2], ONE2, c0); c1 = dot2(vw[i][3], ONE2, c1); }
      cres = sum8(c0 + c1);
      coff = (c8 == 0 && rr < 5) ? WS_CD + (n8 * 160 + 10 * mt + 5 * (w - 2) + rr) * 4 : 0xfffffff0u;
    }
    asm volatile("" : "+v"(cres));
    __builtin_amdgcn_raw_buffer_store_b32(__float_as_uint(cres), rw, coff, 0, 16);
    __builtin_amdgcn_raw_buffer_store_b32(__float_as_uint(rso), rw, (lane & 30) == 0 ? WS_RS + mt * 1024 + (lane & 1) * 512 + (row & 127) * 4 : 0xfffffff0u, 0, 16);
  }
  bf16x8 FX[2][4], FD[2][5];
  s16x4 FW[2][2 * NC][2];
  constexpr int NR = TG ? 20 : 17, NM = TG ? 32 : 36;
  auto rd1 = [&](int s, int buf, int ks, int r) {
    if (r >= 2 && r < 6) {
      const int i = r - 2;
      FX[s][i] = *(LAS bf16x8*)(smem + (ks ? vX1 : vX0) + buf * 16384 + (TG ? i * 2048 : (i >> 1) * 4096 + (i & 1) * 512));
    } else if (r < 4 + 4 * NC) {
      const int h = r < 2 ? r : r - 4, f = h >> 1;
      FW[s][f][h & 1] = __builtin_amdgcn_ds_read_tr16_b64_v4i16((LAS s16x4*)(smem + vW[f >> 1] + buf * 16384 + ks * 4096 + (f & 1) * 8192 + (h & 1) * 512));
    } else {
      const int jj = r - (4 + 4 * NC);
      FD[s][jj] = *(LAS bf16x8*)(smem + (ks ? vD1 : vD0) + buf * 20480 + jj * 2048);
    }
  };
  auto mm1 = [&](int s, int m, bool first) {
    const int f = m >> 2, i = m & 3;
    if (f < 2 * NC) {
      const bf16x8 wf = __builtin_shufflevector(FW[s][f][0], FW[s][f][1], 0, 1, 2, 3, 4, 5, 6, 7);
      mf((f & 1) ? aU[f >> 1][i] : aG[f >> 1][i], wf, FX[s][i], first);
    } else mf(aA[i][f - 2 * NC], FX[s][i], FD[s][f - 2 * NC], first);
  };
  auto seg = [&](int s, bool first, int m0, int m1, int rk, int buf, int ks) {
    int r = 0;
#pragma unroll
    for (int m = m0; m < m1; ++m) {
      mm1(s, m, first);
      if (rk == 2 && r == 0) r = 2;
      if (rk == 2 && r == 6) r = 4 + 4 * NC;
      const int rend = rk == 0 ? 0 : rk == 3 ? 4 + 4 * NC : NR;
      if (r < rend && (TG ? (m % 5 != 4) : (rk == 3 || m % 3 != 2))) { rd1(s ^ 1, buf, ks, r); ++r; }
      PIN();
    }
  };
  BAR();
  STAMP_AT(1);
#pragma unroll
  for (int r = 0; r < NR; ++r) rd1(0, 0, 0, r);
  PIN();
  constexpr int KJ = TG ? 10 : 7;
#pragma unroll
  for (int k = 0; k < 10; ++k) {
    const int m0 = k < KJ ? 0 : 8 * NC;
    seg(0, k == 0, m0, NM, k < KJ ? 1 : 2, k % 3, 1);
    if (k == 1) {
      wait_v<0>();
      if (lane == 0) st_flag(FLAG(C_F0 + (mt * 16 + ns) * 4 + w), tag);
    }
    if (k < 9) {
      WAIT_L0();
      BAR();
      PIN();
    }
    if (k < 9) seg(1, false, m0, NM, k + 1 < KJ ? 1 : 2, (k + 1) % 3, 0);
    else seg(1, false, m0, NM, TG ? 0 : 3, KJ % 3, 0);
    WAIT_L0(); PIN();
  }
  if (!TG) {
    MFMA_DONE();
    after_mfma(aA);
    asm volatile("" : "+v"(lane)); q = lane >> 4; rho = lane & 15;
    STAMP_AT(4);
    const float sgn = (n8 >= 4) ? -1.0f : 1.0f;
    float cs[5];
#pragma unroll
    for (int j = 0; j < 5; ++j) cs[j] = *(LAS float*)(smem + T_CD + (80 * wn + 16 * j + rho) * 4);
    const int kt = (n8 >= 4 ? 32 : 0) + mt * 2 + wm;
#pragma unroll
    for (int I = 0; I < 2; ++I) {
      f32x4 r4[2], m4[2];
#pragma unroll
      for (int t = 0; t < 2; ++t) {
        const int row = 64 * wm + 32 * I + 8 * q + 4 * t;
        r4[t] = *(LAS f32x4*)(smem + T_RS + row * 4) * sgn; m4[t] = *(LAS f32x4*)(smem + T_RM + row * 4);
      }
      const unsigned dst = WS_AB + ((((n8 & 3) * 64 + kt) * 2 + I) * 10 + 5 * wn) * 1024 + lane * 16;
#pragma unroll
      for (int j = 0; j < 5; ++j) {
        const f32x4 v0 = (aA[2 * I][j] - m4[0] * cs[j]) * r4[0], v1 = (aA[2 * I + 1][j] - m4[1] * cs[j]) * r4[1];
        const u32x4 o = {pk_bf16(v0[0], v0[1]), pk_bf16(v0[2], v0[3]), pk_bf16(v1[0], v1[1]), pk_bf16(v1[2], v1[3])};
        st16c(rw, dst + j * 1024, o);
      }
    }
    STAMP_AT(7);
    WAIT_L0(); PIN();
#pragma unroll
    for (int k = KJ; k < 10; ++k) {
      seg(0, false, 0, 8 * NC, 3, k % 3, 1);
      WAIT_L0(); PIN();
      if (k == KJ + AFL) {
        wait_v<0>();
        if (lane == 0) st_flag(FLAG(C_FAB + ((n8 & 3) * 64 + kt) * 2 + wn), tag);
        PIN();
      }
      seg(1, false, 0, 8 * NC, k < 9 ? 3 : 0, (k + 1) % 3, 0);
      WAIT_L0(); PIN();
    }
  }
  MFMA_DONE();
  after_mfma(aG); after_mfma(aU);
  asm volatile("" : "+v"(lane)); q = lane >> 4; rho = lane & 15;
  unsigned wv[2][8];
  if (TG) {
#pragma unroll
    for (int u = 0; u < 2; ++u) {
      const int task = min(((mt * 8 + ns) * 4 + w) * 2 + u, 959);
      const unsigned* src = (const unsigned*)(P.wdn + (size_t)(((task % 240) / 5) * 32 + 4 * q) * D + (task / 240) * 160 + (task % 5) * 32 + 2 * rho);
#pragma unroll
      for (int e = 0; e < 8; ++e) wv[u][e] = src[((e & 3) + (e >> 2) * 16) * (D / 2)];
    }
  }
  BAR();
  STAMP_AT(2);
  {
    const float K0 = -2.0f * 0.7978845608028654f * 1.4426950408889634f, K1 = K0 * 0.044715f;
    float mu[4], c0[4], c1[4], v2[4];
#pragma unroll
    for (int i = 0; i < 4; ++i) {
      const int row = TG ? 64 * wm + 16 * i + rho : 64 * wm + 32 * (i >> 1) + 8 * (rho >> 2) + 4 * (i & 1) + (rho & 3);
      const float rs = *(LAS float*)(smem + T_RS + row * 4);
      mu[i] = *(LAS float*)(smem + T_RM + row * 4);
      c0[i] = K0 * rs; c1[i] = K1 * rs * rs * rs; v2[i] = __builtin_amdgcn_rcpf(rs * rs);
    }
#pragma unroll
    for (int J = 0; J < NC / 2; ++J) {
      f32x4 cg[2], cu[2];
#pragma unroll
      for (int t = 0; t < 2; ++t) {
        const int c = 2 * J + t;
        const int o = TG ? (wn * 256 + c * 64 + q * 16) : ((2 * wn + c) * 64 + q * 16);
        cg[t] = *(LAS f32x4*)(smem + T_CS + o);
        cu[t] = *(LAS f32x4*)(smem + T_CS + (TG ? 512 : 256) + o);
      }
#pragma unroll
      for (int i = 0; i < 4; ++i) {
        u32x4 o;
#pragma unroll
        for (int t = 0; t < 2; ++t) {
          const f32x4 g = aG[2 * J + t][i] - cg[t] * mu[i];
          const f32x4 u = aU[2 * J + t][i] - cu[t] * mu[i];
          const f32x4 z = g * (g * g * c1[i] + c0[i]);
          f32x4 e;
#pragma unroll
          for (int r = 0; r < 4; ++r) e[r] = __builtin_amdgcn_exp2f(z[r]);
          e = e * v2[i] + v2[i];
#pragma unroll
          for (int r = 0; r < 4; ++r) e[r] = __builtin_amdgcn_rcpf(e[r]);
          const f32x4 hv = g * u * e;
          o[2 * t] = pk_bf16(hv[0], hv[1]); o[2 * t + 1] = pk_bf16(hv[2], hv[3]);
        }
        cy.v[J * 4 + i] = o;
        asm volatile("" : "+v"(cy.v[J * 4 + i]));
      }
    }
  }
  if (TG) {
    const bool up = rho & 8;
#pragma unroll
    for (int u = 0; u < 2; ++u)
#pragma unroll
      for (int e = 0; e < 4; ++e) {
        const unsigned lo = __builtin_amdgcn_perm(wv[u][2 * e + 1], wv[u][2 * e], 0x05040100u), hi = __builtin_amdgcn_perm(wv[u][2 * e + 1], wv[u][2 * e], 0x07060302u);
        const unsigned t = __builtin_amdgcn_update_dpp(0, up ? lo : hi, 0x128, 0xf, 0xf, true);
        cy.v[8 + 2 * u][e] = up ? t : lo;
        cy.v[9 + 2 * u][e] = up ? hi : t;
      }
  } else {
#pragma unroll
    for (int n = 4; n < 12; ++n) cy.v[n] = (u32x4){0, 0, 0, 0};
  }
  STAMP_AT(3);
}

DI void mfma_p2(const Params& P, LAS char* smem, int lane, int w, int split, int mt, int nt, int mt1, int ns, const Carry& cy, u64 tag) {
  asm volatile("" : "+v"(lane));
  const int wm = w & 1, wn = w >> 1, q = lane >> 4, r15 = lane & 15;
  unsigned vY0 = wm * 8192 + r15 * 128 + ((q ^ (r15 & 7)) * 16), vY1 = vY0 ^ 64;
  unsigned vB0 = L_B + wn * 5120 + lane * 16, vB2 = vB0 + 2 * B_ST;
  unsigned vH = w * 2048 + lane * 16, vW0 = L_B + lane * 16, vW2 = vW0 + 2 * B_ST;
  asm volatile("" : "+v"(vY0), "+v"(vY1), "+v"(vB0), "+v"(vB2), "+v"(vH), "+v"(vW0), "+v"(vW2));
  const __amdgpu_buffer_rsrc_t rw = __builtin_amdgcn_make_buffer_rsrc(P.ws, 0, WS_END, 0x00020000);
  const int tile = mt * 4 + nt;
  f32x4 acc[4][5], dac[1][5];
  bf16x8 FA[2][4], FB[2][5], GA[2][2], GB[2][10];
  auto rdy = [&](int s, int k, int ks, int r) {
    const int buf = k & 3;
    if (r >= 1 && r < 5) FA[s][r - 1] = *(LAS bf16x8*)(smem + (ks ? vY1 : vY0) + buf * A_ST + (r - 1) * 2048);
    else { const int j = r ? r - 4 : 0; FB[s][j] = *(LAS bf16x8*)(smem + (buf >= 2 ? vB2 : vB0) + (buf & 1) * B_ST + ks * 10240 + j * 1024); }
  };
  auto rdd = [&](int s, int k, int hf, int r) {
    const int buf = k & 3, u = r / 6, j = r % 6;
    if (j == 0) GA[s][u] = *(LAS bf16x8*)(smem + vH + buf * A_ST + hf * 8192 + u * 1024);
    else GB[s][u * 5 + j - 1] = *(LAS bf16x8*)(smem + (buf >= 2 ? vW2 : vW0) + (buf & 1) * B_ST + hf * 10240 + u * 5120 + (j - 1) * 1024);
  };
  auto segy = [&](int s, bool first, int k, int ks) {
    int r = 0;
#pragma unroll
    for (int m = 0; m < 20; ++m) {
      mf(acc[m & 3][m >> 2], FB[s][m >> 2], FA[s][m & 3], first);
      if (k < NY) { if (r < 9 && m % 3 != 2) { rdy(s ^ 1, k, ks, r); ++r; } }
      else if (r < 12) { rdd(s ^ 1, k, ks, r); ++r; }
      PIN();
    }
  };
  auto segd = [&](int s, bool first, bool reads, int k, int hf) {
#pragma unroll
    for (int m = 0; m < 10; ++m) {
      mf(dac[0][m % 5], GB[s][m], GA[s][m / 5], first && m < 5);
      if (reads && m < 6) { rdd(s ^ 1, k, hf, 2 * m); rdd(s ^ 1, k, hf, 2 * m + 1); }
      PIN();
    }
  };
  const bool tg = ns < 8;
  const int f0 = tg ? 128 * ns + wn * 64 : 1024 + 64 * (ns & 7) + wn * 32;
  const unsigned hb = WS_H + (((mt1 * 24 + (f0 >> 6)) * 8 + 4 * wm) * 2 + ((f0 >> 5) & 1)) * 1024;
  const unsigned hv = tg ? lane * 16 : (r15 >> 3) * 2048 + (8 * ((r15 >> 2) & 1) + (r15 & 3) + 16 * q) * 16;
  const unsigned wdv = (2 * (r15 & 7) + (r15 >> 3) + 16 * q) * 16;
  const int wt = (mt1 * 8 + ns) * 4 + w;
  auto late_store = [&](int n) {
    if (n < 4) __builtin_amdgcn_raw_buffer_store_b128(cy.v[n], rw, hv, hb + (tg ? n * 2048 : (n >> 1) * 4096 + (n & 1) * 64), 16);
    else if (n < 8) { if (tg) __builtin_amdgcn_raw_buffer_store_b128(cy.v[n], rw, hv, hb + (n - 4) * 2048 + 1024, 16); }
    else if (tg && wt < 480) {
      const int task = wt * 2 + ((n - 8) >> 1);
      __builtin_amdgcn_raw_buffer_store_b128(cy.v[n], rw, wdv, WS_WD + (((task / 240) * 48 + (task % 240) / 5) * 10 + (task % 5) * 2 + (n & 1)) * 1024, 16);
    }
  };
  BAR();
  STAMP_AT(5);
#pragma unroll
  for (int r = 0; r < 9; ++r) rdy(0, 0, 0, r);
  PIN();
#pragma unroll
  for (int k = 0; k < NY; ++k) {
    segy(0, k == 0, k, 1);
    if (k >= TK0 && k < TK0 + 12 / TPS) {
#pragma unroll
      for (int n = 0; n < TPS; ++n) late_store((k - TK0) * TPS + n);
    }
    if (k == TK0 + 12 / TPS + 1) {
      wait_v<0>();
      if (lane == 0) st_flag(FLAG(C_FH + mt1 * 64 + ns * 4 + w), tag);
    }
    WAIT_L0();
    BAR();
    PIN();
    segy(1, false, k + 1, 0);
    WAIT_L0(); PIN();
  }
  MFMA_DONE();
  after_mfma(acc);
#pragma unroll
  for (int i = 0; i < 4; ++i) {
    unsigned pk[10];
#pragma unroll
    for (int j = 0; j < 5; ++j) { pk[2 * j] = pk_bf16(acc[i][j][0], acc[i][j][1]); pk[2 * j + 1] = pk_bf16(acc[i][j][2], acc[i][j][3]); }
    if (w == split) {
      LAS char* dst = smem + F_Y + i * 2560;
      *(LAS u32x4*)(dst + lane * 16) = (u32x4){pk[0], pk[1], pk[2], pk[3]};
      *(LAS u32x4*)(dst + 1024 + lane * 16) = (u32x4){pk[4], pk[5], pk[6], pk[7]};
      *(LAS u32x2*)(dst + 2048 + lane * 8) = (u32x2){pk[8], pk[9]};
    } else {
      const unsigned dst = WS_P + (((tile * 4 + w) * 3 + (split ^ w) - 1) * 4 + i) * 2560;
      st16c(rw, dst + lane * 16, (u32x4){pk[0], pk[1], pk[2], pk[3]});
      st16c(rw, dst + 1024 + lane * 16, (u32x4){pk[4], pk[5], pk[6], pk[7]});
      __builtin_amdgcn_raw_buffer_store_b64((u32x2){pk[8], pk[9]}, rw, dst + 2048 + lane * 8, 0, 16);
    }
  }
  PIN();
  const int orow = mt * 128 + (split & 1) * 64 + w * 16, ocol = nt * 160 + (split >> 1) * 80;
  const u64* fp = FLAG(C_FP + (tile * 4 + split) * 4 + min(lane, 2));
  u64 fpv = 0;
  u32x4 xv[3], pa[3][2]; u32x2 pb[3];
#pragma unroll
  for (int k = NY; k < NK2; ++k) {
    segd(0, k == NY, true, k, 1);
    if (k == NY + YFL && w != split) {
      wait_v<0>();
      if (lane == 0) st_flag(FLAG(C_FP + (tile * 4 + w) * 4 + (split ^ w) - 1), tag);
    }
    if (k == NY + YPL) fpv = ld_flag(fp);
    if (k == NY + YPL + 1) {
      if (!__all(fpv == tag)) wait_flags(fp, tag);
      asm volatile("" ::: "memory");
#pragma unroll
      for (int s = 0; s < 3; ++s) {
        const unsigned src = WS_P + (((tile * 4 + split) * 3 + s) * 4 + w) * 2560;
        pa[s][0] = ld16c(rw, src + lane * 16); pa[s][1] = ld16c(rw, src + 1024 + lane * 16);
        pb[s] = __builtin_amdgcn_raw_buffer_load_b64(rw, src + 2048 + lane * 8, 0, 16);
      }
#pragma unroll
      for (int i = 0; i < 3; ++i) {
        const int n = min(i * 64 + lane, 159);
        xv[i] = *(const u32x4*)(P.x + (size_t)(orow + n / 10) * D + ocol + (n % 10) * 8);
      }
    }
    if (k + 1 < NK2) {
      WAIT_L0();
      BAR();
      PIN();
    }
    segd(1, false, k + 1 < NK2, k + 1, 0);
    WAIT_L0(); PIN();
  }
  MFMA_DONE();
  after_mfma(dac);
  STAMP_AT(6);
  LAS char* po = smem + F_Y + w * 2560;
  const u32x4 oa0 = *(LAS u32x4*)(po + lane * 16), oa1 = *(LAS u32x4*)(po + 1024 + lane * 16);
  const u32x2 ob = *(LAS u32x2*)(po + 2048 + lane * 8);
  LAS char* pt = smem + F_T + w * (16 * FPITCH);
#pragma unroll
  for (int j = 0; j < 5; ++j) {
    const unsigned l0 = j < 2 ? oa0[(j & 1) * 2] : j < 4 ? oa1[(j & 1) * 2] : ob[0], h0 = j < 2 ? oa0[(j & 1) * 2 + 1] : j < 4 ? oa1[(j & 1) * 2 + 1] : ob[1];
    f32x4 y = {bflo(l0), bfhi(l0), bflo(h0), bfhi(h0)};
#pragma unroll
    for (int s = 0; s < 3; ++s) {
      const unsigned lo = j < 4 ? pa[s][j >> 1][(j & 1) * 2] : pb[s][0], hi = j < 4 ? pa[s][j >> 1][(j & 1) * 2 + 1] : pb[s][1];
      y += (f32x4){bflo(lo), bfhi(lo), bflo(hi), bfhi(hi)};
    }
    const f32x4 d = dac[0][j];
    f32x4 e = d * -1.4426950408889634f;
#pragma unroll
    for (int r = 0; r < 4; ++r) e[r] = __builtin_amdgcn_exp2f(e[r]);
    e = e + 1.0f;
#pragma unroll
    for (int r = 0; r < 4; ++r) e[r] = __builtin_amdgcn_rcpf(e[r]);
    e = e * d * y;
    *(LAS u32x2*)(pt + r15 * FPITCH + j * 32 + q * 8) = (u32x2){pk_bf16(e[0], e[1]), pk_bf16(e[2], e[3])};
  }
  WAIT_L0();
#pragma unroll
  for (int i = 0; i < 3; ++i) {
    const int n = i * 64 + lane, nc = min(n, 159);
    const u32x4 v = *(LAS u32x4*)(pt + (nc / 10) * FPITCH + (nc % 10) * 16);
    u32x4 o;
#pragma unroll
    for (int d = 0; d < 4; ++d) o[d] = pk_bf16(bflo(v[d]) + bflo(xv[i][d]), bfhi(v[d]) + bfhi(xv[i][d]));
    if (n < 160) *(u32x4*)(P.out + (size_t)(orow + nc / 10) * D + ocol + (nc % 10) * 8) = o;
  }
}

__global__ __launch_bounds__(512, 2)
void fused(Params P) {
  extern __shared__ __attribute__((aligned(16))) char smem_[];
  LAS char* smem = (LAS char*)smem_;
  const int tid = threadIdx.x, lane = tid & 63, w = __builtin_amdgcn_readfirstlane(tid >> 6);
  const int bid = blockIdx.x, xcd = bid & 7, idx = bid >> 3;
  u64 tag;
  STAMP_AT(0);
  const int mt1 = (xcd & 1) * 8 + (idx & 7), j = idx >> 3, ns = (j & 1) * 8 + (xcd >> 1) * 2 + (j >> 1);
  const int split = xcd >> 1, mt2 = (xcd & 1) * 8 + (idx >> 2), nt = idx & 3;
  if (w >= 4) {
    __builtin_amdgcn_s_setprio(HPRIO);
    u64 fabv;
    if (j & 1) helper_p1<false>(P, smem, lane, w & 3, mt1, ns, split, mt2, nt, tag, fabv); else helper_p1<true>(P, smem, lane, w & 3, mt1, ns, split, mt2, nt, tag, fabv);
    helper_p2(P, smem, lane, w & 3, split, mt2, nt, fabv, tag);
  } else {
    Carry cy;
    if (j & 1) mfma_p1<false>(P, smem, lane, w, mt1, ns, tag, cy); else mfma_p1<true>(P, smem, lane, w, mt1, ns, tag, cy);
    mfma_p2(P, smem, lane, w, split, mt2, nt, mt1, ns, cy, tag);
    __builtin_amdgcn_s_waitcnt(0);
  }
}

extern "C" void kernel_launch(void* const* d_in, const int* in_sizes, int n_in, void* d_out, int out_size, void* d_ws, size_t ws_size, hipStream_t stream) {
  Params P;
  P.x = (const bf16_t*)d_in[0]; P.wsr = (const bf16_t*)d_in[1]; P.wsi = (const bf16_t*)d_in[2];
  P.wdr = (const bf16_t*)d_in[3]; P.wdi = (const bf16_t*)d_in[4];
  P.wg = (const bf16_t*)d_in[6]; P.wu = (const bf16_t*)d_in[7]; P.wdn = (const bf16_t*)d_in[8];
  P.out = (bf16_t*)d_out; P.ws = (char*)d_ws;
  (void)hipFuncSetAttribute((const void*)fused, hipFuncAttributeMaxDynamicSharedMemorySize, LDS_BYTES);
  fused<<<256, 512, LDS_BYTES, stream>>>(P);
}
```

```cpp
#include <hip/hip_runtime.h>
#include <hip/hip_bf16.h>
typedef unsigned short bf16_t;
typedef unsigned long long u64;
typedef short bf16x8 __attribute__((ext_vector_type(8)));
typedef short s16x4 __attribute__((ext_vector_type(4)));
typedef float f32x4 __attribute__((ext_vector_type(4)));
typedef float f32x2 __attribute__((ext_vector_type(2)));
typedef unsigned u32x4 __attribute__((ext_vector_type(4)));
typedef unsigned u32x2 __attribute__((ext_vector_type(2)));
typedef __bf16 bf16x2_t __attribute__((ext_vector_type(2)));
#define LAS __attribute__((address_space(3)))
#define GAS __attribute__((address_space(1)))
#define DI __device__ __forceinline__

#define STAMP 1
#define NPRE 2
#define ROT2 0
#define YFL 4
#define FABSL 20
#define AFL 2
#define YPL 8
#define XROT 0
#define HPRIO 0
#define PFN 4

constexpr int S = 2048, D = 640, F = 1536;
constexpr unsigned WS_AB = 0, WS_H = WS_AB + 2 * S * D * 2, WS_P = WS_H + S * F * 2, WS_WD = WS_P + 3 * S * D * 2, WS_RS = WS_WD + F * D * 2,
                   WS_CS = WS_RS + S * 8, WS_CD = WS_CS + 16 * 4 * 16 * 64 * 4, WS_CTRL = WS_CD + 8192, WS_END = WS_CTRL + 5120 * 128;
constexpr int C_E = 0, C_F0 = 1, C_FAB = C_F0 + 1024, C_FH = C_FAB + 512, C_FP = C_FH + 1024;
#define FLAG(i) ((u64*)(P.ws + WS_CTRL) + (i) * 16)

DI unsigned pk_bf16(float lo, float hi) { f32x2 v = {lo, hi}; return __builtin_bit_cast(unsigned, __builtin_convertvector(v, bf16x2_t)); }
DI float rbf(float f) { return __uint_as_float(pk_bf16(f, f) << 16); }
DI float bflo(unsigned u) { return __uint_as_float(u << 16); }
DI float bfhi(unsigned u) { return __uint_as_float(u & 0xffff0000u); }
DI float dot2(unsigned a, unsigned b, float c) { return __builtin_amdgcn_fdot2_f32_bf16(__builtin_bit_cast(bf16x2_t, a), __builtin_bit_cast(bf16x2_t, b), c, false); }
#define GLDS4(POL) asm volatile("s_mov_b32 m0, %0\n\ts_nop 0\n\tglobal_load_lds_dwordx4 %2, %1" POL "\n\tglobal_load_lds_dwordx4 %3, %1 offset:1024" POL \
    "\n\tglobal_load_lds_dwordx4 %4, %1 offset:2048" POL "\n\tglobal_load_lds_dwordx4 %5, %1 offset:3072" POL \
    :: "s"(lds), "s"(base), "v"(v0), "v"(v1), "v"(v2), "v"(v3) : "memory")
#define GLDS5(POL) asm volatile("s_mov_b32 m0, %0\n\ts_nop 0\n\tglobal_load_lds_dwordx4 %2, %1" POL "\n\tglobal_load_lds_dwordx4 %3, %1 offset:1024" POL \
    "\n\tglobal_load_lds_dwordx4 %4, %1 offset:2048" POL "\n\tglobal_load_lds_dwordx4 %5, %1 offset:3072" POL \
    "\n\ts_add_u32 m0, %0, 0x1000\n\ts_nop 0\n\tglobal_load_lds_dwordx4 %6, %1" POL \
    :: "s"(lds), "s"(base), "v"(v0), "v"(v1), "v"(v2), "v"(v3), "v"(v4) : "memory", "scc")
template <bool DEV> DI void glds4(unsigned lds, const char* base, unsigned v0, unsigned v1, unsigned v2, unsigned v3) {
  if (DEV) GLDS4(" sc1"); else GLDS4("");
}
template <bool DEV> DI void glds5(unsigned lds, const char* base, unsigned v0, unsigned v1, unsigned v2, unsigned v3, unsigned v4) {
  if (DEV) GLDS5(" sc1"); else GLDS5("");
}
template <int N> DI void wait_v() { asm volatile("s_waitcnt vmcnt(%0)" :: "i"(N) : "memory"); }
#define WAIT_L0() do { __builtin_amdgcn_s_waitcnt(0xC07F); asm volatile("" ::: "memory"); } while (0)
#define BAR() do { asm volatile("" ::: "memory"); __builtin_amdgcn_s_barrier(); asm volatile("" ::: "memory"); } while (0)
#define SGB(m, n) __builtin_amdgcn_sched_group_barrier(m, n, 0)
#define PIN() __builtin_amdgcn_sched_barrier(0)
DI void mf(f32x4& c, const bf16x8& a, const bf16x8& b, bool first) {
  if (first) asm volatile("v_mfma_f32_16x16x32_bf16 %0, %1, %2, 0" : "=&v"(c) : "v"(a), "v"(b));
  else asm volatile("v_mfma_f32_16x16x32_bf16 %0, %1, %2, %0" : "+v"(c) : "v"(a), "v"(b));
}
#define MFMA_DONE() asm volatile("s_nop 15")
template <int A, int B> DI void after_mfma(f32x4 (&c)[A][B]) {
#pragma unroll
  for (int i = 0; i < A; ++i)
#pragma unroll
    for (int j = 0; j < B; ++j) asm volatile("" : "+v"(c[i][j]));
}
#define STAMP_AT(i) do { if ((threadIdx.x & 63) == 0 && (threadIdx.x >> 6) == ((i) >= 8 ? 4 : 0)) { P.stamps[blockIdx.x * 32 + 2 * (i)] = __builtin_amdgcn_s_memrealtime(); P.stamps[blockIdx.x * 32 + 2 * (i) + 1] = __builtin_amdgcn_s_memtime(); } } while (0)

template <int CTRL> DI float dpp_add(float v) { return v + __builtin_bit_cast(float, __builtin_amdgcn_update_dpp(0, __builtin_bit_cast(int, v), CTRL, 0xf, 0xf, true)); }
DI float sum8(float v) { v = dpp_add<0xB1>(v); v = dpp_add<0x4E>(v); return dpp_add<0x141>(v); }
DI float sum16(float v) { return dpp_add<0x140>(sum8(v)); }
DI float xchg16(float keep_lo, float keep_hi) {
  const auto r = __builtin_amdgcn_permlane16_swap(__float_as_uint(keep_lo), __float_as_uint(keep_hi), false, false);
  return __uint_as_float(r[0]) + __uint_as_float(r[1]);
}
DI float xchg32(float keep_lo, float keep_hi) {
  const auto r = __builtin_amdgcn_permlane32_swap(__float_as_uint(keep_lo), __float_as_uint(keep_hi), false, false);
  return __uint_as_float(r[0]) + __uint_as_float(r[1]);
}
#define TIC(v)
#define ACC(a, x, y)
#define PUT(slot, a)
DI u64 ld_flag(const u64* p) { return __hip_atomic_load((const GAS u64*)p, __ATOMIC_RELAXED, __HIP_MEMORY_SCOPE_AGENT); }
DI void st_flag(u64* p, u64 v) { __hip_atomic_store((GAS u64*)p, v, __ATOMIC_RELAXED, __HIP_MEMORY_SCOPE_AGENT); }
DI void wait_flags(const u64* p, u64 tag) {
#pragma unroll 1
  for (int it = 0; it < 60000; ++it) {
    if (__all(ld_flag(p) == tag)) break;
    __builtin_amdgcn_s_sleep(4);
  }
  asm volatile("" ::: "memory");
}
DI void wait_flags2(const u64* p, const u64* p2, u64 tag) {
#pragma unroll 1
  for (int it = 0; it < 60000; ++it) {
    const u64 a = ld_flag(p), b = ld_flag(p2);
    if (__all(a == tag && b == tag)) break;
    __builtin_amdgcn_s_sleep(4);
  }
  asm volatile("" ::: "memory");
}
DI u64 read_tag(const char* ws) {
  u64 e;
  asm volatile("s_load_dwordx2 %0, %1, 0x0\n\ts_waitcnt lgkmcnt(0)" : "=s"(e) : "s"(ws + WS_CTRL) : "memory");
  return e * 6364136223846793005ull + 1442695040888963407ull;
}
DI void st16c(__amdgpu_buffer_rsrc_t r, unsigned off, u32x4 v) { __builtin_amdgcn_raw_buffer_store_b128(v, r, off, 0, 16); }
DI u32x4 ld16c(__amdgpu_buffer_rsrc_t r, unsigned off) { return __builtin_amdgcn_raw_buffer_load_b128(r, off, 0, 16); }

struct Params {
  const bf16_t *x, *wsr, *wsi, *wdr, *wdi, *wg, *wu, *wdn;
  bf16_t* out; char* ws;
  u64* stamps;
};

constexpr int L_W = 49152, L_D = 98304, K1_TAB = 159744, LDS_BYTES = K1_TAB + 4096;
constexpr int T_RS = K1_TAB, T_RM = K1_TAB + 512, T_CS = K1_TAB + 1024, T_CD = K1_TAB + 2048, T_GO = K1_TAB + 3584, T_PF = K1_TAB + 3072;
constexpr int A_ST = 16384, B_ST = 20480, L_B = 4 * A_ST;

constexpr int NY = 16, NK2 = 28;
constexpr int F_Y = 147456, F_T = 0, FPITCH = 176;

template <bool TG>
DI void helper_p1(const Params& P, LAS char* smem, int lane, int lw, int mt, int ns, int split, int mt2, int nt, u64& tag, u64& fabv) {
  constexpr int NL = TG ? 12 : 13;
  const int n8 = ns & 7, hcol0 = TG ? 128 * n8 : 1024 + 64 * n8;
  const int rr = lane >> 3, c8 = lane & 7;
  const unsigned lds0 = (unsigned)(size_t)smem;
  unsigned vX[4], vW[4], vD[5];
#pragma unroll
  for (int j = 0; j < 4; ++j) {
    vX[j] = (32 * lw + 8 * j + rr) * 1280 + ((c8 ^ (TG ? rr : ((rr & 2) | ((j & 2) << 1)))) * 16) - j * 1024;
    vW[j] = (8 * j + rr) * 3072 + ((c8 ^ ((rr & 2) | ((j & 1) << 2))) * 16) - j * 1024;
  }
#pragma unroll
  for (int j = 0; j < 5; ++j) vD[j] = (40 * lw + 8 * j + rr) * 1280 + ((c8 ^ rr) * 16) - (j & 3) * 1024;
  const char* bx = (const char*)P.x + (size_t)mt * 128 * 1280;
  const char* bw = (const char*)((lw >> 1) ? P.wu : P.wg) + hcol0 * 2 + (TG ? (lw & 1) * 128 : (lw & 1) * (32 * 3072));
  const char* bd = (const char*)(n8 >= 4 ? P.wdi : P.wdr) + (size_t)(n8 & 3) * 160 * 1280;
  const unsigned ldsX = __builtin_amdgcn_readfirstlane(lds0 + lw * 4096);
  const unsigned ldsW = __builtin_amdgcn_readfirstlane(lds0 + L_W + (TG ? (lw & 1) * 49152 + (lw >> 1) * 8192 : lw * 4096));
  const unsigned ldsD = __builtin_amdgcn_readfirstlane(lds0 + L_D + lw * 5120);
  auto issue = [&](int kt, int buf) {
    glds4<false>(ldsX + buf * 16384, bx + kt * 128, vX[0], vX[1], vX[2], vX[3]);
    const char* pw = bw + kt * (64 * 3072);
    glds4<false>(ldsW + buf * 16384, pw, vW[0], vW[1], vW[2], vW[3]);
    if (TG) glds4<false>(ldsW + buf * 16384 + 4096, pw + 32 * 3072, vW[0], vW[1], vW[2], vW[3]);
    else glds5<false>(ldsD + buf * 20480, bd + kt * 128, vD[0], vD[1], vD[2], vD[3], vD[4]);
  };
  issue(0, 0); issue(1, 1);
  tag = read_tag(P.ws);
  const char* pfb = (const char*)(split < 2 ? P.wsr : P.wsi) + (size_t)(mt2 * 128 + nt * 32) * 4096 + (split & 1) * 2048;
  const unsigned pfv = (lane & 31) * 4096 + ((lw & 1) * 2 + (lane >> 5)) * 128;
  const u64* fs = FLAG(C_F0 + (lw == 0 ? mt * 64 + lane : (((lane >> 1) & 15) * 16 + ns) * 4 + 2 + (lane & 1)));
  u64 fsv = 0;
  int bn = 2;
#pragma unroll 1
  for (int k = 0; k < 8; ++k) {
    wait_v<NL>();
    BAR();
    if (lw < (TG ? 1 : 2)) {
      if (k == 4) asm volatile("global_load_dwordx2 %0, %1, off sc1" : "+v"(fsv) : "v"(fs) : "memory");
      if (k == 5) {
        asm volatile("" : "+v"(fsv));
        if (!__all(fsv == tag)) wait_flags(fs, tag);
        const char* src = P.ws + (lw == 0 ? WS_RS + mt * 1024 : WS_CD + n8 * 640);
        asm volatile("s_mov_b32 m0, %0\n\ts_nop 0\n\tglobal_load_lds_dwordx4 %1, %2 sc1" :: "s"(lds0 + (lw == 0 ? T_RS : T_CD)), "v"(lane * 16), "s"(src) : "memory");
      }
    }
    issue(k + 2, bn);
    bn = bn == 2 ? 0 : bn + 1;
    const int b = k >> 1;
    if (PFN && b < PFN && (k & 1) == (mt & 1) && lw >= 2)
      asm volatile("s_mov_b32 m0, %0\n\ts_nop 0\n\tglobal_load_lds_dword %1, %2" :: "s"(lds0 + T_PF), "v"(pfv), "s"(pfb + b * 512) : "memory");
  }
  wait_v<NL>();
  BAR();
  wait_v<0>();
  BAR();
  if (TG || lw < 2) {
    wait_flags(FLAG(C_F0 + ((lane & 15) * 16 + ns) * 4 + lw), tag);
    const __amdgpu_buffer_rsrc_t rw = __builtin_amdgcn_make_buffer_rsrc(P.ws, 0, WS_END, 0x00020000);
    f32x4 s = {0, 0, 0, 0};
#pragma unroll
    for (int i = 0; i < 4; ++i) s += __builtin_bit_cast(f32x4, ld16c(rw, WS_CS + ((ns * 4 + lw) * 16) * 256 + i * 1024 + lane * 16));
#pragma unroll
    for (int e = 0; e < 4; ++e) { s[e] = xchg16(s[e], s[e]); s[e] = xchg32(s[e], s[e]); }
    if (lane < 16) *(LAS f32x4*)(smem + T_CS + lw * 256 + lane * 16) = s;
  }
  if (!TG) __builtin_amdgcn_s_sleep(FABSL);
  fabv = ld_flag(FLAG(C_FAB + (nt * 64 + split * 16) * 2 + (lane & 31)));
  asm volatile("" : "+v"(fabv));
  if (lw == 0 && lane == 0) *(volatile LAS unsigned*)(smem + T_GO) = 0;
  WAIT_L0();
  BAR();
}

DI void helper_p2(const Params& P, LAS char* smem, int lane, int lw, int split, int mt, int nt, u64 fabv, u64 tag) {
  const unsigned lds0 = (unsigned)(size_t)smem;
  const int rr = lane >> 3;
  unsigned vY[4];
#pragma unroll
  for (int j = 0; j < 4; ++j) vY[j] = (32 * lw + 8 * j + rr) * 4096 + (((lane & 7) ^ rr) * 16) - j * 1024;
  const unsigned vL = lane * 16, vL4 = vL + 4096;
  const char* baseY = (const char*)(split < 2 ? P.wsr : P.wsi) + (size_t)mt * 128 * 4096 + (split & 1) * 2048;
  const char* baseAB = P.ws + WS_AB + (nt * 64 + split * 16) * B_ST + lw * 5120;
  const char* baseH = P.ws + WS_H + ((mt * 24 + (lw >> 1)) * 16 + (split & 1) * 8 + (lw & 1) * 4) * 1024;
  const char* baseWD = P.ws + WS_WD + ((nt * 48 + lw) * 10 + (split >> 1) * 5) * 1024;
  const int rot = ROT2 ? (nt & 1) * 8 : 0;
  const unsigned ldsA = __builtin_amdgcn_readfirstlane(lds0 + lw * 4096), ldsB = __builtin_amdgcn_readfirstlane(lds0 + L_B + lw * 5120);
  auto issueYA = [&](int k) { glds4<false>(ldsA + (k & 3) * A_ST, baseY + ((k + rot) & 15) * 128, vY[0], vY[1], vY[2], vY[3]); };
  auto issueYB = [&](int k) { glds5<true>(ldsB + (k & 3) * B_ST, baseAB + ((k + rot) & 15) * B_ST, vL, vL, vL, vL, vL4); };
  auto issueD = [&](int k) {
    glds4<true>(ldsA + (k & 3) * A_ST, baseH + (k - NY) * (2 * A_ST), vL, vL, vL, vL);
    glds5<true>(ldsB + (k & 3) * B_ST, baseWD + (k - NY) * (2 * B_ST), vL, vL, vL, vL, vL4);
  };
  STAMP_AT(8);
  issueYA(0); issueYA(1);
  const bool ab_seen = __all(fabv == tag);
  if (lw == 0) {
    if (!ab_seen) wait_flags(FLAG(C_FAB + (nt * 64 + split * 16) * 2 + (lane & 31)), tag);
    if (lane == 0) *(volatile LAS unsigned*)(smem + T_GO) = 1;
  } else if (!ab_seen) {
#pragma unroll 1
    for (int it = 0; it < 4000000; ++it) {
      if (*(volatile LAS unsigned*)(smem + T_GO) != 0) break;
      __builtin_amdgcn_s_sleep(1);
    }
  }
  asm volatile("" ::: "memory");
  STAMP_AT(9);
  const u64* fh = FLAG(C_FH + mt * 64 + (lane & 31) * 2 + (split & 1));
  const int l36 = min(lane, 35);
  const int wt = (nt * 240 + ((l36 / 3) * 4 + lw) * 5 + (split >> 1) * 2 + l36 % 3) >> 1;
  const u64* fw = FLAG(C_FH + (wt >> 5) * 64 + (wt & 31));
  u64 fhv = 0, fwv = 0;
  u64 a0 = 0, a1 = 0, a2 = 0;
  issueYB(0); issueYB(1);
  wait_v<5>(); BAR();
  issueYA(2); issueYB(2);
  wait_v<9>(); BAR();
  issueYA(3); issueYB(3); issueYA(4); issueYB(4);
#pragma unroll 1
  for (int k = 2; k < 13; ++k) {
    TIC(t0); wait_v<18>(); TIC(t1); BAR(); TIC(t2);
    if (k == 10) asm volatile("global_load_dwordx2 %0, %2, off sc1\n\tglobal_load_dwordx2 %1, %3, off sc1" : "+v"(fhv), "+v"(fwv) : "v"(fh), "v"(fw) : "memory");
    issueYA(k + 3); issueYB(k + 3);
    TIC(t3);
    ACC(a0, t0, t1); ACC(a1, t1, t2); ACC(a2, t2, t3);
  }
  PUT(3, a0); PUT(4, a1); PUT(5, a2);
#pragma unroll 1
  for (int k = 13; k < NK2 - 3; ++k) {
    wait_v<18>(); BAR();
    if (k == 13) {
      asm volatile("" : "+v"(fhv), "+v"(fwv));
      if (!__all(fhv == tag && fwv == tag)) wait_flags2(fh, fw, tag);
    }
    issueD(k + 3);
  }
  wait_v<18>(); BAR();
  wait_v<9>(); BAR();
  wait_v<0>(); BAR();
  STAMP_AT(10);
  if (blockIdx.x == 0 && lw == 0) {
#pragma unroll 1
    for (int i = 0; i < 4; ++i) wait_flags(FLAG(C_F0 + (i * 64 + lane) * 4), tag);
    if (lane == 0) st_flag(FLAG(C_E), tag);
  }
}

struct Carry { u32x4 v[12]; };

template <bool TG>
DI void mfma_p1(const Params& P, LAS char* smem, int lane, int w, int mt, int ns, u64& tag, Carry& cy) {
  const int n8 = ns & 7, hcol0 = TG ? 128 * n8 : 1024 + 64 * n8;
  const __amdgpu_buffer_rsrc_t rw = __builtin_amdgcn_make_buffer_rsrc(P.ws, 0, WS_END, 0x00020000);
  const int row = mt * 128 + ns * 8 + 2 * w + (lane >> 5);
  const int rr = lane >> 3, c8 = lane & 7;
  const bool colw = TG || w < 2;
  u32x2 vx[5]; u32x4 vw[10];
  {
    const char* px = (const char*)P.x + (size_t)row * 1280 + (lane & 31) * 8;
#pragma unroll
    for (int i = 0; i < 5; ++i) vx[i] = *(const u32x2*)(px + i * 256);
    if (colw) {
      const bf16_t* wsel = (TG ? (w >> 1) : w) ? P.wu : P.wg;
      const char* pw = (const char*)wsel + (size_t)(40 * mt + rr) * 3072 + (hcol0 + (TG ? (w & 1) * 64 : 0)) * 2 + c8 * 16;
#pragma unroll
      for (int i = 0; i < 5; ++i) vw[i] = *(const u32x4*)(pw + i * (8 * 3072));
    } else {
      const int e = (n8 & 3) * 160 + 10 * mt + 5 * (w - 2) + min(rr, 4);
      const char* pd = (const char*)(n8 >= 4 ? P.wdi : P.wdr) + (size_t)e * 1280 + c8 * 16;
#pragma unroll
      for (int i = 0; i < 10; ++i) vw[i] = *(const u32x4*)(pd + i * 128);
    }
  }
  tag = read_tag(P.ws);
  const int wm = w & 1, wn = w >> 1;
  int q = lane >> 4, rho = lane & 15;
  constexpr int NC = TG ? 4 : 2;
  unsigned vX0 = TG ? wm * 8192 + rho * 128 + ((q ^ (rho & 7)) * 16)
                          : wm * 8192 + (8 * (rho >> 2) + (rho & 3)) * 128 + ((q ^ ((rho & 2) | ((rho >> 1) & 4))) * 16);
  unsigned vX1 = vX0 ^ 64;
  unsigned vW[NC];
  {
    const int qq = rho >> 2, p = lane & 3, swz = (qq & 2) | ((q & 1) << 2);
#pragma unroll
    for (int c = 0; c < NC; ++c) {
      const int cc = TG ? c : (2 * wn + c);
      vW[c] = L_W + (TG ? wn * 49152 : 0) + (8 * q + qq) * 128 + ((((2 * cc) ^ swz) + (p >> 1)) * 16) + 8 * (p & 1);
    }
  }
  unsigned vD0 = L_D + (80 * wn + rho) * 128 + ((q ^ (rho & 7)) * 16), vD1 = vD0 ^ 64;
  asm volatile("" : "+v"(vX0), "+v"(vX1));
  if (!TG) asm volatile("" : "+v"(vD0), "+v"(vD1));
#pragma unroll
  for (int c = 0; c < NC; ++c) asm volatile("" : "+v"(vW[c]));
  f32x4 aG[NC][4], aU[NC][4], aA[4][5];
  {
    unsigned ONE2 = 0x3f803f80u, SLO = 0x00003f80u, SHI = 0x3f800000u;
    asm volatile("" : "+v"(ONE2), "+v"(SLO), "+v"(SHI));
    float sx = 0, sq = 0;
#pragma unroll
    for (int i = 0; i < 5; ++i)
#pragma unroll
      for (int e = 0; e < 2; ++e) { sx = dot2(vx[i][e], ONE2, sx); sq = dot2(vx[i][e], vx[i][e], sq); }
    sx = sum16(sx); sq = sum16(sq);
    sx = xchg16(sx, sx); sq = xchg16(sq, sq);
    const float mean = sx * (1.0f / D);
    const float var = fmaxf(sq * (1.0f / D) - mean * mean, 0.f);
    const float sd = rbf(__builtin_amdgcn_sqrtf(rbf(rbf(var) + 1e-5f)));
    const float rso = (lane & 1) ? rbf(mean) : __builtin_amdgcn_rcpf(sd);
    float cres; unsigned coff;
    if (colw) {
      float c[8] = {0, 0, 0, 0, 0, 0, 0, 0};
#pragma unroll
      for (int i = 0; i < 5; ++i)
#pragma unroll
        for (int e = 0; e < 4; ++e) { c[2 * e] = dot2(vw[i][e], SLO, c[2 * e]); c[2 * e + 1] = dot2(vw[i][e], SHI, c[2 * e + 1]); }
      float d4[4], d2[2];
#pragma unroll
      for (int e = 0; e < 4; ++e) d4[e] = xchg32(c[e], c[4 + e]);
#pragma unroll
      for (int e = 0; e < 2; ++e) d2[e] = xchg16(d4[e], d4[2 + e]);
      d2[0] = dpp_add<0x128>(d2[0]); d2[1] = dpp_add<0x128>(d2[1]);
      cres = (lane & 8) ? d2[1] : d2[0];
      coff = WS_CS + (((ns * 4 + w) * 16 + mt) * 64 + 8 * c8 + rr) * 4;
    } else {
      float c0 = 0, c1 = 0;
#pragma unroll
      for (int i = 0; i < 10; ++i) { c0 = dot2(vw[i][0], ONE2, c0); c1 = dot2(vw[i][1], ONE2, c1); c0 = dot2(vw[i][2], ONE2, c0); c1 = dot2(vw[i][3], ONE2, c1); }
      cres = sum8(c0 + c1);
      coff = (c8 == 0 && rr < 5) ? WS_CD + (n8 * 160 + 10 * mt + 5 * (w - 2) + rr) * 4 : 0xfffffff0u;
    }
    asm volatile("" : "+v"(cres));
    __builtin_amdgcn_raw_buffer_store_b32(__float_as_uint(cres), rw, coff, 0, 16);
    __builtin_amdgcn_raw_buffer_store_b32(__float_as_uint(rso), rw, (lane & 30) == 0 ? WS_RS + mt * 1024 + (lane & 1) * 512 + (row & 127) * 4 : 0xfffffff0u, 0, 16);
  }
  bf16x8 FX[2][4], FD[2][5];
  s16x4 FW[2][2 * NC][2];
  constexpr int NR = TG ? 20 : 17, NM = TG ? 32 : 36;
  auto rd1 = [&](int s, int buf, int ks, int r) {
    if (r >= 2 && r < 6) {
      const int i = r - 2;
      FX[s][i] = *(LAS bf16x8*)(smem + (ks ? vX1 : vX0) + buf * 16384 + (TG ? i * 2048 : (i >> 1) * 4096 + (i & 1) * 512));
    } else if (r < 4 + 4 * NC) {
      const int h = r < 2 ? r : r - 4, f = h >> 1;
      FW[s][f][h & 1] = __builtin_amdgcn_ds_read_tr16_b64_v4i16((LAS s16x4*)(smem + vW[f >> 1] + buf * 16384 + ks * 4096 + (f & 1) * 8192 + (h & 1) * 512));
    } else {
      const int jj = r - (4 + 4 * NC);
      FD[s][jj] = *(LAS bf16x8*)(smem + (ks ? vD1 : vD0) + buf * 20480 + jj * 2048);
    }
  };
  auto mm1 = [&](int s, int m, bool first) {
    const int f = m >> 2, i = m & 3;
    if (f < 2 * NC) {
      const bf16x8 wf = __builtin_shufflevector(FW[s][f][0], FW[s][f][1], 0, 1, 2, 3, 4, 5, 6, 7);
      mf((f & 1) ? aU[f >> 1][i] : aG[f >> 1][i], wf, FX[s][i], first);
    } else mf(aA[i][f - 2 * NC], FX[s][i], FD[s][f - 2 * NC], first);
  };
  auto seg = [&](int s, bool first, int m0, int m1, int rk, int buf, int ks) {
    int r = 0;
#pragma unroll
    for (int m = m0; m < m1; ++m) {
      mm1(s, m, first);
      if (rk == 2 && r == 0) r = 2;
      if (rk == 2 && r == 6) r = 4 + 4 * NC;
      const int rend = rk == 0 ? 0 : rk == 3 ? 4 + 4 * NC : NR;
      if (r < rend && (TG ? (m % 5 != 4) : (rk == 3 || m % 3 != 2))) { rd1(s ^ 1, buf, ks, r); ++r; }
      PIN();
    }
  };
  BAR();
  STAMP_AT(1);
#pragma unroll
  for (int r = 0; r < NR; ++r) rd1(0, 0, 0, r);
  PIN();
  constexpr int KJ = TG ? 10 : 7;
#pragma unroll
  for (int k = 0; k < 10; ++k) {
    const int m0 = k < KJ ? 0 : 8 * NC;
    seg(0, k == 0, m0, NM, k < KJ ? 1 : 2, k % 3, 1);
    if (k == 1) {
      wait_v<0>();
      if (lane == 0) st_flag(FLAG(C_F0 + (mt * 16 + ns) * 4 + w), tag);
    }
    if (k < 9) {
      WAIT_L0();
      BAR();
      PIN();
    }
    if (k < 9) seg(1, false, m0, NM, k + 1 < KJ ? 1 : 2, (k + 1) % 3, 0);
    else seg(1, false, m0, NM, TG ? 0 : 3, KJ % 3, 0);
    WAIT_L0(); PIN();
  }
  if (!TG) {
    MFMA_DONE();
    after_mfma(aA);
    asm volatile("" : "+v"(lane)); q = lane >> 4; rho = lane & 15;
    STAMP_AT(4);
    const float sgn = (n8 >= 4) ? -1.0f : 1.0f;
    float cs[5];
#pragma unroll
    for (int j = 0; j < 5; ++j) cs[j] = *(LAS float*)(smem + T_CD + (80 * wn + 16 * j + rho) * 4);
    const int kt = (n8 >= 4 ? 32 : 0) + mt * 2 + wm;
#pragma unroll
    for (int I = 0; I < 2; ++I) {
      f32x4 r4[2], m4[2];
#pragma unroll
      for (int t = 0; t < 2; ++t) {
        const int row = 64 * wm + 32 * I + 8 * q + 4 * t;
        r4[t] = *(LAS f32x4*)(smem + T_RS + row * 4) * sgn; m4[t] = *(LAS f32x4*)(smem + T_RM + row * 4);
      }
      const unsigned dst = WS_AB + ((((n8 & 3) * 64 + kt) * 2 + I) * 10 + 5 * wn) * 1024 + lane * 16;
#pragma unroll
      for (int j = 0; j < 5; ++j) {
        const f32x4 v0 = (aA[2 * I][j] - m4[0] * cs[j]) * r4[0], v1 = (aA[2 * I + 1][j] - m4[1] * cs[j]) * r4[1];
        const u32x4 o = {pk_bf16(v0[0], v0[1]), pk_bf16(v0[2], v0[3]), pk_bf16(v1[0], v1[1]), pk_bf16(v1[2], v1[3])};
        st16c(rw, dst + j * 1024, o);
      }
    }
    STAMP_AT(7);
    WAIT_L0(); PIN();
#pragma unroll
    for (int k = KJ; k < 10; ++k) {
      seg(0, false, 0, 8 * NC, 3, k % 3, 1);
      WAIT_L0(); PIN();
      if (k == KJ + AFL) {
        wait_v<0>();
        if (lane == 0) st_flag(FLAG(C_FAB + ((n8 & 3) * 64 + kt) * 2 + wn), tag);
        PIN();
      }
      seg(1, false, 0, 8 * NC, k < 9 ? 3 : 0, (k + 1) % 3, 0);
      WAIT_L0(); PIN();
    }
  }
  MFMA_DONE();
  after_mfma(aG); after_mfma(aU);
  asm volatile("" : "+v"(lane)); q = lane >> 4; rho = lane & 15;
  unsigned wv[2][8];
  if (TG) {
#pragma unroll
    for (int u = 0; u < 2; ++u) {
      const int task = min(((mt * 8 + ns) * 4 + w) * 2 + u, 959);
      const unsigned* src = (const unsigned*)(P.wdn + (size_t)(((task % 240) / 5) * 32 + 4 * q) * D + (task / 240) * 160 + (task % 5) * 32 + 2 * rho);
#pragma unroll
      for (int e = 0; e < 8; ++e) wv[u][e] = src[((e & 3) + (e >> 2) * 16) * (D / 2)];
    }
  }
  BAR();
  STAMP_AT(2);
  {
    const float K0 = -2.0f * 0.7978845608028654f * 1.4426950408889634f, K1 = K0 * 0.044715f;
    float mu[4], c0[4], c1[4], v2[4];
#pragma unroll
    for (int i = 0; i < 4; ++i) {
      const int row = TG ? 64 * wm + 16 * i + rho : 64 * wm + 32 * (i >> 1) + 8 * (rho >> 2) + 4 * (i & 1) + (rho & 3);
      const float rs = *(LAS float*)(smem + T_RS + row * 4);
      mu[i] = *(LAS float*)(smem + T_RM + row * 4);
      c0[i] = K0 * rs; c1[i] = K1 * rs * rs * rs; v2[i] = __builtin_amdgcn_rcpf(rs * rs);
    }
#pragma unroll
    for (int J = 0; J < NC / 2; ++J) {
      f32x4 cg[2], cu[2];
#pragma unroll
      for (int t = 0; t < 2; ++t) {
        const int c = 2 * J + t;
        const int o = TG ? (wn * 256 + c * 64 + q * 16) : ((2 * wn + c) * 64 + q * 16);
        cg[t] = *(LAS f32x4*)(smem + T_CS + o);
        cu[t] = *(LAS f32x4*)(smem + T_CS + (TG ? 512 : 256) + o);
      }
#pragma unroll
      for (int i = 0; i < 4; ++i) {
        u32x4 o;
#pragma unroll
        for (int t = 0; t < 2; ++t) {
          const f32x4 g = aG[2 * J + t][i] - cg[t] * mu[i];
          const f32x4 u = aU[2 * J + t][i] - cu[t] * mu[i];
          const f32x4 z = g * (g * g * c1[i] + c0[i]);
          f32x4 e;
#pragma unroll
          for (int r = 0; r < 4; ++r) e[r] = __builtin_amdgcn_exp2f(z[r]);
          e = e * v2[i] + v2[i];
#pragma unroll
          for (int r = 0; r < 4; ++r) e[r] = __builtin_amdgcn_rcpf(e[r]);
          const f32x4 hv = g * u * e;
          o[2 * t] = pk_bf16(hv[0], hv[1]); o[2 * t + 1] = pk_bf16(hv[2], hv[3]);
        }
        cy.v[J * 4 + i] = o;
        asm volatile("" : "+v"(cy.v[J * 4 + i]));
      }
    }
  }
  if (TG) {
    const bool up = rho & 8;
#pragma unroll
    for (int u = 0; u < 2; ++u)
#pragma unroll
      for (int e = 0; e < 4; ++e) {
        const unsigned lo = __builtin_amdgcn_perm(wv[u][2 * e + 1], wv[u][2 * e], 0x05040100u), hi = __builtin_amdgcn_perm(wv[u][2 * e + 1], wv[u][2 * e], 0x07060302u);
        const unsigned t = __builtin_amdgcn_update_dpp(0, up ? lo : hi, 0x128, 0xf, 0xf, true);
        cy.v[8 + 2 * u][e] = up ? t : lo;
        cy.v[9 + 2 * u][e] = up ? hi : t;
      }
  } else {
#pragma unroll
    for (int n = 4; n < 12; ++n) cy.v[n] = (u32x4){0, 0, 0, 0};
  }
  STAMP_AT(3);
}

DI void mfma_p2(const Params& P, LAS char* smem, int lane, int w, int split, int mt, int nt, int mt1, int ns, const Carry& cy, u64 tag) {
  asm volatile("" : "+v"(lane));
  const int wm = w & 1, wn = w >> 1, q = lane >> 4, r15 = lane & 15;
  unsigned vY0 = wm * 8192 + r15 * 128 + ((q ^ (r15 & 7)) * 16), vY1 = vY0 ^ 64;
  unsigned vB0 = L_B + wn * 5120 + lane * 16, vB2 = vB0 + 2 * B_ST;
  unsigned vH = w * 2048 + lane * 16, vW0 = L_B + lane * 16, vW2 = vW0 + 2 * B_ST;
  asm volatile("" : "+v"(vY0), "+v"(vY1), "+v"(vB0), "+v"(vB2), "+v"(vH), "+v"(vW0), "+v"(vW2));
  const __amdgpu_buffer_rsrc_t rw = __builtin_amdgcn_make_buffer_rsrc(P.ws, 0, WS_END, 0x00020000);
  const int tile = mt * 4 + nt;
  f32x4 acc[4][5], dac[1][5];
  bf16x8 FA[2][4], FB[2][5], GA[2][2], GB[2][10];
  auto rdy = [&](int s, int k, int ks, int r) {
    const int buf = k & 3;
    if (r >= 1 && r < 5) FA[s][r - 1] = *(LAS bf16x8*)(smem + (ks ? vY1 : vY0) + buf * A_ST + (r - 1) * 2048);
    else { const int j = r ? r - 4 : 0; FB[s][j] = *(LAS bf16x8*)(smem + (buf >= 2 ? vB2 : vB0) + (buf & 1) * B_ST + ks * 10240 + j * 1024); }
  };
  auto rdd = [&](int s, int k, int hf, int r) {
    const int buf = k & 3, u = r / 6, j = r % 6;
    if (j == 0) GA[s][u] = *(LAS bf16x8*)(smem + vH + buf * A_ST + hf * 8192 + u * 1024);
    else GB[s][u * 5 + j - 1] = *(LAS bf16x8*)(smem + (buf >= 2 ? vW2 : vW0) + (buf & 1) * B_ST + hf * 10240 + u * 5120 + (j - 1) * 1024);
  };
  auto segy = [&](int s, bool first, int k, int ks) {
    int r = 0;
#pragma unroll
    for (int m = 0; m < 20; ++m) {
      mf(acc[m & 3][m >> 2], FB[s][m >> 2], FA[s][m & 3], first);
      if (k < NY) { if (r < 9 && m % 3 != 2) { rdy(s ^ 1, k, ks, r); ++r; } }
      else if (r < 12) { rdd(s ^ 1, k, ks, r); ++r; }
      PIN();
    }
  };
  auto segd = [&](int s, bool first, bool reads, int k, int hf) {
#pragma unroll
    for (int m = 0; m < 10; ++m) {
      mf(dac[0][m % 5], GB[s][m], GA[s][m / 5], first && m < 5);
      if (reads && m < 6) { rdd(s ^ 1, k, hf, 2 * m); rdd(s ^ 1, k, hf, 2 * m + 1); }
      PIN();
    }
  };
  const bool tg = ns < 8;
  const int f0 = tg ? 128 * ns + wn * 64 : 1024 + 64 * (ns & 7) + wn * 32;
  const unsigned hb = WS_H + (((mt1 * 24 + (f0 >> 6)) * 8 + 4 * wm) * 2 + ((f0 >> 5) & 1)) * 1024;
  const unsigned hv = tg ? lane * 16 : (r15 >> 3) * 2048 + (8 * ((r15 >> 2) & 1) + (r15 & 3) + 16 * q) * 16;
  const unsigned wdv = (2 * (r15 & 7) + (r15 >> 3) + 16 * q) * 16;
  const int wt = (mt1 * 8 + ns) * 4 + w;
  auto late_store = [&](int n) {
    if (n < 4) __builtin_amdgcn_raw_buffer_store_b128(cy.v[n], rw, hv, hb + (tg ? n * 2048 : (n >> 1) * 4096 + (n & 1) * 64), 16);
    else if (n < 8) { if (tg) __builtin_amdgcn_raw_buffer_store_b128(cy.v[n], rw, hv, hb + (n - 4) * 2048 + 1024, 16); }
    else if (tg && wt < 480) {
      const int task = wt * 2 + ((n - 8) >> 1);
      __builtin_amdgcn_raw_buffer_store_b128(cy.v[n], rw, wdv, WS_WD + (((task / 240) * 48 + (task % 240) / 5) * 10 + (task % 5) * 2 + (n & 1)) * 1024, 16);
    }
  };
  BAR();
  STAMP_AT(5);
#pragma unroll
  for (int r = 0; r < 9; ++r) rdy(0, 0, 0, r);
  PIN();
#pragma unroll
  for (int k = 0; k < NY; ++k) {
    segy(0, k == 0, k, 1);
    if (k < 6) { late_store(2 * k); late_store(2 * k + 1); }
    if (k == 7) {
      wait_v<0>();
      if (lane == 0) st_flag(FLAG(C_FH + mt1 * 64 + ns * 4 + w), tag);
    }
    WAIT_L0();
    BAR();
    PIN();
    segy(1, false, k + 1, 0);
    WAIT_L0(); PIN();
  }
  MFMA_DONE();
  after_mfma(acc);
  if (threadIdx.x == 0) { P.stamps[blockIdx.x * 32 + 2 * 14] = __builtin_amdgcn_s_memrealtime(); }
#pragma unroll
  for (int i = 0; i < 4; ++i) {
    unsigned pk[10];
#pragma unroll
    for (int j = 0; j < 5; ++j) { pk[2 * j] = pk_bf16(acc[i][j][0], acc[i][j][1]); pk[2 * j + 1] = pk_bf16(acc[i][j][2], acc[i][j][3]); }
    if (w == split) {
      LAS char* dst = smem + F_Y + i * 2560;
      *(LAS u32x4*)(dst + lane * 16) = (u32x4){pk[0], pk[1], pk[2], pk[3]};
      *(LAS u32x4*)(dst + 1024 + lane * 16) = (u32x4){pk[4], pk[5], pk[6], pk[7]};
      *(LAS u32x2*)(dst + 2048 + lane * 8) = (u32x2){pk[8], pk[9]};
    } else {
      const unsigned dst = WS_P + (((tile * 4 + w) * 3 + (split ^ w) - 1) * 4 + i) * 2560;
      st16c(rw, dst + lane * 16, (u32x4){pk[0], pk[1], pk[2], pk[3]});
      st16c(rw, dst + 1024 + lane * 16, (u32x4){pk[4], pk[5], pk[6], pk[7]});
      __builtin_amdgcn_raw_buffer_store_b64((u32x2){pk[8], pk[9]}, rw, dst + 2048 + lane * 8, 0, 16);
    }
  }
  if (threadIdx.x == 0) { P.stamps[blockIdx.x * 32 + 2 * 15] = __builtin_amdgcn_s_memrealtime(); }
  PIN();
  const int orow = mt * 128 + (split & 1) * 64 + w * 16, ocol = nt * 160 + (split >> 1) * 80;
  const u64* fp = FLAG(C_FP + (tile * 4 + split) * 4 + min(lane, 2));
  u64 fpv = 0;
  u32x4 xv[3], pa[3][2]; u32x2 pb[3];
#pragma unroll
  for (int k = NY; k < NK2; ++k) {
    segd(0, k == NY, true, k, 1);
    if (k == NY + YFL && w != split) {
      wait_v<0>();
      if (lane == 0) st_flag(FLAG(C_FP + (tile * 4 + w) * 4 + (split ^ w) - 1), tag);
    }
    if (k == NY + YPL) fpv = ld_flag(fp);
    if (k == NY + YPL + 1) {
      if (!__all(fpv == tag)) wait_flags(fp, tag);
      asm volatile("" ::: "memory");
#pragma unroll
      for (int s = 0; s < 3; ++s) {
        const unsigned src = WS_P + (((tile * 4 + split) * 3 + s) * 4 + w) * 2560;
        pa[s][0] = ld16c(rw, src + lane * 16); pa[s][1] = ld16c(rw, src + 1024 + lane * 16);
        pb[s] = __builtin_amdgcn_raw_buffer_load_b64(rw, src + 2048 + lane * 8, 0, 16);
      }
#pragma unroll
      for (int i = 0; i < 3; ++i) {
        const int n = min(i * 64 + lane, 159);
        xv[i] = *(const u32x4*)(P.x + (size_t)(orow + n / 10) * D + ocol + (n % 10) * 8);
      }
    }
    if (k + 1 < NK2) {
      WAIT_L0();
      BAR();
      PIN();
    }
    segd(1, false, k + 1 < NK2, k + 1, 0);
    WAIT_L0(); PIN();
  }
  MFMA_DONE();
  after_mfma(dac);
  STAMP_AT(6);
  LAS char* po = smem + F_Y + w * 2560;
  const u32x4 oa0 = *(LAS u32x4*)(po + lane * 16), oa1 = *(LAS u32x4*)(po + 1024 + lane * 16);
  const u32x2 ob = *(LAS u32x2*)(po + 2048 + lane * 8);
  LAS char* pt = smem + F_T + w * (16 * FPITCH);
#pragma unroll
  for (int j = 0; j < 5; ++j) {
    const unsigned l0 = j < 2 ? oa0[(j & 1) * 2] : j < 4 ? oa1[(j & 1) * 2] : ob[0], h0 = j < 2 ? oa0[(j & 1) * 2 + 1] : j < 4 ? oa1[(j & 1) * 2 + 1] : ob[1];
    f32x4 y = {bflo(l0), bfhi(l0), bflo(h0), bfhi(h0)};
#pragma unroll
    for (int s = 0; s < 3; ++s) {
      const unsigned lo = j < 4 ? pa[s][j >> 1][(j & 1) * 2] : pb[s][0], hi = j < 4 ? pa[s][j >> 1][(j & 1) * 2 + 1] : pb[s][1];
      y += (f32x4){bflo(lo), bfhi(lo), bflo(hi), bfhi(hi)};
    }
    const f32x4 d = dac[0][j];
    f32x4 e = d * -1.4426950408889634f;
#pragma unroll
    for (int r = 0; r < 4; ++r) e[r] = __builtin_amdgcn_exp2f(e[r]);
    e = e + 1.0f;
#pragma unroll
    for (int r = 0; r < 4; ++r) e[r] = __builtin_amdgcn_rcpf(e[r]);
    e = e * d * y;
    *(LAS u32x2*)(pt + r15 * FPITCH + j * 32 + q * 8) = (u32x2){pk_bf16(e[0], e[1]), pk_bf16(e[2], e[3])};
  }
  WAIT_L0();
#pragma unroll
  for (int i = 0; i < 3; ++i) {
    const int n = i * 64 + lane, nc = min(n, 159);
    const u32x4 v = *(LAS u32x4*)(pt + (nc / 10) * FPITCH + (nc % 10) * 16);
    u32x4 o;
#pragma unroll
    for (int d = 0; d < 4; ++d) o[d] = pk_bf16(bflo(v[d]) + bflo(xv[i][d]), bfhi(v[d]) + bfhi(xv[i][d]));
    if (n < 160) *(u32x4*)(P.out + (size_t)(orow + nc / 10) * D + ocol + (nc % 10) * 8) = o;
  }
  wait_v<0>();
  if (threadIdx.x == 0) { P.stamps[blockIdx.x * 32 + 2 * 12] = __builtin_amdgcn_s_memrealtime(); }
}

__global__ __launch_bounds__(512, 2)
void fused(Params P) {
  extern __shared__ __attribute__((aligned(16))) char smem_[];
  LAS char* smem = (LAS char*)smem_;
  const int tid = threadIdx.x, lane = tid & 63, w = __builtin_amdgcn_readfirstlane(tid >> 6);
  const int bid = blockIdx.x, xcd = bid & 7, idx = bid >> 3;
  u64 tag;
  STAMP_AT(0);
  const int mt1 = (xcd & 1) * 8 + (idx & 7), j = idx >> 3, ns = (j & 1) * 8 + (xcd >> 1) * 2 + (j >> 1);
  const int split = xcd >> 1, mt2 = (xcd & 1) * 8 + (idx >> 2), nt = idx & 3;
  if (w >= 4) {
    __builtin_amdgcn_s_setprio(HPRIO);
    u64 fabv;
    if (j & 1) helper_p1<false>(P, smem, lane, w & 3, mt1, ns, split, mt2, nt, tag, fabv); else helper_p1<true>(P, smem, lane, w & 3, mt1, ns, split, mt2, nt, tag, fabv);
    helper_p2(P, smem, lane, w & 3, split, mt2, nt, fabv, tag);
  } else {
    Carry cy;
    if (j & 1) mfma_p1<false>(P, smem, lane, w, mt1, ns, tag, cy); else mfma_p1<true>(P, smem, lane, w, mt1, ns, tag, cy);
    mfma_p2(P, smem, lane, w, split, mt2, nt, mt1, ns, cy, tag);
    __builtin_amdgcn_s_waitcnt(0);
  }
}

__global__ void diag(const u64* st, int a, int b, int mode) {
  const int lane = threadIdx.x;
  u64 mn = ~0ull, mx = 0, sum = 0;
  for (int i = 0; i < 4; ++i) { const int blk = i * 64 + lane; const u64 x = st[blk * 32 + 2 * a], y = st[blk * 32 + 2 * b];
    mn = x < mn ? x : mn; mx = y > mx ? y : mx; sum += mode == 2 ? (st[blk * 32 + 2 * b + 1] - st[blk * 32 + 2 * a + 1]) / 24 : y - x; }
  for (int o = 1; o < 64; o <<= 1) { const u64 m2 = __shfl_xor(mn, o), x2 = __shfl_xor(mx, o); mn = m2 < mn ? m2 : mn; mx = x2 > mx ? x2 : mx; sum += __shfl_xor(sum, o); }
  u64 d = mode == 0 ? mx - mn : sum / 256;
  if (a < 0 || d > 20000) d = 0;
  const u64 t0 = __builtin_amdgcn_s_memrealtime();
  while (__builtin_amdgcn_s_memrealtime() - t0 < 2 * d) __builtin_amdgcn_s_sleep(1);
}
extern "C" void kernel_launch(void* const* d_in, const int* in_sizes, int n_in, void* d_out, int out_size, void* d_ws, size_t ws_size, hipStream_t stream) {
  Params P;
  P.x = (const bf16_t*)d_in[0]; P.wsr = (const bf16_t*)d_in[1]; P.wsi = (const bf16_t*)d_in[2];
  P.wdr = (const bf16_t*)d_in[3]; P.wdi = (const bf16_t*)d_in[4];
  P.wg = (const bf16_t*)d_in[6]; P.wu = (const bf16_t*)d_in[7]; P.wdn = (const bf16_t*)d_in[8];
  P.out = (bf16_t*)d_out; P.ws = (char*)d_ws;
  P.stamps = (u64*)((char*)d_ws + (128u << 20));
  (void)hipFuncSetAttribute((const void*)fused, hipFuncAttributeMaxDynamicSharedMemorySize, LDS_BYTES);
  fused<<<256, 512, LDS_BYTES, stream>>>(P);
  diag<<<1, 64, 0, stream>>>(P.stamps, 0, 0, 1);
}
```

```cpp
#include <hip/hip_runtime.h>
#include <hip/hip_bf16.h>
typedef unsigned short bf16_t;
typedef unsigned long long u64;
typedef short bf16x8 __attribute__((ext_vector_type(8)));
typedef short s16x4 __attribute__((ext_vector_type(4)));
typedef float f32x4 __attribute__((ext_vector_type(4)));
typedef float f32x2 __attribute__((ext_vector_type(2)));
typedef unsigned u32x4 __attribute__((ext_vector_type(4)));
typedef unsigned u32x2 __attribute__((ext_vector_type(2)));
typedef __bf16 bf16x2_t __attribute__((ext_vector_type(2)));
#define LAS __attribute__((address_space(3)))
#define GAS __attribute__((address_space(1)))
#define DI __device__ __forceinline__

#define STAMP 0
#define NPRE 2
#define ROT2 0
#define YFL 4
#define FABSL 1
#define AFL 2
#define HFL 2
#define PSPL 1
#define XG 2
#define XY 1
#define XD 1
#define YSB 2
#define WK 3
#define FABM 0
#define SK 5
#define YPL 8
#define XROT 0
#define HPRIO 0
#define PFN 4

constexpr int S = 2048, D = 640, F = 1536;
constexpr unsigned WS_AB = 0, WS_H = WS_AB + 2 * S * D * 2, WS_P = WS_H + S * F * 2, WS_WD = WS_P + 3 * S * D * 2, WS_RS = WS_WD + F * D * 2,
                   WS_CS = WS_RS + S * 8, WS_CD = WS_CS + 16 * 4 * 16 * 64 * 4, WS_CTRL = WS_CD + 8192, WS_END = WS_CTRL + 5120 * 128;
constexpr int C_E = 0, C_F0 = 1, C_FAB = C_F0 + 1024, C_FH = C_FAB + 512, C_FP = C_FH + 1024;
#define FLAG(i) ((u64*)(P.ws + WS_CTRL) + (i) * 16)

DI unsigned pk_bf16(float lo, float hi) { f32x2 v = {lo, hi}; return __builtin_bit_cast(unsigned, __builtin_convertvector(v, bf16x2_t)); }
DI float rbf(float f) { return __uint_as_float(pk_bf16(f, f) << 16); }
DI float bflo(unsigned u) { return __uint_as_float(u << 16); }
DI float bfhi(unsigned u) { return __uint_as_float(u & 0xffff0000u); }
DI float dot2(unsigned a, unsigned b, float c) { return __builtin_amdgcn_fdot2_f32_bf16(__builtin_bit_cast(bf16x2_t, a), __builtin_bit_cast(bf16x2_t, b), c, false); }
#define GLDS4(POL) asm volatile("s_mov_b32 m0, %0\n\ts_nop 0\n\tglobal_load_lds_dwordx4 %2, %1" POL "\n\tglobal_load_lds_dwordx4 %3, %1 offset:1024" POL \
    "\n\tglobal_load_lds_dwordx4 %4, %1 offset:2048" POL "\n\tglobal_load_lds_dwordx4 %5, %1 offset:3072" POL \
    :: "s"(lds), "s"(base), "v"(v0), "v"(v1), "v"(v2), "v"(v3) : "memory")
#define GLDS5(POL) asm volatile("s_mov_b32 m0, %0\n\ts_nop 0\n\tglobal_load_lds_dwordx4 %2, %1" POL "\n\tglobal_load_lds_dwordx4 %3, %1 offset:1024" POL \
    "\n\tglobal_load_lds_dwordx4 %4, %1 offset:2048" POL "\n\tglobal_load_lds_dwordx4 %5, %1 offset:3072" POL \
    "\n\ts_add_u32 m0, %0, 0x1000\n\ts_nop 0\n\tglobal_load_lds_dwordx4 %6, %1" POL \
    :: "s"(lds), "s"(base), "v"(v0), "v"(v1), "v"(v2), "v"(v3), "v"(v4) : "memory", "scc")
template <bool DEV> DI void glds4(unsigned lds, const char* base, unsigned v0, unsigned v1, unsigned v2, unsigned v3) {
  if (DEV) GLDS4(" sc1"); else GLDS4("");
}
template <bool DEV> DI void glds5(unsigned lds, const char* base, unsigned v0, unsigned v1, unsigned v2, unsigned v3, unsigned v4) {
  if (DEV) GLDS5(" sc1"); else GLDS5("");
}
DI void touch_lines(unsigned lds, unsigned voff, const char* base) {
  asm volatile("s_mov_b32 m0, %0\n\ts_nop 0\n\tglobal_load_lds_dword %1, %2" :: "s"(lds), "v"(voff), "s"(base) : "memory");
}
template <int N> DI void wait_v() { asm volatile("s_waitcnt vmcnt(%0)" :: "i"(N) : "memory"); }
#define WAIT_L0() do { __builtin_amdgcn_s_waitcnt(0xC07F); asm volatile("" ::: "memory"); } while (0)
#define BAR() do { asm volatile("" ::: "memory"); __builtin_amdgcn_s_barrier(); asm volatile("" ::: "memory"); } while (0)
#define SGB(m, n) __builtin_amdgcn_sched_group_barrier(m, n, 0)
#define PIN() __builtin_amdgcn_sched_barrier(0)
template <int V> struct IC { static constexpr int v = V; };
template <int I, int N, class Fn> DI void sfor(Fn&& f) { if constexpr (I < N) { f(IC<I>{}); sfor<I + 1, N>(f); } }
DI void mf(f32x4& c, const bf16x8& a, const bf16x8& b, bool first) {
  if (first) asm volatile("v_mfma_f32_16x16x32_bf16 %0, %1, %2, 0" : "=&v"(c) : "v"(a), "v"(b));
  else asm volatile("v_mfma_f32_16x16x32_bf16 %0, %1, %2, %0" : "+v"(c) : "v"(a), "v"(b));
}
#define MFMA_DONE() asm volatile("s_nop 15")
template <int A, int B> DI void after_mfma(f32x4 (&c)[A][B]) {
#pragma unroll
  for (int i = 0; i < A; ++i)
#pragma unroll
    for (int j = 0; j < B; ++j) asm volatile("" : "+v"(c[i][j]));
}
#define STAMP_AT(i)

template <int CTRL> DI float dpp_add(float v) { return v + __builtin_bit_cast(float, __builtin_amdgcn_update_dpp(0, __builtin_bit_cast(int, v), CTRL, 0xf, 0xf, true)); }
DI float sum8(float v) { v = dpp_add<0xB1>(v); v = dpp_add<0x4E>(v); return dpp_add<0x141>(v); }
DI float sum16(float v) { return dpp_add<0x140>(sum8(v)); }
DI float xchg16(float keep_lo, float keep_hi) {
  const auto r = __builtin_amdgcn_permlane16_swap(__float_as_uint(keep_lo), __float_as_uint(keep_hi), false, false);
  return __uint_as_float(r[0]) + __uint_as_float(r[1]);
}
DI float xchg32(float keep_lo, float keep_hi) {
  const auto r = __builtin_amdgcn_permlane32_swap(__float_as_uint(keep_lo), __float_as_uint(keep_hi), false, false);
  return __uint_as_float(r[0]) + __uint_as_float(r[1]);
}
#define TIC(v)
#define ACC(a, x, y)
#define PUT(slot, a)
DI u64 ld_flag(const u64* p) { return __hip_atomic_load((const GAS u64*)p, __ATOMIC_RELAXED, __HIP_MEMORY_SCOPE_AGENT); }
DI void st_flag(u64* p, u64 v) { __hip_atomic_store((GAS u64*)p, v, __ATOMIC_RELAXED, __HIP_MEMORY_SCOPE_AGENT); }
DI void wait_flags(const u64* p, u64 tag) {
#pragma unroll 1
  for (int it = 0; it < 60000; ++it) {
    if (__all(ld_flag(p) == tag)) break;
    __builtin_amdgcn_s_sleep(4);
  }
  asm volatile("" ::: "memory");
}
DI void wait_flags2(const u64* p, const u64* p2, u64 tag) {
#pragma unroll 1
  for (int it = 0; it < 60000; ++it) {
    const u64 a = ld_flag(p), b = ld_flag(p2);
    if (__all(a == tag && b == tag)) break;
    __builtin_amdgcn_s_sleep(4);
  }
  asm volatile("" ::: "memory");
}
DI u64 read_tag(const char* ws) {
  u64 e;
  asm volatile("s_load_dwordx2 %0, %1, 0x0\n\ts_waitcnt lgkmcnt(0)" : "=s"(e) : "s"(ws + WS_CTRL) : "memory");
  return e * 6364136223846793005ull + 1442695040888963407ull;
}
DI void st16c(__amdgpu_buffer_rsrc_t r, unsigned off, u32x4 v) { __builtin_amdgcn_raw_buffer_store_b128(v, r, off, 0, 16); }
DI u32x4 ld16c(__amdgpu_buffer_rsrc_t r, unsigned off) { return __builtin_amdgcn_raw_buffer_load_b128(r, off, 0, 16); }

struct Params {
  const bf16_t *x, *wsr, *wsi, *wdr, *wdi, *wg, *wu, *wdn;
  bf16_t* out; char* ws;
};

constexpr int L_W = 49152, L_D = 98304, K1_TAB = 159744, LDS_BYTES = K1_TAB + 4096;
constexpr int T_RS = K1_TAB, T_RM = K1_TAB + 512, T_CS = K1_TAB + 1024, T_CD = K1_TAB + 2048, T_GO = K1_TAB + 3584, T_PF = K1_TAB + 3072;
constexpr int A_ST = 16384, B_ST = 20480, L_B = 4 * A_ST;

#define DMASK 0xFFF0000u
constexpr int NK2 = 28;
constexpr bool is_d(int k) { return (DMASK >> k) & 1; }
constexpr int first_d() { int k = 0; while (!is_d(k)) ++k; return k; }
constexpr int last_y() { int k = NK2 - 1; while (is_d(k)) --k; return k; }
constexpr int KD0 = first_d(), KYL = last_y();
constexpr int d_index(int k) { int n = 0; for (int i = 0; i < k; ++i) n += is_d(i); return n; }

constexpr int F_Y = 147456, F_T = 0, F_R = 12288, FPITCH = 176;

DI void helper(const Params& P, LAS char* smem, int lane, int lw, int mt, int ns, int split, int mt2, int nt) {
  constexpr int NL = 12;
  const bool TG = ns < 8;
  const int n8 = ns & 7, hcol0 = TG ? 128 * n8 : 1024 + 64 * n8;
  const int rr = lane >> 3, c8 = lane & 7;
  const unsigned lds0 = (unsigned)(size_t)smem;
  unsigned vX[4], vW[4], vD[5];
#pragma unroll
  for (int j = 0; j < 4; ++j) {
    vX[j] = (32 * lw + 8 * j + rr) * 1280 + ((c8 ^ (TG ? rr : ((rr & 2) | ((j & 2) << 1)))) * 16) - j * 1024;
    vW[j] = (8 * j + rr) * 3072 + ((c8 ^ ((rr & 2) | ((j & 1) << 2))) * 16) - j * 1024;
  }
#pragma unroll
  for (int j = 0; j < 5; ++j) vD[j] = (40 * lw + 8 * j + rr) * 1280 + ((c8 ^ rr) * 16) - (j & 3) * 1024;
  const char* bx = (const char*)P.x + (size_t)mt * 128 * 1280;
  const char* bw = (const char*)((lw >> 1) ? P.wu : P.wg) + hcol0 * 2 + (TG ? (lw & 1) * 128 : (lw & 1) * (32 * 3072));
  const char* bd = (const char*)(n8 >= 4 ? P.wdi : P.wdr) + (size_t)(n8 & 3) * 160 * 1280;
  const unsigned ldsX = __builtin_amdgcn_readfirstlane(lds0 + lw * 4096);
  const unsigned ldsW = __builtin_amdgcn_readfirstlane(lds0 + L_W + (TG ? (lw & 1) * 49152 + (lw >> 1) * 8192 : lw * 4096));
  const unsigned ldsD = __builtin_amdgcn_readfirstlane(lds0 + L_D + lw * 5120);
  auto issue = [&](int kt) {
    const int buf = kt % 3;
    glds4<false>(ldsX + buf * 16384, bx + kt * 128, vX[0], vX[1], vX[2], vX[3]);
    const char* pw = bw + kt * (64 * 3072);
    glds4<false>(ldsW + buf * 16384, pw, vW[0], vW[1], vW[2], vW[3]);
    if (TG) glds4<false>(ldsW + buf * 16384 + 4096, pw + 32 * 3072, vW[0], vW[1], vW[2], vW[3]);
    else glds5<false>(ldsD + buf * 20480, bd + kt * 128, vD[0], vD[1], vD[2], vD[3], vD[4]);
  };
  issue(0); issue(1);
  const u64 tag = read_tag(P.ws);
  const char* pfb = (const char*)(split < 2 ? P.wsr : P.wsi) + (size_t)(mt2 * 128 + nt * 32) * 4096 + (split & 1) * 2048;
  const unsigned pfv = (lane & 31) * 4096 + ((lw & 1) * 2 + (lane >> 5)) * 128;
  const u64* fa = FLAG(C_F0 + (lw == 0 ? mt * 64 + (lane & 31) : (((lane >> 1) & 15) * 16 + n8) * 4 + 2 + (lane & 1)));
  u64 fav = 0;
  const int xg = ((mt2 ^ nt) & 1) * XG;
  const unsigned vL = lane * 16, vL4 = vL + 4096;
  const char* baseWD = P.ws + WS_WD + ((nt * 48 + lw) * 10 + (split >> 1) * 5) * 1024;
  sfor<0, 8>([&](auto K) __attribute__((always_inline)) {
    constexpr int k = decltype(K)::v;
    if (TG) wait_v<12>(); else wait_v<13>();
    BAR();
    if (lw < (TG ? 1 : 2)) {
      if (k == SK) asm volatile("global_load_dwordx2 %0, %1, off sc1" : "+v"(fav) : "v"(fa) : "memory");
      if (k == SK + 1) {
        asm volatile("" : "+v"(fav));
        if (!__all(fav == tag)) wait_flags(fa, tag);
        const char* src = P.ws + (lw == 0 ? WS_RS + mt * 1024 : WS_CD + n8 * 640);
        asm volatile("s_mov_b32 m0, %0\n\ts_nop 0\n\tglobal_load_lds_dwordx4 %1, %2 sc1" :: "s"(lds0 + (lw == 0 ? T_RS : T_CD)), "v"(vL), "s"(src) : "memory");
      }
    }
    issue(k + 2);
    if (PFN && (k >> 1) < PFN && (k & 1) == (mt & 1) && lw >= 2)
      touch_lines(lds0 + T_PF, pfv, pfb + (k >> 1) * 512);
  });
  wait_v<NL>();
  BAR();
  wait_v<0>();
  BAR();
  if (TG || lw < 2) {
    wait_flags(FLAG(C_F0 + ((lane & 15) * 16 + n8) * 4 + lw), tag);
    const __amdgpu_buffer_rsrc_t rw = __builtin_amdgcn_make_buffer_rsrc(P.ws, 0, WS_END, 0x00020000);
    f32x4 c = {0, 0, 0, 0};
#pragma unroll
    for (int i = 0; i < 4; ++i) c += __builtin_bit_cast(f32x4, ld16c(rw, WS_CS + (ns * 4 + lw) * 4096 + i * 1024 + lane * 16));
#pragma unroll
    for (int e = 0; e < 4; ++e) { c[e] = xchg16(c[e], c[e]); c[e] = xchg32(c[e], c[e]); }
    if (lane < 16) *(LAS f32x4*)(smem + T_CS + lw * 256 + lane * 16) = c;
  }
  if (lw == 0 && lane == 0) *(volatile LAS unsigned*)(smem + T_GO) = 0;
  WAIT_L0();
  if (blockIdx.x == 64 && lw == 3) {
    wait_flags2(FLAG(C_F0 + lane * 4), FLAG(C_F0 + (64 + lane) * 4), tag);
    wait_flags2(FLAG(C_F0 + (128 + lane) * 4), FLAG(C_F0 + (192 + lane) * 4), tag);
    if (lane == 0) st_flag(FLAG(C_E), tag);
  }
  const u64* fab = FLAG(C_FAB + (nt * 64 + split * 16) * 2 + (lane & 31));
  unsigned vY[4];
#pragma unroll
  for (int j = 0; j < 4; ++j) vY[j] = (32 * lw + 8 * j + rr) * 4096 + ((c8 ^ rr) * 16) - j * 1024;
  const char* baseY = (const char*)(split < 2 ? P.wsr : P.wsi) + (size_t)mt2 * 128 * 4096 + (split & 1) * 2048;
  const char* baseAB = P.ws + WS_AB + (nt * 64 + split * 16) * B_ST + lw * 5120;
  const char* baseH = P.ws + WS_H + ((mt2 * 24 + (lw >> 1)) * 16 + (split & 1) * 8 + (lw & 1) * 4) * 1024;
  const unsigned ldsA = ldsX, ldsB = __builtin_amdgcn_readfirstlane(lds0 + L_B + lw * 5120);
  auto issueYA = [&](int k, int yi) { glds4<false>(ldsA + (k & 3) * A_ST, baseY + (yi ^ xg) * 128, vY[0], vY[1], vY[2], vY[3]); };
  auto issueYB = [&](int k, int yi) { glds5<true>(ldsB + (k & 3) * B_ST, baseAB + (yi ^ xg) * B_ST, vL, vL, vL, vL, vL4); };
  BAR();
  STAMP_AT(8);
  u64 fabv;
  if (FABM) asm volatile("global_load_dwordx2 %0, %1, off sc1" : "=&v"(fabv) : "v"(fab) : "memory"); else fabv = ~tag;
  issueYA(0, 0); issueYA(1, 1);
  if (FABM) wait_v<8>();
  asm volatile("" : "+v"(fabv));
  const bool ab_seen = __all(fabv == tag);
  if (lw == 0) {
    if (!ab_seen) wait_flags(fab, tag);
    if (lane == 0) *(volatile LAS unsigned*)(smem + T_GO) = 1;
  } else if (!ab_seen) {
#pragma unroll 1
    for (int it = 0; it < 4000000; ++it) {
      if (*(volatile LAS unsigned*)(smem + T_GO) != 0) break;
      __builtin_amdgcn_s_sleep(1);
    }
  }
  asm volatile("" ::: "memory");
  STAMP_AT(9);
  const u64* fh = FLAG(C_FH + mt2 * 64 + (lane & 31) * 2 + (split & 1));
  const int l36 = min(lane, 35);
  const int wt = (nt * 240 + ((l36 / 3) * 4 + lw) * 5 + (split >> 1) * 2 + l36 % 3) >> 1;
  const u64* fw = FLAG(C_FH + (wt >> 5) * 64 + (wt & 31));
  u64 fhv = 0, fwv = 0;
  issueYB(0, 0); issueYB(1, 1);
  wait_v<5>(); BAR();
  issueYA(2, 2); issueYB(2, 2);
  wait_v<9>(); BAR();
  issueYA(3, 3); issueYB(3, 3); issueYA(4, 4); issueYB(4, 4);
  int yi = 5, di = 0;
#pragma unroll 1
  for (int k = 2; k < NK2 - 3; ++k) {
    wait_v<18>();
    BAR();
    if (k == KD0 - 6) asm volatile("global_load_dwordx2 %0, %2, off sc1\n\tglobal_load_dwordx2 %1, %3, off sc1" : "+v"(fhv), "+v"(fwv) : "v"(fh), "v"(fw) : "memory");
    if (k == KD0 - 3) {
      asm volatile("" : "+v"(fhv), "+v"(fwv));
      if (!__all(fhv == tag && fwv == tag)) wait_flags2(fh, fw, tag);
    }
    if ((DMASK >> (k + 3)) & 1) {
      glds4<true>(ldsA + ((k + 3) & 3) * A_ST, baseH + (di ^ xg) * (2 * A_ST), vL, vL, vL, vL);
      glds5<true>(ldsB + ((k + 3) & 3) * B_ST, baseWD + (di ^ xg) * (2 * B_ST), vL, vL, vL, vL, vL4);
      ++di;
    } else { issueYA(k + 3, yi); issueYB(k + 3, yi); ++yi; }
  }
  wait_v<18>(); BAR();
  wait_v<9>(); BAR();
  wait_v<0>(); BAR();
  STAMP_AT(10);
}

template <bool TG>
DI void mfma_p1(const Params& P, LAS char* smem, int lane, int w, int mt, int ns, u64& tag) {
  const int n8 = ns & 7, hcol0 = TG ? 128 * n8 : 1024 + 64 * n8;
  const __amdgpu_buffer_rsrc_t rw = __builtin_amdgcn_make_buffer_rsrc(P.ws, 0, WS_END, 0x00020000);
  int q = lane >> 4, rho = lane & 15;
  const int rr = lane >> 3, c8 = lane & 7;
  u32x4 vx[5], vw[5], vm[10];
  if (TG) {
    const char* px = (const char*)P.x + (size_t)(mt * 128 + n8 * 16 + 4 * w + q) * 1280 + rho * 16;
#pragma unroll
    for (int i = 0; i < 5; ++i) vx[i] = *(const u32x4*)(px + i * 256);
    const char* pw = (const char*)((w >> 1) ? P.wu : P.wg) + (size_t)(40 * mt + rr) * 3072 + (hcol0 + (w & 1) * 64) * 2 + c8 * 16;
#pragma unroll
    for (int i = 0; i < 5; ++i) vw[i] = *(const u32x4*)(pw + i * (8 * 3072));
    if (w < 2) {
      const char* pm = (const char*)(w ? P.wu : P.wg) + (size_t)(40 * mt + rr) * 3072 + (1024 + 64 * n8) * 2 + c8 * 16;
#pragma unroll
      for (int i = 0; i < 5; ++i) vm[i] = *(const u32x4*)(pm + i * (8 * 3072));
    } else {
      const int e = (n8 & 3) * 160 + 10 * mt + 5 * (w - 2) + min(rr, 4);
      const char* pd = (const char*)(n8 >= 4 ? P.wdi : P.wdr) + (size_t)e * 1280 + c8 * 16;
#pragma unroll
      for (int i = 0; i < 10; ++i) vm[i] = *(const u32x4*)(pd + i * 128);
    }
  }
  tag = read_tag(P.ws);
  const int wm = w & 1, wn = w >> 1;
  constexpr int NC = TG ? 4 : 2;
  unsigned vX0 = TG ? wm * 8192 + rho * 128 + ((q ^ (rho & 7)) * 16)
                          : wm * 8192 + (8 * (rho >> 2) + (rho & 3)) * 128 + ((q ^ ((rho & 2) | ((rho >> 1) & 4))) * 16);
  unsigned vX1 = vX0 ^ 64;
  unsigned vW[NC];
  {
    const int qq = rho >> 2, p = lane & 3, swz = (qq & 2) | ((q & 1) << 2);
#pragma unroll
    for (int c = 0; c < NC; ++c) {
      const int cc = TG ? c : (2 * wn + c);
      vW[c] = L_W + (TG ? wn * 49152 : 0) + (8 * q + qq) * 128 + ((((2 * cc) ^ swz) + (p >> 1)) * 16) + 8 * (p & 1);
    }
  }
  unsigned vD0 = L_D + (80 * wn + rho) * 128 + ((q ^ (rho & 7)) * 16), vD1 = vD0 ^ 64;
  asm volatile("" : "+v"(vX0), "+v"(vX1));
  if (!TG) asm volatile("" : "+v"(vD0), "+v"(vD1));
#pragma unroll
  for (int c = 0; c < NC; ++c) asm volatile("" : "+v"(vW[c]));
  f32x4 aG[NC][4], aU[NC][4], aA[4][5];
  if (TG) {
    unsigned ONE2 = 0x3f803f80u, SLO = 0x00003f80u, SHI = 0x3f800000u;
    asm volatile("" : "+v"(ONE2), "+v"(SLO), "+v"(SHI));
    float sx[2] = {0, 0}, sq[2] = {0, 0};
#pragma unroll
    for (int i = 0; i < 5; ++i)
#pragma unroll
      for (int e = 0; e < 4; ++e) { sx[e & 1] = dot2(vx[i][e], ONE2, sx[e & 1]); sq[e & 1] = dot2(vx[i][e], vx[i][e], sq[e & 1]); }
    const float mean = sum16(sx[0] + sx[1]) * (1.0f / D);
    const float var = fmaxf(sum16(sq[0] + sq[1]) * (1.0f / D) - mean * mean, 0.f);
    const float sd = rbf(__builtin_amdgcn_sqrtf(rbf(rbf(var) + 1e-5f)));
    float rso = (lane & 1) ? rbf(mean) : __builtin_amdgcn_rcpf(sd);
    auto colsum = [&](const u32x4* v) -> float {
      float c[8] = {0, 0, 0, 0, 0, 0, 0, 0};
#pragma unroll
      for (int i = 0; i < 5; ++i)
#pragma unroll
        for (int e = 0; e < 4; ++e) { c[2 * e] = dot2(v[i][e], SLO, c[2 * e]); c[2 * e + 1] = dot2(v[i][e], SHI, c[2 * e + 1]); }
      float d4[4], d2[2];
#pragma unroll
      for (int e = 0; e < 4; ++e) d4[e] = xchg32(c[e], c[4 + e]);
#pragma unroll
      for (int e = 0; e < 2; ++e) d2[e] = xchg16(d4[e], d4[2 + e]);
      d2[0] = dpp_add<0x128>(d2[0]); d2[1] = dpp_add<0x128>(d2[1]);
      return (lane & 8) ? d2[1] : d2[0];
    };
    float c1 = colsum(vw), c2; unsigned off2;
    if (w < 2) {
      c2 = colsum(vm);
      off2 = WS_CS + ((((8 + n8) * 4 + w) * 16 + mt) * 64 + 8 * c8 + rr) * 4;
    } else {
      float a = 0, b = 0;
#pragma unroll
      for (int i = 0; i < 10; ++i) { a = dot2(vm[i][0], ONE2, a); b = dot2(vm[i][1], ONE2, b); a = dot2(vm[i][2], ONE2, a); b = dot2(vm[i][3], ONE2, b); }
      c2 = sum8(a + b);
      off2 = (c8 == 0 && rr < 5) ? WS_CD + (n8 * 160 + 10 * mt + 5 * (w - 2) + rr) * 4 : 0xfffffff0u;
    }
    asm volatile("" : "+v"(c1), "+v"(c2), "+v"(rso));
    __builtin_amdgcn_raw_buffer_store_b32(__float_as_uint(c1), rw, WS_CS + (((n8 * 4 + w) * 16 + mt) * 64 + 8 * c8 + rr) * 4, 0, 16);
    __builtin_amdgcn_raw_buffer_store_b32(__float_as_uint(c2), rw, off2, 0, 16);
    __builtin_amdgcn_raw_buffer_store_b32(__float_as_uint(rso), rw, rho < 2 ? WS_RS + mt * 1024 + rho * 512 + (n8 * 16 + 4 * w + q) * 4 : 0xfffffff0u, 0, 16);
  }
  bf16x8 FX[2][4], FD[2][5];
  s16x4 FW[2][2 * NC][2];
  constexpr int NR = TG ? 20 : 17, NM = TG ? 32 : 36;
  auto rd1 = [&](int s, int buf, int ks, int r) {
    if (r >= 2 && r < 6) {
      const int i = r - 2;
      FX[s][i] = *(LAS bf16x8*)(smem + (ks ? vX1 : vX0) + buf * 16384 + (TG ? i * 2048 : (i >> 1) * 4096 + (i & 1) * 512));
    } else if (r < 4 + 4 * NC) {
      const int h = r < 2 ? r : r - 4, f = h >> 1;
      FW[s][f][h & 1] = __builtin_amdgcn_ds_read_tr16_b64_v4i16((LAS s16x4*)(smem + vW[f >> 1] + buf * 16384 + ks * 4096 + (f & 1) * 8192 + (h & 1) * 512));
    } else {
      const int jj = r - (4 + 4 * NC);
      FD[s][jj] = *(LAS bf16x8*)(smem + (ks ? vD1 : vD0) + buf * 20480 + jj * 2048);
    }
  };
  auto mm1 = [&](int s, int m, bool first) {
    const int f = m >> 2, i = (f & 1) ? 3 - (m & 3) : (m & 3);
    if (f < 2 * NC) {
      const bf16x8 wf = __builtin_shufflevector(FW[s][f][0], FW[s][f][1], 0, 1, 2, 3, 4, 5, 6, 7);
      mf((f & 1) ? aU[f >> 1][i] : aG[f >> 1][i], wf, FX[s][i], first);
    } else mf(aA[i][f - 2 * NC], FX[s][i], FD[s][f - 2 * NC], first);
  };
  auto seg = [&](int s, bool first, int m0, int m1, int rk, int buf, int ks) __attribute__((always_inline)) {
    int r = 0;
#pragma unroll
    for (int m = m0; m < m1; ++m) {
      mm1(s, m, first);
      if (rk == 2 && r == 0) r = 2;
      if (rk == 2 && r == 6) r = 4 + 4 * NC;
      const int rend = rk == 0 ? 0 : rk == 3 ? 4 + 4 * NC : NR;
      if (r < rend && (TG ? (m % 5 != 4) : (rk == 3 || m % 3 != 2))) { rd1(s ^ 1, buf, ks, r); ++r; }
      PIN();
    }
  };
  BAR();
  STAMP_AT(1);
#pragma unroll
  for (int r = 0; r < NR; ++r) rd1(0, 0, 0, r);
  PIN();
  unsigned wv[2][8];
  constexpr int KJ = TG ? 10 : 7;
  sfor<0, 10>([&](auto K) __attribute__((always_inline)) {
    constexpr int k = decltype(K)::v;
    constexpr int m0 = k < KJ ? 0 : 8 * NC;
    seg(0, k == 0, m0, NM, k < KJ ? 1 : 2, k % 3, 1);
    if (k == 1) {
      wait_v<0>();
      if (lane == 0) st_flag(FLAG(C_F0 + (mt * 16 + ns) * 4 + w), tag);
    }
  if (TG && k == WK) {
#pragma unroll
    for (int u = 0; u < 2; ++u) {
      const int task = min(((mt * 8 + ns) * 4 + w) * 2 + u, 959);
      const unsigned* src = (const unsigned*)(P.wdn + (size_t)(((task % 240) / 5) * 32 + 4 * q) * D + (task / 240) * 160 + (task % 5) * 32 + 2 * rho);
#pragma unroll
      for (int e = 0; e < 8; ++e) wv[u][e] = src[((e & 3) + (e >> 2) * 16) * (D / 2)];
    }
  }
  if (TG && k == WK + 2 && (mt * 8 + ns) * 4 + w < 480) {
    const bool up = rho & 8;
#pragma unroll
    for (int u = 0; u < 2; ++u) {
      u32x4 wo[2];
#pragma unroll
      for (int e = 0; e < 4; ++e) {
        const unsigned lo = __builtin_amdgcn_perm(wv[u][2 * e + 1], wv[u][2 * e], 0x05040100u), hi = __builtin_amdgcn_perm(wv[u][2 * e + 1], wv[u][2 * e], 0x07060302u);
        const unsigned t = __builtin_amdgcn_update_dpp(0, up ? lo : hi, 0x128, 0xf, 0xf, true);
        wo[0][e] = up ? t : lo;
        wo[1][e] = up ? hi : t;
      }
      const int task = ((mt * 8 + ns) * 4 + w) * 2 + u;
      const unsigned dst = WS_WD + (((task / 240) * 48 + (task % 240) / 5) * 10 + (task % 5) * 2) * 1024 + (2 * (rho & 7) + (rho >> 3) + 16 * q) * 16;
      st16c(rw, dst, wo[0]); st16c(rw, dst + 1024, wo[1]);
    }
  }
    if (k < 9) {
      WAIT_L0();
      BAR();
      PIN();
    }
    if (k < 9) seg(1, false, m0, NM, k + 1 < KJ ? 1 : 2, (k + 1) % 3, 0);
    else seg(1, false, m0, NM, TG ? 0 : 3, KJ % 3, 0);
    WAIT_L0(); PIN();
  });
  if (!TG) {
    MFMA_DONE();
    after_mfma(aA);
    asm volatile("" : "+v"(lane)); q = lane >> 4; rho = lane & 15;
    STAMP_AT(4);
    const float sgn = (n8 >= 4) ? -1.0f : 1.0f;
    float cs[5];
#pragma unroll
    for (int j = 0; j < 5; ++j) cs[j] = *(LAS float*)(smem + T_CD + (80 * wn + 16 * j + rho) * 4);
    const int kt = (n8 >= 4 ? 32 : 0) + mt * 2 + wm;
#pragma unroll
    for (int I = 0; I < 2; ++I) {
      f32x4 r4[2], m4[2];
#pragma unroll
      for (int t = 0; t < 2; ++t) {
        const int row = 64 * wm + 32 * I + 8 * q + 4 * t;
        r4[t] = *(LAS f32x4*)(smem + T_RS + row * 4) * sgn; m4[t] = *(LAS f32x4*)(smem + T_RM + row * 4);
      }
      const unsigned dst = WS_AB + ((((n8 & 3) * 64 + kt) * 2 + I) * 10 + 5 * wn) * 1024 + lane * 16;
#pragma unroll
      for (int j = 0; j < 5; ++j) {
        const f32x4 v0 = (aA[2 * I][j] - m4[0] * cs[j]) * r4[0], v1 = (aA[2 * I + 1][j] - m4[1] * cs[j]) * r4[1];
        const u32x4 o = {pk_bf16(v0[0], v0[1]), pk_bf16(v0[2], v0[3]), pk_bf16(v1[0], v1[1]), pk_bf16(v1[2], v1[3])};
        st16c(rw, dst + j * 1024, o);
      }
    }
    STAMP_AT(7);
    WAIT_L0(); PIN();
#pragma unroll
    for (int k = KJ; k < 10; ++k) {
      seg(0, false, 0, 8 * NC, 3, k % 3, 1);
      WAIT_L0(); PIN();
      if (k == KJ + AFL) {
        wait_v<0>();
        if (lane == 0) st_flag(FLAG(C_FAB + ((n8 & 3) * 64 + kt) * 2 + wn), tag);
        PIN();
      }
      seg(1, false, 0, 8 * NC, k < 9 ? 3 : 0, (k + 1) % 3, 0);
      WAIT_L0(); PIN();
    }
  }
  MFMA_DONE();
  after_mfma(aG); after_mfma(aU);
  asm volatile("" : "+v"(lane)); q = lane >> 4; rho = lane & 15;
  BAR();
  STAMP_AT(2);
  {
    const float K0 = -2.0f * 0.7978845608028654f * 1.4426950408889634f, K1 = K0 * 0.044715f;
    float mu[4], c0[4], c1[4], v2[4];
#pragma unroll
    for (int i = 0; i < 4; ++i) {
      const int row = TG ? 64 * wm + 16 * i + rho : 64 * wm + 32 * (i >> 1) + 8 * (rho >> 2) + 4 * (i & 1) + (rho & 3);
      const float rs = *(LAS float*)(smem + T_RS + row * 4);
      mu[i] = *(LAS float*)(smem + T_RM + row * 4);
      c0[i] = K0 * rs; c1[i] = K1 * rs * rs * rs; v2[i] = __builtin_amdgcn_rcpf(rs * rs);
    }
#pragma unroll
    for (int J = 0; J < NC / 2; ++J) {
      f32x4 cg[2], cu[2];
#pragma unroll
      for (int t = 0; t < 2; ++t) {
        const int c = 2 * J + t;
        const int o = TG ? (wn * 256 + c * 64 + q * 16) : ((2 * wn + c) * 64 + q * 16);
        cg[t] = *(LAS f32x4*)(smem + T_CS + o);
        cu[t] = *(LAS f32x4*)(smem + T_CS + (TG ? 512 : 256) + o);
      }
#pragma unroll
      for (int i = 0; i < 4; ++i) {
        u32x4 o;
#pragma unroll
        for (int t = 0; t < 2; ++t) {
          const f32x4 g = aG[2 * J + t][i] - cg[t] * mu[i];
          const f32x4 u = aU[2 * J + t][i] - cu[t] * mu[i];
          const f32x4 z = g * (g * g * c1[i] + c0[i]);
          f32x4 e;
#pragma unroll
          for (int r = 0; r < 4; ++r) e[r] = __builtin_amdgcn_exp2f(z[r]);
          e = e * v2[i] + v2[i];
#pragma unroll
          for (int r = 0; r < 4; ++r) e[r] = __builtin_amdgcn_rcpf(e[r]);
          const f32x4 hv = g * u * e;
          o[2 * t] = pk_bf16(hv[0], hv[1]); o[2 * t + 1] = pk_bf16(hv[2], hv[3]);
        }
        const int f0 = hcol0 + (TG ? wn * 64 + J * 32 : wn * 32);
        const int rb = TG ? 4 * wm + i : 4 * wm + 2 * (i >> 1) + (rho >> 3), pos = TG ? lane : 8 * ((rho >> 2) & 1) + 4 * (i & 1) + (rho & 3) + 16 * q;
        st16c(rw, WS_H + (((mt * 24 + (f0 >> 6)) * 8 + rb) * 2 + ((f0 >> 5) & 1)) * 1024 + pos * 16, o);
      }
    }
  }
  STAMP_AT(3);
}

DI void mfma_p2(const Params& P, LAS char* smem, int lane, int w, int split, int mt, int nt, int mt1, int ns, u64 tag) {
  asm volatile("" : "+v"(lane));
  const int wm = w & 1, wn = w >> 1, q = lane >> 4, r15 = lane & 15;
  unsigned vY0 = wm * 8192 + r15 * 128 + ((q ^ (r15 & 7)) * 16), vY1 = vY0 ^ 64;
  unsigned vB0 = L_B + wn * 5120 + lane * 16, vB2 = vB0 + 2 * B_ST;
  const int rt = 2 * wm + wn;
  unsigned vH0 = wn * 8192 + rt * 2048 + lane * 16, vH1 = vH0 ^ 2048, vW0 = L_B + wn * 10240 + lane * 16, vW2 = vW0 + 2 * B_ST;
  asm volatile("" : "+v"(vY0), "+v"(vY1), "+v"(vB0), "+v"(vB2), "+v"(vH0), "+v"(vH1), "+v"(vW0), "+v"(vW2));
  const __amdgpu_buffer_rsrc_t rw = __builtin_amdgcn_make_buffer_rsrc(P.ws, 0, WS_END, 0x00020000);
  const int tile = mt * 4 + nt;
  f32x4 acc[4][5], dac[2][5];
  bf16x8 FA[2][4], FB[2][5], GA[2][4], GB[2][10];
  auto rdy = [&](int s, int k, int ks, int r) {
    const int buf = k & 3;
    if (r >= 1 && r < 5) FA[s][r - 1] = *(LAS bf16x8*)(smem + (ks ? vY1 : vY0) + buf * A_ST + (r - 1) * 2048);
    else { const int j = r ? r - 4 : 0; FB[s][j] = *(LAS bf16x8*)(smem + (buf >= 2 ? vB2 : vB0) + (buf & 1) * B_ST + ks * 10240 + j * 1024); }
  };
  auto rdd = [&](int k, int r) {
    const int buf = k & 3, u = r / 7, j = r % 7;
    if (j == 1 || j == 2) GA[k & 1][u * 2 + j - 1] = *(LAS bf16x8*)(smem + (j == 2 ? vH1 : vH0) + buf * A_ST + u * 1024);
    else { const int jj = j ? j - 2 : 0; GB[k & 1][u * 5 + jj] = *(LAS bf16x8*)(smem + (buf >= 2 ? vW2 : vW0) + (buf & 1) * B_ST + u * 5120 + jj * 1024); }
  };
  auto seg = [&](auto K, auto HF) __attribute__((always_inline)) {
    constexpr int k = decltype(K)::v, hf = decltype(HF)::v, rk = k + hf, rh = hf ^ 1;
    constexpr int nr = rk >= NK2 ? 0 : is_d(rk) ? 14 : 9;
    int r = 0;
#pragma unroll
    for (int m = 0; m < 20; ++m) {
      if constexpr (is_d(k)) { const int i = (m ^ (m >> 1)) & 1; mf(dac[i][(m >> 1) % 5], GB[k & 1][m >> 1], GA[k & 1][(m / 10) * 2 + i], k == KD0 && m < 10); }
      else { const int i = (m & 4) ? 3 - (m & 3) : (m & 3); mf(acc[i][m >> 2], FB[hf][m >> 2], FA[hf][i], k == 0 && hf == 0); }
      if (r < nr && (nr == 14 || m % 3 != 2)) { if constexpr (nr == 14) rdd(rk, r); else rdy(rh, rk, rh, r); ++r; }
      PIN();
    }
  };
  auto publish = [&](int i) {
    unsigned pk[10];
#pragma unroll
    for (int j = 0; j < 5; ++j) { pk[2 * j] = pk_bf16(acc[i][j][0], acc[i][j][1]); pk[2 * j + 1] = pk_bf16(acc[i][j][2], acc[i][j][3]); }
    if (w == split) {
      LAS char* dst = smem + F_Y + i * 2560;
      *(LAS u32x4*)(dst + lane * 16) = (u32x4){pk[0], pk[1], pk[2], pk[3]};
      *(LAS u32x4*)(dst + 1024 + lane * 16) = (u32x4){pk[4], pk[5], pk[6], pk[7]};
      *(LAS u32x2*)(dst + 2048 + lane * 8) = (u32x2){pk[8], pk[9]};
    } else {
      const unsigned dst = WS_P + (((tile * 4 + w) * 3 + (split ^ w) - 1) * 4 + i) * 2560;
      st16c(rw, dst + lane * 16, (u32x4){pk[0], pk[1], pk[2], pk[3]});
      st16c(rw, dst + 1024 + lane * 16, (u32x4){pk[4], pk[5], pk[6], pk[7]});
      __builtin_amdgcn_raw_buffer_store_b64((u32x2){pk[8], pk[9]}, rw, dst + 2048 + lane * 8, 0, 16);
    }
    PIN();
  };
  const int orow = mt * 128 + (split & 1) * 64 + rt * 16, ocol = nt * 160 + (split >> 1) * 80;
  const u64* fp = FLAG(C_FP + (tile * 4 + split) * 4 + min(lane, 2));
  u64 fpv = 0;
  u32x4 xv[3], pa[3][2]; u32x2 pb[3];
  f32x4 ys[5];
  BAR();
  STAMP_AT(5);
#pragma unroll
  for (int r = 0; r < 9; ++r) rdy(0, 0, 0, r);
  PIN();
  sfor<0, NK2>([&](auto K) __attribute__((always_inline)) {
    constexpr int k = decltype(K)::v;
    if constexpr (!is_d(k)) seg(K, IC<0>{});
    if (k == HFL) {
      wait_v<0>();
      if (lane == 0) st_flag(FLAG(C_FH + mt1 * 64 + ns * 4 + w), tag);
    }
    if (k == KYL + YFL && w != split) {
      wait_v<0>();
      if (lane == 0) st_flag(FLAG(C_FP + (tile * 4 + w) * 4 + (split ^ w) - 1), tag);
    }
    if (k == KYL + YPL) fpv = ld_flag(fp);
    if (k == KYL + YPL + 1) {
      if (!__all(fpv == tag)) wait_flags(fp, tag);
      asm volatile("" ::: "memory");
#pragma unroll
      for (int s = 0; s < 3; ++s) {
        const unsigned src = WS_P + (((tile * 4 + split) * 3 + s) * 4 + rt) * 2560;
        pa[s][0] = ld16c(rw, src + lane * 16); pa[s][1] = ld16c(rw, src + 1024 + lane * 16);
        pb[s] = __builtin_amdgcn_raw_buffer_load_b64(rw, src + 2048 + lane * 8, 0, 16);
      }
    }
    if (k == KYL - 1) {
#pragma unroll
      for (int i = 0; i < 3; ++i) {
        const int n = min(i * 64 + lane, 159);
        xv[i] = *(const u32x4*)(P.x + (size_t)(orow + n / 10) * D + ocol + (n % 10) * 8);
      }
    }
    if (k == NK2 - YSB) {
      LAS char* po = smem + F_Y + rt * 2560;
      const u32x4 oa0 = *(LAS u32x4*)(po + lane * 16), oa1 = *(LAS u32x4*)(po + 1024 + lane * 16);
      const u32x2 ob = *(LAS u32x2*)(po + 2048 + lane * 8);
#pragma unroll
      for (int j = 0; j < 5; ++j) {
        const unsigned l0 = j < 2 ? oa0[(j & 1) * 2] : j < 4 ? oa1[(j & 1) * 2] : ob[0], h0 = j < 2 ? oa0[(j & 1) * 2 + 1] : j < 4 ? oa1[(j & 1) * 2 + 1] : ob[1];
        ys[j] = (f32x4){bflo(l0), bfhi(l0), bflo(h0), bfhi(h0)};
#pragma unroll
        for (int s = 0; s < 3; ++s) {
          const unsigned lo = j < 4 ? pa[s][j >> 1][(j & 1) * 2] : pb[s][0], hi = j < 4 ? pa[s][j >> 1][(j & 1) * 2 + 1] : pb[s][1];
          ys[j] += (f32x4){bflo(lo), bfhi(lo), bflo(hi), bfhi(hi)};
        }
        asm volatile("" : "+v"(ys[j]));
      }
    }
    if (k + 1 < NK2) {
      WAIT_L0();
      BAR();
      PIN();
    }
    seg(K, IC<1>{});
    WAIT_L0(); PIN();
    if (k == KYL) {
      MFMA_DONE();
      after_mfma(acc);
      publish(0); publish(1);
      if (!PSPL) { publish(2); publish(3); }
    }
    if (k == KYL + 1) {
      if (PSPL) { publish(2); publish(3); }
    }
  });
  MFMA_DONE();
  after_mfma(dac);
  STAMP_AT(6);
#pragma unroll
  for (int j = 0; j < 5; ++j) *(LAS f32x4*)(smem + F_R + (w * 5 + j) * 1024 + lane * 16) = dac[1][j];
  WAIT_L0();
  BAR();
#pragma unroll
  for (int j = 0; j < 5; ++j) dac[0][j] += *(LAS f32x4*)(smem + F_R + ((w ^ 2) * 5 + j) * 1024 + lane * 16);
  LAS char* pt = smem + F_T + w * (16 * FPITCH);
#pragma unroll
  for (int j = 0; j < 5; ++j) {
    const f32x4 d = dac[0][j];
    f32x4 e = d * -1.4426950408889634f;
#pragma unroll
    for (int r = 0; r < 4; ++r) e[r] = __builtin_amdgcn_exp2f(e[r]);
    e = e + 1.0f;
#pragma unroll
    for (int r = 0; r < 4; ++r) e[r] = __builtin_amdgcn_rcpf(e[r]);
    e = e * d * ys[j];
    *(LAS u32x2*)(pt + r15 * FPITCH + j * 32 + q * 8) = (u32x2){pk_bf16(e[0], e[1]), pk_bf16(e[2], e[3])};
  }
  WAIT_L0();
#pragma unroll
  for (int i = 0; i < 3; ++i) {
    const int n = i * 64 + lane, nc = min(n, 159);
    const u32x4 v = *(LAS u32x4*)(pt + (nc / 10) * FPITCH + (nc % 10) * 16);
    u32x4 o;
#pragma unroll
    for (int d = 0; d < 4; ++d) o[d] = pk_bf16(bflo(v[d]) + bflo(xv[i][d]), bfhi(v[d]) + bfhi(xv[i][d]));
    if (n < 160) *(u32x4*)(P.out + (size_t)(orow + nc / 10) * D + ocol + (nc % 10) * 8) = o;
  }
}

__global__ __launch_bounds__(512, 2)
void fused(Params P) {
  extern __shared__ __attribute__((aligned(16))) char smem_[];
  LAS char* smem = (LAS char*)smem_;
  const int tid = threadIdx.x, lane = tid & 63, w = __builtin_amdgcn_readfirstlane(tid >> 6);
  const int bid = blockIdx.x, xcd = bid & 7, idx = bid >> 3;
  u64 tag;
  STAMP_AT(0);
  const int mt1 = (xcd & 1) * 8 + (idx & 7), j = idx >> 3, ns = (j & 1) * 8 + (xcd >> 1) * 2 + (j >> 1);
  const int split = xcd >> 1, mt2 = (xcd & 1) * 8 + (idx >> 2), nt = idx & 3;
  if (w >= 4) {
    __builtin_amdgcn_s_setprio(HPRIO);
    helper(P, smem, lane, w & 3, mt1, ns, split, mt2, nt);
  } else {
    if (j & 1) mfma_p1<false>(P, smem, lane, w, mt1, ns, tag); else mfma_p1<true>(P, smem, lane, w, mt1, ns, tag);
    mfma_p2(P, smem, lane, w, split, mt2, nt, mt1, ns, tag);
    __builtin_amdgcn_s_waitcnt(0);
  }
}

extern "C" void kernel_launch(void* const* d_in, const int* in_sizes, int n_in, void* d_out, int out_size, void* d_ws, size_t ws_size, hipStream_t stream) {
  Params P;
  P.x = (const bf16_t*)d_in[0]; P.wsr = (const bf16_t*)d_in[1]; P.wsi = (const bf16_t*)d_in[2];
  P.wdr = (const bf16_t*)d_in[3]; P.wdi = (const bf16_t*)d_in[4];
  P.wg = (const bf16_t*)d_in[6]; P.wu = (const bf16_t*)d_in[7]; P.wdn = (const bf16_t*)d_in[8];
  P.out = (bf16_t*)d_out; P.ws = (char*)d_ws;
  (void)hipFuncSetAttribute((const void*)fused, hipFuncAttributeMaxDynamicSharedMemorySize, LDS_BYTES);
  fused<<<256, 512, LDS_BYTES, stream>>>(P);
}
```

```cpp
#include <hip/hip_runtime.h>
#include <hip/hip_bf16.h>
typedef unsigned short bf16_t;
typedef unsigned long long u64;
typedef short bf16x8 __attribute__((ext_vector_type(8)));
typedef short s16x4 __attribute__((ext_vector_type(4)));
typedef float f32x4 __attribute__((ext_vector_type(4)));
typedef float f32x2 __attribute__((ext_vector_type(2)));
typedef unsigned u32x4 __attribute__((ext_vector_type(4)));
typedef unsigned u32x2 __attribute__((ext_vector_type(2)));
typedef __bf16 bf16x2_t __attribute__((ext_vector_type(2)));
#define LAS __attribute__((address_space(3)))
#define GAS __attribute__((address_space(1)))
#define DI __device__ __forceinline__

#define STAMP 1
#define NPRE 2
#define ROT2 0
#define YFL 4
#define FABSL 1
#define AFL 2
#define HFL 2
#define PSPL 1
#define XG 2
#define XY 1
#define XD 1
#define YSB 2
#define WK 3
#define FABM 0
#define SK 5
#define YPL 8
#define XROT 0
#define HPRIO 0
#define PFN 4

constexpr int S = 2048, D = 640, F = 1536;
constexpr unsigned WS_AB = 0, WS_H = WS_AB + 2 * S * D * 2, WS_P = WS_H + S * F * 2, WS_WD = WS_P + 3 * S * D * 2, WS_RS = WS_WD + F * D * 2,
                   WS_CS = WS_RS + S * 8, WS_CD = WS_CS + 16 * 4 * 16 * 64 * 4, WS_CTRL = WS_CD + 8192, WS_END = WS_CTRL + 5120 * 128;
constexpr int C_E = 0, C_F0 = 1, C_FAB = C_F0 + 1024, C_FH = C_FAB + 512, C_FP = C_FH + 1024;
#define FLAG(i) ((u64*)(P.ws + WS_CTRL) + (i) * 16)

DI unsigned pk_bf16(float lo, float hi) { f32x2 v = {lo, hi}; return __builtin_bit_cast(unsigned, __builtin_convertvector(v, bf16x2_t)); }
DI float rbf(float f) { return __uint_as_float(pk_bf16(f, f) << 16); }
DI float bflo(unsigned u) { return __uint_as_float(u << 16); }
DI float bfhi(unsigned u) { return __uint_as_float(u & 0xffff0000u); }
DI float dot2(unsigned a, unsigned b, float c) { return __builtin_amdgcn_fdot2_f32_bf16(__builtin_bit_cast(bf16x2_t, a), __builtin_bit_cast(bf16x2_t, b), c, false); }
#define GLDS4(POL) asm volatile("s_mov_b32 m0, %0\n\ts_nop 0\n\tglobal_load_lds_dwordx4 %2, %1" POL "\n\tglobal_load_lds_dwordx4 %3, %1 offset:1024" POL \
    "\n\tglobal_load_lds_dwordx4 %4, %1 offset:2048" POL "\n\tglobal_load_lds_dwordx4 %5, %1 offset:3072" POL \
    :: "s"(lds), "s"(base), "v"(v0), "v"(v1), "v"(v2), "v"(v3) : "memory")
#define GLDS5(POL) asm volatile("s_mov_b32 m0, %0\n\ts_nop 0\n\tglobal_load_lds_dwordx4 %2, %1" POL "\n\tglobal_load_lds_dwordx4 %3, %1 offset:1024" POL \
    "\n\tglobal_load_lds_dwordx4 %4, %1 offset:2048" POL "\n\tglobal_load_lds_dwordx4 %5, %1 offset:3072" POL \
    "\n\ts_add_u32 m0, %0, 0x1000\n\ts_nop 0\n\tglobal_load_lds_dwordx4 %6, %1" POL \
    :: "s"(lds), "s"(base), "v"(v0), "v"(v1), "v"(v2), "v"(v3), "v"(v4) : "memory", "scc")
template <bool DEV> DI void glds4(unsigned lds, const char* base, unsigned v0, unsigned v1, unsigned v2, unsigned v3) {
  if (DEV) GLDS4(" sc1"); else GLDS4("");
}
template <bool DEV> DI void glds5(unsigned lds, const char* base, unsigned v0, unsigned v1, unsigned v2, unsigned v3, unsigned v4) {
  if (DEV) GLDS5(" sc1"); else GLDS5("");
}
DI void touch_lines(unsigned lds, unsigned voff, const char* base) {
  asm volatile("s_mov_b32 m0, %0\n\ts_nop 0\n\tglobal_load_lds_dword %1, %2" :: "s"(lds), "v"(voff), "s"(base) : "memory");
}
template <int N> DI void wait_v() { asm volatile("s_waitcnt vmcnt(%0)" :: "i"(N) : "memory"); }
#define WAIT_L0() do { __builtin_amdgcn_s_waitcnt(0xC07F); asm volatile("" ::: "memory"); } while (0)
#define BAR() do { asm volatile("" ::: "memory"); __builtin_amdgcn_s_barrier(); asm volatile("" ::: "memory"); } while (0)
#define SGB(m, n) __builtin_amdgcn_sched_group_barrier(m, n, 0)
#define PIN() __builtin_amdgcn_sched_barrier(0)
template <int V> struct IC { static constexpr int v = V; };
template <int I, int N, class Fn> DI void sfor(Fn&& f) { if constexpr (I < N) { f(IC<I>{}); sfor<I + 1, N>(f); } }
DI void mf(f32x4& c, const bf16x8& a, const bf16x8& b, bool first) {
  if (first) asm volatile("v_mfma_f32_16x16x32_bf16 %0, %1, %2, 0" : "=&v"(c) : "v"(a), "v"(b));
  else asm volatile("v_mfma_f32_16x16x32_bf16 %0, %1, %2, %0" : "+v"(c) : "v"(a), "v"(b));
}
#define MFMA_DONE() asm volatile("s_nop 15")
template <int A, int B> DI void after_mfma(f32x4 (&c)[A][B]) {
#pragma unroll
  for (int i = 0; i < A; ++i)
#pragma unroll
    for (int j = 0; j < B; ++j) asm volatile("" : "+v"(c[i][j]));
}
#define STAMP_AT(i) do { if ((threadIdx.x & 63) == 0 && (threadIdx.x >> 6) == ((i) >= 8 ? 4 : 0)) { P.stamps[blockIdx.x * 32 + 2 * (i)] = __builtin_amdgcn_s_memrealtime(); P.stamps[blockIdx.x * 32 + 2 * (i) + 1] = __builtin_amdgcn_s_memtime(); } } while (0)

template <int CTRL> DI float dpp_add(float v) { return v + __builtin_bit_cast(float, __builtin_amdgcn_update_dpp(0, __builtin_bit_cast(int, v), CTRL, 0xf, 0xf, true)); }
DI float sum8(float v) { v = dpp_add<0xB1>(v); v = dpp_add<0x4E>(v); return dpp_add<0x141>(v); }
DI float sum16(float v) { return dpp_add<0x140>(sum8(v)); }
DI float xchg16(float keep_lo, float keep_hi) {
  const auto r = __builtin_amdgcn_permlane16_swap(__float_as_uint(keep_lo), __float_as_uint(keep_hi), false, false);
  return __uint_as_float(r[0]) + __uint_as_float(r[1]);
}
DI float xchg32(float keep_lo, float keep_hi) {
  const auto r = __builtin_amdgcn_permlane32_swap(__float_as_uint(keep_lo), __float_as_uint(keep_hi), false, false);
  return __uint_as_float(r[0]) + __uint_as_float(r[1]);
}
#define TIC(v)
#define ACC(a, x, y)
#define PUT(slot, a)
DI u64 ld_flag(const u64* p) { return __hip_atomic_load((const GAS u64*)p, __ATOMIC_RELAXED, __HIP_MEMORY_SCOPE_AGENT); }
DI void st_flag(u64* p, u64 v) { __hip_atomic_store((GAS u64*)p, v, __ATOMIC_RELAXED, __HIP_MEMORY_SCOPE_AGENT); }
DI void wait_flags(const u64* p, u64 tag) {
#pragma unroll 1
  for (int it = 0; it < 60000; ++it) {
    if (__all(ld_flag(p) == tag)) break;
    __builtin_amdgcn_s_sleep(4);
  }
  asm volatile("" ::: "memory");
}
DI void wait_flags2(const u64* p, const u64* p2, u64 tag) {
#pragma unroll 1
  for (int it = 0; it < 60000; ++it) {
    const u64 a = ld_flag(p), b = ld_flag(p2);
    if (__all(a == tag && b == tag)) break;
    __builtin_amdgcn_s_sleep(4);
  }
  asm volatile("" ::: "memory");
}
DI u64 read_tag(const char* ws) {
  u64 e;
  asm volatile("s_load_dwordx2 %0, %1, 0x0\n\ts_waitcnt lgkmcnt(0)" : "=s"(e) : "s"(ws + WS_CTRL) : "memory");
  return e * 6364136223846793005ull + 1442695040888963407ull;
}
DI void st16c(__amdgpu_buffer_rsrc_t r, unsigned off, u32x4 v) { __builtin_amdgcn_raw_buffer_store_b128(v, r, off, 0, 16); }
DI u32x4 ld16c(__amdgpu_buffer_rsrc_t r, unsigned off) { return __builtin_amdgcn_raw_buffer_load_b128(r, off, 0, 16); }

struct Params {
  const bf16_t *x, *wsr, *wsi, *wdr, *wdi, *wg, *wu, *wdn;
  bf16_t* out; char* ws;
  u64* stamps;
};

constexpr int L_W = 49152, L_D = 98304, K1_TAB = 159744, LDS_BYTES = K1_TAB + 4096;
constexpr int T_RS = K1_TAB, T_RM = K1_TAB + 512, T_CS = K1_TAB + 1024, T_CD = K1_TAB + 2048, T_GO = K1_TAB + 3584, T_PF = K1_TAB + 3072;
constexpr int A_ST = 16384, B_ST = 20480, L_B = 4 * A_ST;

#define DMASK 0xFFF0000u
constexpr int NK2 = 28;
constexpr bool is_d(int k) { return (DMASK >> k) & 1; }
constexpr int first_d() { int k = 0; while (!is_d(k)) ++k; return k; }
constexpr int last_y() { int k = NK2 - 1; while (is_d(k)) --k; return k; }
constexpr int KD0 = first_d(), KYL = last_y();
constexpr int d_index(int k) { int n = 0; for (int i = 0; i < k; ++i) n += is_d(i); return n; }

constexpr int F_Y = 147456, F_T = 0, F_R = 12288, FPITCH = 176;

DI void helper(const Params& P, LAS char* smem, int lane, int lw, int mt, int ns, int split, int mt2, int nt) {
  constexpr int NL = 12;
  const bool TG = ns < 8;
  const int n8 = ns & 7, hcol0 = TG ? 128 * n8 : 1024 + 64 * n8;
  const int rr = lane >> 3, c8 = lane & 7;
  const unsigned lds0 = (unsigned)(size_t)smem;
  unsigned vX[4], vW[4], vD[5];
#pragma unroll
  for (int j = 0; j < 4; ++j) {
    vX[j] = (32 * lw + 8 * j + rr) * 1280 + ((c8 ^ (TG ? rr : ((rr & 2) | ((j & 2) << 1)))) * 16) - j * 1024;
    vW[j] = (8 * j + rr) * 3072 + ((c8 ^ ((rr & 2) | ((j & 1) << 2))) * 16) - j * 1024;
  }
#pragma unroll
  for (int j = 0; j < 5; ++j) vD[j] = (40 * lw + 8 * j + rr) * 1280 + ((c8 ^ rr) * 16) - (j & 3) * 1024;
  const char* bx = (const char*)P.x + (size_t)mt * 128 * 1280;
  const char* bw = (const char*)((lw >> 1) ? P.wu : P.wg) + hcol0 * 2 + (TG ? (lw & 1) * 128 : (lw & 1) * (32 * 3072));
  const char* bd = (const char*)(n8 >= 4 ? P.wdi : P.wdr) + (size_t)(n8 & 3) * 160 * 1280;
  const unsigned ldsX = __builtin_amdgcn_readfirstlane(lds0 + lw * 4096);
  const unsigned ldsW = __builtin_amdgcn_readfirstlane(lds0 + L_W + (TG ? (lw & 1) * 49152 + (lw >> 1) * 8192 : lw * 4096));
  const unsigned ldsD = __builtin_amdgcn_readfirstlane(lds0 + L_D + lw * 5120);
  auto issue = [&](int kt) {
    const int buf = kt % 3;
    glds4<false>(ldsX + buf * 16384, bx + kt * 128, vX[0], vX[1], vX[2], vX[3]);
    const char* pw = bw + kt * (64 * 3072);
    glds4<false>(ldsW + buf * 16384, pw, vW[0], vW[1], vW[2], vW[3]);
    if (TG) glds4<false>(ldsW + buf * 16384 + 4096, pw + 32 * 3072, vW[0], vW[1], vW[2], vW[3]);
    else glds5<false>(ldsD + buf * 20480, bd + kt * 128, vD[0], vD[1], vD[2], vD[3], vD[4]);
  };
  issue(0); issue(1);
  const u64 tag = read_tag(P.ws);
  const char* pfb = (const char*)(split < 2 ? P.wsr : P.wsi) + (size_t)(mt2 * 128 + nt * 32) * 4096 + (split & 1) * 2048;
  const unsigned pfv = (lane & 31) * 4096 + ((lw & 1) * 2 + (lane >> 5)) * 128;
  const u64* fa = FLAG(C_F0 + (lw == 0 ? mt * 64 + (lane & 31) : (((lane >> 1) & 15) * 16 + n8) * 4 + 2 + (lane & 1)));
  u64 fav = 0;
  const int xg = ((mt2 ^ nt) & 1) * XG;
  const unsigned vL = lane * 16, vL4 = vL + 4096;
  const char* baseWD = P.ws + WS_WD + ((nt * 48 + lw) * 10 + (split >> 1) * 5) * 1024;
  sfor<0, 8>([&](auto K) __attribute__((always_inline)) {
    constexpr int k = decltype(K)::v;
    if (TG) wait_v<12>(); else wait_v<13>();
    BAR();
    if (lw < (TG ? 1 : 2)) {
      if (k == SK) asm volatile("global_load_dwordx2 %0, %1, off sc1" : "+v"(fav) : "v"(fa) : "memory");
      if (k == SK + 1) {
        asm volatile("" : "+v"(fav));
        if (!__all(fav == tag)) wait_flags(fa, tag);
        const char* src = P.ws + (lw == 0 ? WS_RS + mt * 1024 : WS_CD + n8 * 640);
        asm volatile("s_mov_b32 m0, %0\n\ts_nop 0\n\tglobal_load_lds_dwordx4 %1, %2 sc1" :: "s"(lds0 + (lw == 0 ? T_RS : T_CD)), "v"(vL), "s"(src) : "memory");
      }
    }
    issue(k + 2);
    if (PFN && (k >> 1) < PFN && (k & 1) == (mt & 1) && lw >= 2)
      touch_lines(lds0 + T_PF, pfv, pfb + (k >> 1) * 512);
  });
  wait_v<NL>();
  BAR();
  wait_v<0>();
  BAR();
  if (TG || lw < 2) {
    wait_flags(FLAG(C_F0 + ((lane & 15) * 16 + n8) * 4 + lw), tag);
    const __amdgpu_buffer_rsrc_t rw = __builtin_amdgcn_make_buffer_rsrc(P.ws, 0, WS_END, 0x00020000);
    f32x4 c = {0, 0, 0, 0};
#pragma unroll
    for (int i = 0; i < 4; ++i) c += __builtin_bit_cast(f32x4, ld16c(rw, WS_CS + (ns * 4 + lw) * 4096 + i * 1024 + lane * 16));
#pragma unroll
    for (int e = 0; e < 4; ++e) { c[e] = xchg16(c[e], c[e]); c[e] = xchg32(c[e], c[e]); }
    if (lane < 16) *(LAS f32x4*)(smem + T_CS + lw * 256 + lane * 16) = c;
  }
  if (lw == 0 && lane == 0) *(volatile LAS unsigned*)(smem + T_GO) = 0;
  WAIT_L0();
  if (blockIdx.x == 64 && lw == 3) {
    wait_flags2(FLAG(C_F0 + lane * 4), FLAG(C_F0 + (64 + lane) * 4), tag);
    wait_flags2(FLAG(C_F0 + (128 + lane) * 4), FLAG(C_F0 + (192 + lane) * 4), tag);
    if (lane == 0) st_flag(FLAG(C_E), tag);
  }
  const u64* fab = FLAG(C_FAB + (nt * 64 + split * 16) * 2 + (lane & 31));
  unsigned vY[4];
#pragma unroll
  for (int j = 0; j < 4; ++j) vY[j] = (32 * lw + 8 * j + rr) * 4096 + ((c8 ^ rr) * 16) - j * 1024;
  const char* baseY = (const char*)(split < 2 ? P.wsr : P.wsi) + (size_t)mt2 * 128 * 4096 + (split & 1) * 2048;
  const char* baseAB = P.ws + WS_AB + (nt * 64 + split * 16) * B_ST + lw * 5120;
  const char* baseH = P.ws + WS_H + ((mt2 * 24 + (lw >> 1)) * 16 + (split & 1) * 8 + (lw & 1) * 4) * 1024;
  const unsigned ldsA = ldsX, ldsB = __builtin_amdgcn_readfirstlane(lds0 + L_B + lw * 5120);
  auto issueYA = [&](int k, int yi) { glds4<false>(ldsA + (k & 3) * A_ST, baseY + (yi ^ xg) * 128, vY[0], vY[1], vY[2], vY[3]); };
  auto issueYB = [&](int k, int yi) { glds5<true>(ldsB + (k & 3) * B_ST, baseAB + (yi ^ xg) * B_ST, vL, vL, vL, vL, vL4); };
  BAR();
  STAMP_AT(8);
  u64 fabv;
  if (FABM) asm volatile("global_load_dwordx2 %0, %1, off sc1" : "=&v"(fabv) : "v"(fab) : "memory"); else fabv = ~tag;
  issueYA(0, 0); issueYA(1, 1);
  if (FABM) wait_v<8>();
  asm volatile("" : "+v"(fabv));
  const bool ab_seen = __all(fabv == tag);
  if (lw == 0) {
    if (!ab_seen) wait_flags(fab, tag);
    if (lane == 0) *(volatile LAS unsigned*)(smem + T_GO) = 1;
  } else if (!ab_seen) {
#pragma unroll 1
    for (int it = 0; it < 4000000; ++it) {
      if (*(volatile LAS unsigned*)(smem + T_GO) != 0) break;
      __builtin_amdgcn_s_sleep(1);
    }
  }
  asm volatile("" ::: "memory");
  STAMP_AT(9);
  const u64* fh = FLAG(C_FH + mt2 * 64 + (lane & 31) * 2 + (split & 1));
  const int l36 = min(lane, 35);
  const int wt = (nt * 240 + ((l36 / 3) * 4 + lw) * 5 + (split >> 1) * 2 + l36 % 3) >> 1;
  const u64* fw = FLAG(C_FH + (wt >> 5) * 64 + (wt & 31));
  u64 fhv = 0, fwv = 0;
  issueYB(0, 0); issueYB(1, 1);
  wait_v<5>(); BAR();
  issueYA(2, 2); issueYB(2, 2);
  wait_v<9>(); BAR();
  issueYA(3, 3); issueYB(3, 3); issueYA(4, 4); issueYB(4, 4);
  int yi = 5, di = 0;
#pragma unroll 1
  for (int k = 2; k < NK2 - 3; ++k) {
    wait_v<18>();
    BAR();
    if (k == KD0 - 6) asm volatile("global_load_dwordx2 %0, %2, off sc1\n\tglobal_load_dwordx2 %1, %3, off sc1" : "+v"(fhv), "+v"(fwv) : "v"(fh), "v"(fw) : "memory");
    if (k == KD0 - 3) {
      asm volatile("" : "+v"(fhv), "+v"(fwv));
      if (!__all(fhv == tag && fwv == tag)) wait_flags2(fh, fw, tag);
    }
    if ((DMASK >> (k + 3)) & 1) {
      glds4<true>(ldsA + ((k + 3) & 3) * A_ST, baseH + (di ^ xg) * (2 * A_ST), vL, vL, vL, vL);
      glds5<true>(ldsB + ((k + 3) & 3) * B_ST, baseWD + (di ^ xg) * (2 * B_ST), vL, vL, vL, vL, vL4);
      ++di;
    } else { issueYA(k + 3, yi); issueYB(k + 3, yi); ++yi; }
  }
  wait_v<18>(); BAR();
  wait_v<9>(); BAR();
  wait_v<0>(); BAR();
  STAMP_AT(10);
}

template <bool TG>
DI void mfma_p1(const Params& P, LAS char* smem, int lane, int w, int mt, int ns, u64& tag) {
  const int n8 = ns & 7, hcol0 = TG ? 128 * n8 : 1024 + 64 * n8;
  const __amdgpu_buffer_rsrc_t rw = __builtin_amdgcn_make_buffer_rsrc(P.ws, 0, WS_END, 0x00020000);
  int q = lane >> 4, rho = lane & 15;
  const int rr = lane >> 3, c8 = lane & 7;
  u32x4 vx[5], vw[5], vm[10];
  if (TG) {
    const char* px = (const char*)P.x + (size_t)(mt * 128 + n8 * 16 + 4 * w + q) * 1280 + rho * 16;
#pragma unroll
    for (int i = 0; i < 5; ++i) vx[i] = *(const u32x4*)(px + i * 256);
    const char* pw = (const char*)((w >> 1) ? P.wu : P.wg) + (size_t)(40 * mt + rr) * 3072 + (hcol0 + (w & 1) * 64) * 2 + c8 * 16;
#pragma unroll
    for (int i = 0; i < 5; ++i) vw[i] = *(const u32x4*)(pw + i * (8 * 3072));
    if (w < 2) {
      const char* pm = (const char*)(w ? P.wu : P.wg) + (size_t)(40 * mt + rr) * 3072 + (1024 + 64 * n8) * 2 + c8 * 16;
#pragma unroll
      for (int i = 0; i < 5; ++i) vm[i] = *(const u32x4*)(pm + i * (8 * 3072));
    } else {
      const int e = (n8 & 3) * 160 + 10 * mt + 5 * (w - 2) + min(rr, 4);
      const char* pd = (const char*)(n8 >= 4 ? P.wdi : P.wdr) + (size_t)e * 1280 + c8 * 16;
#pragma unroll
      for (int i = 0; i < 10; ++i) vm[i] = *(const u32x4*)(pd + i * 128);
    }
  }
  tag = read_tag(P.ws);
  const int wm = w & 1, wn = w >> 1;
  constexpr int NC = TG ? 4 : 2;
  unsigned vX0 = TG ? wm * 8192 + rho * 128 + ((q ^ (rho & 7)) * 16)
                          : wm * 8192 + (8 * (rho >> 2) + (rho & 3)) * 128 + ((q ^ ((rho & 2) | ((rho >> 1) & 4))) * 16);
  unsigned vX1 = vX0 ^ 64;
  unsigned vW[NC];
  {
    const int qq = rho >> 2, p = lane & 3, swz = (qq & 2) | ((q & 1) << 2);
#pragma unroll
    for (int c = 0; c < NC; ++c) {
      const int cc = TG ? c : (2 * wn + c);
      vW[c] = L_W + (TG ? wn * 49152 : 0) + (8 * q + qq) * 128 + ((((2 * cc) ^ swz) + (p >> 1)) * 16) + 8 * (p & 1);
    }
  }
  unsigned vD0 = L_D + (80 * wn + rho) * 128 + ((q ^ (rho & 7)) * 16), vD1 = vD0 ^ 64;
  asm volatile("" : "+v"(vX0), "+v"(vX1));
  if (!TG) asm volatile("" : "+v"(vD0), "+v"(vD1));
#pragma unroll
  for (int c = 0; c < NC; ++c) asm volatile("" : "+v"(vW[c]));
  f32x4 aG[NC][4], aU[NC][4], aA[4][5];
  if (TG) {
    unsigned ONE2 = 0x3f803f80u, SLO = 0x00003f80u, SHI = 0x3f800000u;
    asm volatile("" : "+v"(ONE2), "+v"(SLO), "+v"(SHI));
    float sx[2] = {0, 0}, sq[2] = {0, 0};
#pragma unroll
    for (int i = 0; i < 5; ++i)
#pragma unroll
      for (int e = 0; e < 4; ++e) { sx[e & 1] = dot2(vx[i][e], ONE2, sx[e & 1]); sq[e & 1] = dot2(vx[i][e], vx[i][e], sq[e & 1]); }
    const float mean = sum16(sx[0] + sx[1]) * (1.0f / D);
    const float var = fmaxf(sum16(sq[0] + sq[1]) * (1.0f / D) - mean * mean, 0.f);
    const float sd = rbf(__builtin_amdgcn_sqrtf(rbf(rbf(var) + 1e-5f)));
    float rso = (lane & 1) ? rbf(mean) : __builtin_amdgcn_rcpf(sd);
    auto colsum = [&](const u32x4* v) -> float {
      float c[8] = {0, 0, 0, 0, 0, 0, 0, 0};
#pragma unroll
      for (int i = 0; i < 5; ++i)
#pragma unroll
        for (int e = 0; e < 4; ++e) { c[2 * e] = dot2(v[i][e], SLO, c[2 * e]); c[2 * e + 1] = dot2(v[i][e], SHI, c[2 * e + 1]); }
      float d4[4], d2[2];
#pragma unroll
      for (int e = 0; e < 4; ++e) d4[e] = xchg32(c[e], c[4 + e]);
#pragma unroll
      for (int e = 0; e < 2; ++e) d2[e] = xchg16(d4[e], d4[2 + e]);
      d2[0] = dpp_add<0x128>(d2[0]); d2[1] = dpp_add<0x128>(d2[1]);
      return (lane & 8) ? d2[1] : d2[0];
    };
    float c1 = colsum(vw), c2; unsigned off2;
    if (w < 2) {
      c2 = colsum(vm);
      off2 = WS_CS + ((((8 + n8) * 4 + w) * 16 + mt) * 64 + 8 * c8 + rr) * 4;
    } else {
      float a = 0, b = 0;
#pragma unroll
      for (int i = 0; i < 10; ++i) { a = dot2(vm[i][0], ONE2, a); b = dot2(vm[i][1], ONE2, b); a = dot2(vm[i][2], ONE2, a); b = dot2(vm[i][3], ONE2, b); }
      c2 = sum8(a + b);
      off2 = (c8 == 0 && rr < 5) ? WS_CD + (n8 * 160 + 10 * mt + 5 * (w - 2) + rr) * 4 : 0xfffffff0u;
    }
    asm volatile("" : "+v"(c1), "+v"(c2), "+v"(rso));
    __builtin_amdgcn_raw_buffer_store_b32(__float_as_uint(c1), rw, WS_CS + (((n8 * 4 + w) * 16 + mt) * 64 + 8 * c8 + rr) * 4, 0, 16);
    __builtin_amdgcn_raw_buffer_store_b32(__float_as_uint(c2), rw, off2, 0, 16);
    __builtin_amdgcn_raw_buffer_store_b32(__float_as_uint(rso), rw, rho < 2 ? WS_RS + mt * 1024 + rho * 512 + (n8 * 16 + 4 * w + q) * 4 : 0xfffffff0u, 0, 16);
  }
  bf16x8 FX[2][4], FD[2][5];
  s16x4 FW[2][2 * NC][2];
  constexpr int NR = TG ? 20 : 17, NM = TG ? 32 : 36;
  auto rd1 = [&](int s, int buf, int ks, int r) {
    if (r >= 2 && r < 6) {
      const int i = r - 2;
      FX[s][i] = *(LAS bf16x8*)(smem + (ks ? vX1 : vX0) + buf * 16384 + (TG ? i * 2048 : (i >> 1) * 4096 + (i & 1) * 512));
    } else if (r < 4 + 4 * NC) {
      const int h = r < 2 ? r : r - 4, f = h >> 1;
      FW[s][f][h & 1] = __builtin_amdgcn_ds_read_tr16_b64_v4i16((LAS s16x4*)(smem + vW[f >> 1] + buf * 16384 + ks * 4096 + (f & 1) * 8192 + (h & 1) * 512));
    } else {
      const int jj = r - (4 + 4 * NC);
      FD[s][jj] = *(LAS bf16x8*)(smem + (ks ? vD1 : vD0) + buf * 20480 + jj * 2048);
    }
  };
  auto mm1 = [&](int s, int m, bool first) {
    const int f = m >> 2, i = (f & 1) ? 3 - (m & 3) : (m & 3);
    if (f < 2 * NC) {
      const bf16x8 wf = __builtin_shufflevector(FW[s][f][0], FW[s][f][1], 0, 1, 2, 3, 4, 5, 6, 7);
      mf((f & 1) ? aU[f >> 1][i] : aG[f >> 1][i], wf, FX[s][i], first);
    } else mf(aA[i][f - 2 * NC], FX[s][i], FD[s][f - 2 * NC], first);
  };
  auto seg = [&](int s, bool first, int m0, int m1, int rk, int buf, int ks) __attribute__((always_inline)) {
    int r = 0;
#pragma unroll
    for (int m = m0; m < m1; ++m) {
      mm1(s, m, first);
      if (rk == 2 && r == 0) r = 2;
      if (rk == 2 && r == 6) r = 4 + 4 * NC;
      const int rend = rk == 0 ? 0 : rk == 3 ? 4 + 4 * NC : NR;
      if (r < rend && (TG ? (m % 5 != 4) : (rk == 3 || m % 3 != 2))) { rd1(s ^ 1, buf, ks, r); ++r; }
      PIN();
    }
  };
  BAR();
  STAMP_AT(1);
#pragma unroll
  for (int r = 0; r < NR; ++r) rd1(0, 0, 0, r);
  PIN();
  unsigned wv[2][8];
  constexpr int KJ = TG ? 10 : 7;
  sfor<0, 10>([&](auto K) __attribute__((always_inline)) {
    constexpr int k = decltype(K)::v;
    constexpr int m0 = k < KJ ? 0 : 8 * NC;
    seg(0, k == 0, m0, NM, k < KJ ? 1 : 2, k % 3, 1);
    if (k == 1) {
      wait_v<0>();
      if (lane == 0) st_flag(FLAG(C_F0 + (mt * 16 + ns) * 4 + w), tag);
    }
  if (TG && k == WK) {
#pragma unroll
    for (int u = 0; u < 2; ++u) {
      const int task = min(((mt * 8 + ns) * 4 + w) * 2 + u, 959);
      const unsigned* src = (const unsigned*)(P.wdn + (size_t)(((task % 240) / 5) * 32 + 4 * q) * D + (task / 240) * 160 + (task % 5) * 32 + 2 * rho);
#pragma unroll
      for (int e = 0; e < 8; ++e) wv[u][e] = src[((e & 3) + (e >> 2) * 16) * (D / 2)];
    }
  }
  if (TG && k == WK + 2 && (mt * 8 + ns) * 4 + w < 480) {
    const bool up = rho & 8;
#pragma unroll
    for (int u = 0; u < 2; ++u) {
      u32x4 wo[2];
#pragma unroll
      for (int e = 0; e < 4; ++e) {
        const unsigned lo = __builtin_amdgcn_perm(wv[u][2 * e + 1], wv[u][2 * e], 0x05040100u), hi = __builtin_amdgcn_perm(wv[u][2 * e + 1], wv[u][2 * e], 0x07060302u);
        const unsigned t = __builtin_amdgcn_update_dpp(0, up ? lo : hi, 0x128, 0xf, 0xf, true);
        wo[0][e] = up ? t : lo;
        wo[1][e] = up ? hi : t;
      }
      const int task = ((mt * 8 + ns) * 4 + w) * 2 + u;
      const unsigned dst = WS_WD + (((task / 240) * 48 + (task % 240) / 5) * 10 + (task % 5) * 2) * 1024 + (2 * (rho & 7) + (rho >> 3) + 16 * q) * 16;
      st16c(rw, dst, wo[0]); st16c(rw, dst + 1024, wo[1]);
    }
  }
    if (k < 9) {
      WAIT_L0();
      BAR();
      PIN();
    }
    if (k < 9) seg(1, false, m0, NM, k + 1 < KJ ? 1 : 2, (k + 1) % 3, 0);
    else seg(1, false, m0, NM, TG ? 0 : 3, KJ % 3, 0);
    WAIT_L0(); PIN();
  });
  if (!TG) {
    MFMA_DONE();
    after_mfma(aA);
    asm volatile("" : "+v"(lane)); q = lane >> 4; rho = lane & 15;
    STAMP_AT(4);
    const float sgn = (n8 >= 4) ? -1.0f : 1.0f;
    float cs[5];
#pragma unroll
    for (int j = 0; j < 5; ++j) cs[j] = *(LAS float*)(smem + T_CD + (80 * wn + 16 * j + rho) * 4);
    const int kt = (n8 >= 4 ? 32 : 0) + mt * 2 + wm;
#pragma unroll
    for (int I = 0; I < 2; ++I) {
      f32x4 r4[2], m4[2];
#pragma unroll
      for (int t = 0; t < 2; ++t) {
        const int row = 64 * wm + 32 * I + 8 * q + 4 * t;
        r4[t] = *(LAS f32x4*)(smem + T_RS + row * 4) * sgn; m4[t] = *(LAS f32x4*)(smem + T_RM + row * 4);
      }
      const unsigned dst = WS_AB + ((((n8 & 3) * 64 + kt) * 2 + I) * 10 + 5 * wn) * 1024 + lane * 16;
#pragma unroll
      for (int j = 0; j < 5; ++j) {
        const f32x4 v0 = (aA[2 * I][j] - m4[0] * cs[j]) * r4[0], v1 = (aA[2 * I + 1][j] - m4[1] * cs[j]) * r4[1];
        const u32x4 o = {pk_bf16(v0[0], v0[1]), pk_bf16(v0[2], v0[3]), pk_bf16(v1[0], v1[1]), pk_bf16(v1[2], v1[3])};
        st16c(rw, dst + j * 1024, o);
      }
    }
    STAMP_AT(7);
    WAIT_L0(); PIN();
#pragma unroll
    for (int k = KJ; k < 10; ++k) {
      seg(0, false, 0, 8 * NC, 3, k % 3, 1);
      WAIT_L0(); PIN();
      if (k == KJ + AFL) {
        wait_v<0>();
        if (lane == 0) st_flag(FLAG(C_FAB + ((n8 & 3) * 64 + kt) * 2 + wn), tag);
        PIN();
      }
      seg(1, false, 0, 8 * NC, k < 9 ? 3 : 0, (k + 1) % 3, 0);
      WAIT_L0(); PIN();
    }
  }
  MFMA_DONE();
  after_mfma(aG); after_mfma(aU);
  asm volatile("" : "+v"(lane)); q = lane >> 4; rho = lane & 15;
  BAR();
  STAMP_AT(2);
  {
    const float K0 = -2.0f * 0.7978845608028654f * 1.4426950408889634f, K1 = K0 * 0.044715f;
    float mu[4], c0[4], c1[4], v2[4];
#pragma unroll
    for (int i = 0; i < 4; ++i) {
      const int row = TG ? 64 * wm + 16 * i + rho : 64 * wm + 32 * (i >> 1) + 8 * (rho >> 2) + 4 * (i & 1) + (rho & 3);
      const float rs = *(LAS float*)(smem + T_RS + row * 4);
      mu[i] = *(LAS float*)(smem + T_RM + row * 4);
      c0[i] = K0 * rs; c1[i] = K1 * rs * rs * rs; v2[i] = __builtin_amdgcn_rcpf(rs * rs);
    }
#pragma unroll
    for (int J = 0; J < NC / 2; ++J) {
      f32x4 cg[2], cu[2];
#pragma unroll
      for (int t = 0; t < 2; ++t) {
        const int c = 2 * J + t;
        const int o = TG ? (wn * 256 + c * 64 + q * 16) : ((2 * wn + c) * 64 + q * 16);
        cg[t] = *(LAS f32x4*)(smem + T_CS + o);
        cu[t] = *(LAS f32x4*)(smem + T_CS + (TG ? 512 : 256) + o);
      }
#pragma unroll
      for (int i = 0; i < 4; ++i) {
        u32x4 o;
#pragma unroll
        for (int t = 0; t < 2; ++t) {
          const f32x4 g = aG[2 * J + t][i] - cg[t] * mu[i];
          const f32x4 u = aU[2 * J + t][i] - cu[t] * mu[i];
          const f32x4 z = g * (g * g * c1[i] + c0[i]);
          f32x4 e;
#pragma unroll
          for (int r = 0; r < 4; ++r) e[r] = __builtin_amdgcn_exp2f(z[r]);
          e = e * v2[i] + v2[i];
#pragma unroll
          for (int r = 0; r < 4; ++r) e[r] = __builtin_amdgcn_rcpf(e[r]);
          const f32x4 hv = g * u * e;
          o[2 * t] = pk_bf16(hv[0], hv[1]); o[2 * t + 1] = pk_bf16(hv[2], hv[3]);
        }
        const int f0 = hcol0 + (TG ? wn * 64 + J * 32 : wn * 32);
        const int rb = TG ? 4 * wm + i : 4 * wm + 2 * (i >> 1) + (rho >> 3), pos = TG ? lane : 8 * ((rho >> 2) & 1) + 4 * (i & 1) + (rho & 3) + 16 * q;
        st16c(rw, WS_H + (((mt * 24 + (f0 >> 6)) * 8 + rb) * 2 + ((f0 >> 5) & 1)) * 1024 + pos * 16, o);
      }
    }
  }
  STAMP_AT(3);
}

DI void mfma_p2(const Params& P, LAS char* smem, int lane, int w, int split, int mt, int nt, int mt1, int ns, u64 tag) {
  asm volatile("" : "+v"(lane));
  const int wm = w & 1, wn = w >> 1, q = lane >> 4, r15 = lane & 15;
  unsigned vY0 = wm * 8192 + r15 * 128 + ((q ^ (r15 & 7)) * 16), vY1 = vY0 ^ 64;
  unsigned vB0 = L_B + wn * 5120 + lane * 16, vB2 = vB0 + 2 * B_ST;
  const int rt = 2 * wm + wn;
  unsigned vH0 = wn * 8192 + rt * 2048 + lane * 16, vH1 = vH0 ^ 2048, vW0 = L_B + wn * 10240 + lane * 16, vW2 = vW0 + 2 * B_ST;
  asm volatile("" : "+v"(vY0), "+v"(vY1), "+v"(vB0), "+v"(vB2), "+v"(vH0), "+v"(vH1), "+v"(vW0), "+v"(vW2));
  const __amdgpu_buffer_rsrc_t rw = __builtin_amdgcn_make_buffer_rsrc(P.ws, 0, WS_END, 0x00020000);
  const int tile = mt * 4 + nt;
  f32x4 acc[4][5], dac[2][5];
  bf16x8 FA[2][4], FB[2][5], GA[2][4], GB[2][10];
  auto rdy = [&](int s, int k, int ks, int r) {
    const int buf = k & 3;
    if (r >= 1 && r < 5) FA[s][r - 1] = *(LAS bf16x8*)(smem + (ks ? vY1 : vY0) + buf * A_ST + (r - 1) * 2048);
    else { const int j = r ? r - 4 : 0; FB[s][j] = *(LAS bf16x8*)(smem + (buf >= 2 ? vB2 : vB0) + (buf & 1) * B_ST + ks * 10240 + j * 1024); }
  };
  auto rdd = [&](int k, int r) {
    const int buf = k & 3, u = r / 7, j = r % 7;
    if (j == 1 || j == 2) GA[k & 1][u * 2 + j - 1] = *(LAS bf16x8*)(smem + (j == 2 ? vH1 : vH0) + buf * A_ST + u * 1024);
    else { const int jj = j ? j - 2 : 0; GB[k & 1][u * 5 + jj] = *(LAS bf16x8*)(smem + (buf >= 2 ? vW2 : vW0) + (buf & 1) * B_ST + u * 5120 + jj * 1024); }
  };
  auto seg = [&](auto K, auto HF) __attribute__((always_inline)) {
    constexpr int k = decltype(K)::v, hf = decltype(HF)::v, rk = k + hf, rh = hf ^ 1;
    constexpr int nr = rk >= NK2 ? 0 : is_d(rk) ? 14 : 9;
    int r = 0;
#pragma unroll
    for (int m = 0; m < 20; ++m) {
      if constexpr (is_d(k)) { const int i = (m ^ (m >> 1)) & 1; mf(dac[i][(m >> 1) % 5], GB[k & 1][m >> 1], GA[k & 1][(m / 10) * 2 + i], k == KD0 && m < 10); }
      else { const int i = (m & 4) ? 3 - (m & 3) : (m & 3); mf(acc[i][m >> 2], FB[hf][m >> 2], FA[hf][i], k == 0 && hf == 0); }
      if (r < nr && (nr == 14 || m % 3 != 2)) { if constexpr (nr == 14) rdd(rk, r); else rdy(rh, rk, rh, r); ++r; }
      PIN();
    }
  };
  auto publish = [&](int i) {
    unsigned pk[10];
#pragma unroll
    for (int j = 0; j < 5; ++j) { pk[2 * j] = pk_bf16(acc[i][j][0], acc[i][j][1]); pk[2 * j + 1] = pk_bf16(acc[i][j][2], acc[i][j][3]); }
    if (w == split) {
      LAS char* dst = smem + F_Y + i * 2560;
      *(LAS u32x4*)(dst + lane * 16) = (u32x4){pk[0], pk[1], pk[2], pk[3]};
      *(LAS u32x4*)(dst + 1024 + lane * 16) = (u32x4){pk[4], pk[5], pk[6], pk[7]};
      *(LAS u32x2*)(dst + 2048 + lane * 8) = (u32x2){pk[8], pk[9]};
    } else {
      const unsigned dst = WS_P + (((tile * 4 + w) * 3 + (split ^ w) - 1) * 4 + i) * 2560;
      st16c(rw, dst + lane * 16, (u32x4){pk[0], pk[1], pk[2], pk[3]});
      st16c(rw, dst + 1024 + lane * 16, (u32x4){pk[4], pk[5], pk[6], pk[7]});
      __builtin_amdgcn_raw_buffer_store_b64((u32x2){pk[8], pk[9]}, rw, dst + 2048 + lane * 8, 0, 16);
    }
    PIN();
  };
  const int orow = mt * 128 + (split & 1) * 64 + rt * 16, ocol = nt * 160 + (split >> 1) * 80;
  const u64* fp = FLAG(C_FP + (tile * 4 + split) * 4 + min(lane, 2));
  u64 fpv = 0;
  u32x4 xv[3], pa[3][2]; u32x2 pb[3];
  f32x4 ys[5];
  BAR();
  STAMP_AT(5);
#pragma unroll
  for (int r = 0; r < 9; ++r) rdy(0, 0, 0, r);
  PIN();
  sfor<0, NK2>([&](auto K) __attribute__((always_inline)) {
    constexpr int k = decltype(K)::v;
    if constexpr (!is_d(k)) seg(K, IC<0>{});
    if (k == HFL) {
      wait_v<0>();
      if (lane == 0) st_flag(FLAG(C_FH + mt1 * 64 + ns * 4 + w), tag);
    }
    if (k == KYL + YFL && w != split) {
      wait_v<0>();
      if (lane == 0) st_flag(FLAG(C_FP + (tile * 4 + w) * 4 + (split ^ w) - 1), tag);
    }
    if (k == KYL + YPL) fpv = ld_flag(fp);
    if (k == KYL + YPL + 1) {
      if (!__all(fpv == tag)) wait_flags(fp, tag);
      asm volatile("" ::: "memory");
#pragma unroll
      for (int s = 0; s < 3; ++s) {
        const unsigned src = WS_P + (((tile * 4 + split) * 3 + s) * 4 + rt) * 2560;
        pa[s][0] = ld16c(rw, src + lane * 16); pa[s][1] = ld16c(rw, src + 1024 + lane * 16);
        pb[s] = __builtin_amdgcn_raw_buffer_load_b64(rw, src + 2048 + lane * 8, 0, 16);
      }
    }
    if (k == KYL - 1) {
#pragma unroll
      for (int i = 0; i < 3; ++i) {
        const int n = min(i * 64 + lane, 159);
        xv[i] = *(const u32x4*)(P.x + (size_t)(orow + n / 10) * D + ocol + (n % 10) * 8);
      }
    }
    if (k == NK2 - YSB) {
      LAS char* po = smem + F_Y + rt * 2560;
      const u32x4 oa0 = *(LAS u32x4*)(po + lane * 16), oa1 = *(LAS u32x4*)(po + 1024 + lane * 16);
      const u32x2 ob = *(LAS u32x2*)(po + 2048 + lane * 8);
#pragma unroll
      for (int j = 0; j < 5; ++j) {
        const unsigned l0 = j < 2 ? oa0[(j & 1) * 2] : j < 4 ? oa1[(j & 1) * 2] : ob[0], h0 = j < 2 ? oa0[(j & 1) * 2 + 1] : j < 4 ? oa1[(j & 1) * 2 + 1] : ob[1];
        ys[j] = (f32x4){bflo(l0), bfhi(l0), bflo(h0), bfhi(h0)};
#pragma unroll
        for (int s = 0; s < 3; ++s) {
          const unsigned lo = j < 4 ? pa[s][j >> 1][(j & 1) * 2] : pb[s][0], hi = j < 4 ? pa[s][j >> 1][(j & 1) * 2 + 1] : pb[s][1];
          ys[j] += (f32x4){bflo(lo), bfhi(lo), bflo(hi), bfhi(hi)};
        }
        asm volatile("" : "+v"(ys[j]));
      }
    }
    if (k + 1 < NK2) {
      WAIT_L0();
      BAR();
      PIN();
    }
    seg(K, IC<1>{});
    WAIT_L0(); PIN();
    if (k == KYL) {
      MFMA_DONE();
      after_mfma(acc);
      if (threadIdx.x == 0) { P.stamps[blockIdx.x * 32 + 2 * 14] = __builtin_amdgcn_s_memrealtime(); }
      publish(0); publish(1);
      if (!PSPL) { publish(2); publish(3); }
    }
    if (k == KYL + 1) {
      if (PSPL) { publish(2); publish(3); }
      if (threadIdx.x == 0) { P.stamps[blockIdx.x * 32 + 2 * 15] = __builtin_amdgcn_s_memrealtime(); }
    }
  });
  MFMA_DONE();
  after_mfma(dac);
  STAMP_AT(6);
#pragma unroll
  for (int j = 0; j < 5; ++j) *(LAS f32x4*)(smem + F_R + (w * 5 + j) * 1024 + lane * 16) = dac[1][j];
  WAIT_L0();
  BAR();
#pragma unroll
  for (int j = 0; j < 5; ++j) dac[0][j] += *(LAS f32x4*)(smem + F_R + ((w ^ 2) * 5 + j) * 1024 + lane * 16);
  LAS char* pt = smem + F_T + w * (16 * FPITCH);
#pragma unroll
  for (int j = 0; j < 5; ++j) {
    const f32x4 d = dac[0][j];
    f32x4 e = d * -1.4426950408889634f;
#pragma unroll
    for (int r = 0; r < 4; ++r) e[r] = __builtin_amdgcn_exp2f(e[r]);
    e = e + 1.0f;
#pragma unroll
    for (int r = 0; r < 4; ++r) e[r] = __builtin_amdgcn_rcpf(e[r]);
    e = e * d * ys[j];
    *(LAS u32x2*)(pt + r15 * FPITCH + j * 32 + q * 8) = (u32x2){pk_bf16(e[0], e[1]), pk_bf16(e[2], e[3])};
  }
  WAIT_L0();
#pragma unroll
  for (int i = 0; i < 3; ++i) {
    const int n = i * 64 + lane, nc = min(n, 159);
    const u32x4 v = *(LAS u32x4*)(pt + (nc / 10) * FPITCH + (nc % 10) * 16);
    u32x4 o;
#pragma unroll
    for (int d = 0; d < 4; ++d) o[d] = pk_bf16(bflo(v[d]) + bflo(xv[i][d]), bfhi(v[d]) + bfhi(xv[i][d]));
    if (n < 160) *(u32x4*)(P.out + (size_t)(orow + nc / 10) * D + ocol + (nc % 10) * 8) = o;
  }
  wait_v<0>();
  if (threadIdx.x == 0) { P.stamps[blockIdx.x * 32 + 2 * 12] = __builtin_amdgcn_s_memrealtime(); }
}

__global__ __launch_bounds__(512, 2)
void fused(Params P) {
  extern __shared__ __attribute__((aligned(16))) char smem_[];
  LAS char* smem = (LAS char*)smem_;
  const int tid = threadIdx.x, lane = tid & 63, w = __builtin_amdgcn_readfirstlane(tid >> 6);
  const int bid = blockIdx.x, xcd = bid & 7, idx = bid >> 3;
  u64 tag;
  STAMP_AT(0);
  const int mt1 = (xcd & 1) * 8 + (idx & 7), j = idx >> 3, ns = (j & 1) * 8 + (xcd >> 1) * 2 + (j >> 1);
  const int split = xcd >> 1, mt2 = (xcd & 1) * 8 + (idx >> 2), nt = idx & 3;
  if (w >= 4) {
    __builtin_amdgcn_s_setprio(HPRIO);
    helper(P, smem, lane, w & 3, mt1, ns, split, mt2, nt);
  } else {
    if (j & 1) mfma_p1<false>(P, smem, lane, w, mt1, ns, tag); else mfma_p1<true>(P, smem, lane, w, mt1, ns, tag);
    mfma_p2(P, smem, lane, w, split, mt2, nt, mt1, ns, tag);
    __builtin_amdgcn_s_waitcnt(0);
  }
}

__global__ void diag(const u64* st, int a, int b, int mode) {
  const int lane = threadIdx.x;
  u64 mn = ~0ull, mx = 0, sum = 0;
  for (int i = 0; i < 4; ++i) { const int blk = i * 64 + lane; const u64 x = st[blk * 32 + 2 * a], y = st[blk * 32 + 2 * b];
    mn = x < mn ? x : mn; mx = y > mx ? y : mx; sum += mode == 2 ? (st[blk * 32 + 2 * b + 1] - st[blk * 32 + 2 * a + 1]) / 24 : y - x; }
  for (int o = 1; o < 64; o <<= 1) { const u64 m2 = __shfl_xor(mn, o), x2 = __shfl_xor(mx, o); mn = m2 < mn ? m2 : mn; mx = x2 > mx ? x2 : mx; sum += __shfl_xor(sum, o); }
  u64 d = mode == 0 ? mx - mn : sum / 256;
  if (a < 0 || d > 20000) d = 0;
  const u64 t0 = __builtin_amdgcn_s_memrealtime();
  while (__builtin_amdgcn_s_memrealtime() - t0 < 2 * d) __builtin_amdgcn_s_sleep(1);
}
extern "C" void kernel_launch(void* const* d_in, const int* in_sizes, int n_in, void* d_out, int out_size, void* d_ws, size_t ws_size, hipStream_t stream) {
  Params P;
  P.x = (const bf16_t*)d_in[0]; P.wsr = (const bf16_t*)d_in[1]; P.wsi = (const bf16_t*)d_in[2];
  P.wdr = (const bf16_t*)d_in[3]; P.wdi = (const bf16_t*)d_in[4];
  P.wg = (const bf16_t*)d_in[6]; P.wu = (const bf16_t*)d_in[7]; P.wdn = (const bf16_t*)d_in[8];
  P.out = (bf16_t*)d_out; P.ws = (char*)d_ws;
  P.stamps = (u64*)((char*)d_ws + (128u << 20));
  (void)hipFuncSetAttribute((const void*)fused, hipFuncAttributeMaxDynamicSharedMemorySize, LDS_BYTES);
  fused<<<256, 512, LDS_BYTES, stream>>>(P);
  diag<<<1, 64, 0, stream>>>(P.stamps, 0, 0, 1);
}
```

```cpp
#include <hip/hip_runtime.h>
#include <hip/hip_bf16.h>
typedef unsigned short bf16_t;
typedef unsigned long long u64;
typedef short bf16x8 __attribute__((ext_vector_type(8)));
typedef short s16x4 __attribute__((ext_vector_type(4)));
typedef float f32x4 __attribute__((ext_vector_type(4)));
typedef float f32x2 __attribute__((ext_vector_type(2)));
typedef unsigned u32x4 __attribute__((ext_vector_type(4)));
typedef unsigned u32x2 __attribute__((ext_vector_type(2)));
typedef __bf16 bf16x2_t __attribute__((ext_vector_type(2)));
#define LAS __attribute__((address_space(3)))
#define GAS __attribute__((address_space(1)))
#define DI __device__ __forceinline__

#define STAMP 0
#define NPRE 2
#define ROT2 0
#define YFL 4
#define FABSL 1
#define AFL 2
#define HFL 2
#define PSPL 1
#define XG 2
#define XY 1
#define XD 1
#define YSB 2
#define WK 3
#define FABM 0
#define SK 5
#define HOLD 0
#define PD0 60
#define PD1 8
#define DPF 0
#define DFK 8
#define DP0 4
#define DPS 2
#define NOPUB 0
#define DPRIO 2
#define CPK 99
#define S0E 2
#define TPRIO 3
#define B0E 1
#define YPL 8
#define XROT 0
#define HPRIO 0
#define PFN 4

constexpr int S = 2048, D = 640, F = 1536;
constexpr unsigned WS_AB = 0, WS_H = WS_AB + 2 * S * D * 2, WS_P = WS_H + S * F * 2, WS_WD = WS_P + 3 * S * D * 2, WS_RS = WS_WD + F * D * 2,
                   WS_CS = WS_RS + S * 8, WS_CD = WS_CS + 16 * 4 * 16 * 64 * 4, WS_CTRL = WS_CD + 8192, WS_END = WS_CTRL + 5120 * 128;
constexpr int C_E = 0, C_F0 = 1, C_FAB = C_F0 + 1024, C_FH = C_FAB + 512, C_FP = C_FH + 1024;
#define FLAG(i) ((u64*)(P.ws + WS_CTRL) + (i) * 16)

DI unsigned pk_bf16(float lo, float hi) { f32x2 v = {lo, hi}; return __builtin_bit_cast(unsigned, __builtin_convertvector(v, bf16x2_t)); }
DI float rbf(float f) { return __uint_as_float(pk_bf16(f, f) << 16); }
DI float bflo(unsigned u) { return __uint_as_float(u << 16); }
DI float bfhi(unsigned u) { return __uint_as_float(u & 0xffff0000u); }
DI float dot2(unsigned a, unsigned b, float c) { return __builtin_amdgcn_fdot2_f32_bf16(__builtin_bit_cast(bf16x2_t, a), __builtin_bit_cast(bf16x2_t, b), c, false); }
#define GLDS4(POL) asm volatile("s_mov_b32 m0, %0\n\ts_nop 0\n\tglobal_load_lds_dwordx4 %2, %1" POL "\n\tglobal_load_lds_dwordx4 %3, %1 offset:1024" POL \
    "\n\tglobal_load_lds_dwordx4 %4, %1 offset:2048" POL "\n\tglobal_load_lds_dwordx4 %5, %1 offset:3072" POL \
    :: "s"(lds), "s"(base), "v"(v0), "v"(v1), "v"(v2), "v"(v3) : "memory")
#define GLDS5(POL) asm volatile("s_mov_b32 m0, %0\n\ts_nop 0\n\tglobal_load_lds_dwordx4 %2, %1" POL "\n\tglobal_load_lds_dwordx4 %3, %1 offset:1024" POL \
    "\n\tglobal_load_lds_dwordx4 %4, %1 offset:2048" POL "\n\tglobal_load_lds_dwordx4 %5, %1 offset:3072" POL \
    "\n\ts_add_u32 m0, %0, 0x1000\n\ts_nop 0\n\tglobal_load_lds_dwordx4 %6, %1" POL \
    :: "s"(lds), "s"(base), "v"(v0), "v"(v1), "v"(v2), "v"(v3), "v"(v4) : "memory", "scc")
template <bool DEV> DI void glds4(unsigned lds, const char* base, unsigned v0, unsigned v1, unsigned v2, unsigned v3) {
  if (DEV) GLDS4(" sc1"); else GLDS4("");
}
template <bool DEV> DI void glds5(unsigned lds, const char* base, unsigned v0, unsigned v1, unsigned v2, unsigned v3, unsigned v4) {
  if (DEV) GLDS5(" sc1"); else GLDS5("");
}
template <bool DEV = false> DI void touch_lines(unsigned lds, unsigned voff, const char* base) {
  if (DEV) asm volatile("s_mov_b32 m0, %0\n\ts_nop 0\n\tglobal_load_lds_dword %1, %2 sc1" :: "s"(lds), "v"(voff), "s"(base) : "memory");
  else asm volatile("s_mov_b32 m0, %0\n\ts_nop 0\n\tglobal_load_lds_dword %1, %2" :: "s"(lds), "v"(voff), "s"(base) : "memory");
}
template <int N> DI void wait_v() { asm volatile("s_waitcnt vmcnt(%0)" :: "i"(N) : "memory"); }
#define WAIT_L0() do { __builtin_amdgcn_s_waitcnt(0xC07F); asm volatile("" ::: "memory"); } while (0)
#define BAR() do { asm volatile("" ::: "memory"); __builtin_amdgcn_s_barrier(); asm volatile("" ::: "memory"); } while (0)
#define SGB(m, n) __builtin_amdgcn_sched_group_barrier(m, n, 0)
#define PIN() __builtin_amdgcn_sched_barrier(0)
template <int V> struct IC { static constexpr int v = V; };
template <int I, int N, class Fn> DI void sfor(Fn&& f) { if constexpr (I < N) { f(IC<I>{}); sfor<I + 1, N>(f); } }
DI void mf(f32x4& c, const bf16x8& a, const bf16x8& b, bool first) {
  if (first) asm volatile("v_mfma_f32_16x16x32_bf16 %0, %1, %2, 0" : "=&v"(c) : "v"(a), "v"(b));
  else asm volatile("v_mfma_f32_16x16x32_bf16 %0, %1, %2, %0" : "+v"(c) : "v"(a), "v"(b));
}
#define MFMA_DONE() asm volatile("s_nop 15")
template <int A, int B> DI void after_mfma(f32x4 (&c)[A][B]) {
#pragma unroll
  for (int i = 0; i < A; ++i)
#pragma unroll
    for (int j = 0; j < B; ++j) asm volatile("" : "+v"(c[i][j]));
}
#define STAMP_AT(i)

template <int CTRL> DI float dpp_add(float v) { return v + __builtin_bit_cast(float, __builtin_amdgcn_update_dpp(0, __builtin_bit_cast(int, v), CTRL, 0xf, 0xf, true)); }
DI float sum8(float v) { v = dpp_add<0xB1>(v); v = dpp_add<0x4E>(v); return dpp_add<0x141>(v); }
DI float sum16(float v) { return dpp_add<0x140>(sum8(v)); }
DI float xchg16(float keep_lo, float keep_hi) {
  const auto r = __builtin_amdgcn_permlane16_swap(__float_as_uint(keep_lo), __float_as_uint(keep_hi), false, false);
  return __uint_as_float(r[0]) + __uint_as_float(r[1]);
}
DI float xchg32(float keep_lo, float keep_hi) {
  const auto r = __builtin_amdgcn_permlane32_swap(__float_as_uint(keep_lo), __float_as_uint(keep_hi), false, false);
  return __uint_as_float(r[0]) + __uint_as_float(r[1]);
}
#define TIC(v)
#define ACC(a, x, y)
#define PUT(slot, a)
DI u64 ld_flag(const u64* p) { return __hip_atomic_load((const GAS u64*)p, __ATOMIC_RELAXED, __HIP_MEMORY_SCOPE_AGENT); }
DI void st_flag(u64* p, u64 v) { __hip_atomic_store((GAS u64*)p, v, __ATOMIC_RELAXED, __HIP_MEMORY_SCOPE_AGENT); }
DI void wait_flags(const u64* p, u64 tag) {
#pragma unroll 1
  for (int it = 0; it < 60000; ++it) {
    if (__all(ld_flag(p) == tag)) break;
    __builtin_amdgcn_s_sleep(4);
  }
  asm volatile("" ::: "memory");
}
DI void wait_flags2(const u64* p, const u64* p2, u64 tag) {
#pragma unroll 1
  for (int it = 0; it < 60000; ++it) {
    const u64 a = ld_flag(p), b = ld_flag(p2);
    if (__all(a == tag && b == tag)) break;
    __builtin_amdgcn_s_sleep(4);
  }
  asm volatile("" ::: "memory");
}
DI u64 read_tag(const char* ws) {
  u64 e;
  asm volatile("s_load_dwordx2 %0, %1, 0x0\n\ts_waitcnt lgkmcnt(0)" : "=s"(e) : "s"(ws + WS_CTRL) : "memory");
  return e * 6364136223846793005ull + 1442695040888963407ull;
}
DI void st16c(__amdgpu_buffer_rsrc_t r, unsigned off, u32x4 v) { __builtin_amdgcn_raw_buffer_store_b128(v, r, off, 0, 16); }
DI u32x4 ld16c(__amdgpu_buffer_rsrc_t r, unsigned off) { return __builtin_amdgcn_raw_buffer_load_b128(r, off, 0, 16); }

struct Params {
  const bf16_t *x, *wsr, *wsi, *wdr, *wdi, *wg, *wu, *wdn;
  bf16_t* out; char* ws;
};

constexpr int L_W = 49152, L_D = 98304, K1_TAB = 159744, LDS_BYTES = K1_TAB + 4096;
constexpr int T_RS = K1_TAB, T_RM = K1_TAB + 512, T_CS = K1_TAB + 1024, T_CD = K1_TAB + 2048, T_GO = K1_TAB + 3584, T_PF = K1_TAB + 3072;
constexpr int A_ST = 16384, B_ST = 20480, L_B = 4 * A_ST;

constexpr int NY = 16;
constexpr int F_Y = 147456, F_T = 106496, FPITCH = 176;

DI int f_red(int lw) { return (lw >> 1) * L_B + (lw & 1) * 20480; }

DI void helper(const Params& P, LAS char* smem, int lane, int lw, int mt, int ns, int split, int mt2, int nt) {
  constexpr int NL = 12;
  const bool TG = ns < 8;
  const int n8 = ns & 7, hcol0 = TG ? 128 * n8 : 1024 + 64 * n8;
  const int rr = lane >> 3, c8 = lane & 7;
  const unsigned lds0 = (unsigned)(size_t)smem;
  unsigned vX[4], vW[4], vD[5];
#pragma unroll
  for (int j = 0; j < 4; ++j) {
    vX[j] = (32 * lw + 8 * j + rr) * 1280 + ((c8 ^ (TG ? rr : ((rr & 2) | ((j & 2) << 1)))) * 16) - j * 1024;
    vW[j] = (8 * j + rr) * 3072 + ((c8 ^ ((rr & 2) | ((j & 1) << 2))) * 16) - j * 1024;
  }
#pragma unroll
  for (int j = 0; j < 5; ++j) vD[j] = (40 * lw + 8 * j + rr) * 1280 + ((c8 ^ rr) * 16) - (j & 3) * 1024;
  const char* bx = (const char*)P.x + (size_t)mt * 128 * 1280;
  const char* bw = (const char*)((lw >> 1) ? P.wu : P.wg) + hcol0 * 2 + (TG ? (lw & 1) * 128 : (lw & 1) * (32 * 3072));
  const char* bd = (const char*)(n8 >= 4 ? P.wdi : P.wdr) + (size_t)(n8 & 3) * 160 * 1280;
  const unsigned ldsX = __builtin_amdgcn_readfirstlane(lds0 + lw * 4096);
  const unsigned ldsW = __builtin_amdgcn_readfirstlane(lds0 + L_W + (TG ? (lw & 1) * 49152 + (lw >> 1) * 8192 : lw * 4096));
  const unsigned ldsD = __builtin_amdgcn_readfirstlane(lds0 + L_D + lw * 5120);
  auto issue_a = [&](int kt) {
    const int buf = kt % 3;
    glds4<false>(ldsX + buf * 16384, bx + kt * 128, vX[0], vX[1], vX[2], vX[3]);
    glds4<false>(ldsW + buf * 16384, bw + kt * (64 * 3072), vW[0], vW[1], vW[2], vW[3]);
  };
  auto issue_b = [&](int kt) {
    const int buf = kt % 3;
    if (TG) glds4<false>(ldsW + buf * 16384 + 4096, bw + kt * (64 * 3072) + 32 * 3072, vW[0], vW[1], vW[2], vW[3]);
    else glds5<false>(ldsD + buf * 20480, bd + kt * 128, vD[0], vD[1], vD[2], vD[3], vD[4]);
  };
  auto issue = [&](int kt) { issue_a(kt); issue_b(kt); };
  issue(0);
  issue_a(1);
  u64 tag;
  if (!S0E) issue_b(1);
  const char* pfb = (const char*)(split < 2 ? P.wsr : P.wsi) + (size_t)(mt2 * 128 + nt * 32) * 4096 + (split & 1) * 2048;
  const unsigned pfv = (lane & 31) * 4096 + ((lw & 1) * 2 + (lane >> 5)) * 128;
  const u64* fa = FLAG(C_F0 + (lw == 0 ? mt * 64 + (lane & 31) : (((lane >> 1) & 15) * 16 + n8) * 4 + 2 + (lane & 1)));
  u64 fav = 0;
  const int xg = ((mt2 ^ nt) & 1) * XG;
  const unsigned vL = lane * 16, vL4 = vL + 4096;
  const char* baseWD = P.ws + WS_WD + ((nt * 48 + lw) * 10 + (split >> 1) * 5) * 1024;
  sfor<0, 8>([&](auto K) __attribute__((always_inline)) {
    constexpr int k = decltype(K)::v;
    if (S0E && k == 0) wait_v<8>(); else if (S0E == 2 && k == 1) wait_v<8>(); else if (TG) wait_v<12>(); else wait_v<13>();
    BAR();
    if (S0E && k == 0) issue_b(1);
    if (S0E == 2 && k == 1) issue_b(2);
    if (lw < (TG ? 1 : 2)) {
      if (k == SK) asm volatile("global_load_dwordx2 %0, %1, off sc1" : "+v"(fav) : "v"(fa) : "memory");
      if (k == SK + 1) {
        asm volatile("" : "+v"(fav));
        if (!__all(fav == tag)) wait_flags(fa, tag);
        const char* src = P.ws + (lw == 0 ? WS_RS + mt * 1024 : WS_CD + n8 * 640);
        asm volatile("s_mov_b32 m0, %0\n\ts_nop 0\n\tglobal_load_lds_dwordx4 %1, %2 sc1" :: "s"(lds0 + (lw == 0 ? T_RS : T_CD)), "v"(vL), "s"(src) : "memory");
      }
    }
    if (S0E == 2 && k == 0) issue_a(2); else issue(k + 2);
    if (k == 0) tag = read_tag(P.ws);
    if (PFN && (k >> 1) < PFN && (k & 1) == (mt & 1) && lw >= 2)
      touch_lines(lds0 + T_PF, pfv, pfb + (k >> 1) * 512);
  });
  wait_v<NL>();
  BAR();
  wait_v<0>();
  BAR();
  if (TG || lw < 2) {
    wait_flags(FLAG(C_F0 + ((lane & 15) * 16 + n8) * 4 + lw), tag);
    const __amdgpu_buffer_rsrc_t rw = __builtin_amdgcn_make_buffer_rsrc(P.ws, 0, WS_END, 0x00020000);
    f32x4 c = {0, 0, 0, 0};
#pragma unroll
    for (int i = 0; i < 4; ++i) c += __builtin_bit_cast(f32x4, ld16c(rw, WS_CS + (ns * 4 + lw) * 4096 + i * 1024 + lane * 16));
#pragma unroll
    for (int e = 0; e < 4; ++e) { c[e] = xchg16(c[e], c[e]); c[e] = xchg32(c[e], c[e]); }
    if (lane < 16) *(LAS f32x4*)(smem + T_CS + lw * 256 + lane * 16) = c;
  }
  if (lw == 0 && lane == 0) *(volatile LAS unsigned*)(smem + T_GO) = 0;
  WAIT_L0();
  if (blockIdx.x == 64 && lw == 3) {
    wait_flags2(FLAG(C_F0 + lane * 4), FLAG(C_F0 + (64 + lane) * 4), tag);
    wait_flags2(FLAG(C_F0 + (128 + lane) * 4), FLAG(C_F0 + (192 + lane) * 4), tag);
    if (lane == 0) st_flag(FLAG(C_E), tag);
  }
  const u64* fab = FLAG(C_FAB + (nt * 64 + split * 16) * 2 + (lane & 31));
  unsigned vY[4];
#pragma unroll
  for (int j = 0; j < 4; ++j) vY[j] = (32 * lw + 8 * j + rr) * 4096 + ((c8 ^ rr) * 16) - j * 1024;
  const char* baseY = (const char*)(split < 2 ? P.wsr : P.wsi) + (size_t)mt2 * 128 * 4096 + (split & 1) * 2048;
  const char* baseAB = P.ws + WS_AB + (nt * 64 + split * 16) * B_ST + lw * 5120;
  const char* baseH = P.ws + WS_H + ((mt2 * 24 + (lw >> 1)) * 16 + (split & 1) * 8 + (lw & 1)) * 1024;
  const unsigned ldsA = ldsX, ldsB = __builtin_amdgcn_readfirstlane(lds0 + L_B + lw * 5120);
  auto issueYA = [&](int k, int yi) { glds4<false>(ldsA + (k & 3) * A_ST, baseY + (yi ^ xg) * 128, vY[0], vY[1], vY[2], vY[3]); };
  auto issueYB = [&](int k, int yi) { glds5<true>(ldsB + (k & 3) * B_ST, baseAB + (yi ^ xg) * B_ST, vL, vL, vL, vL, vL4); };
  BAR();
  __builtin_amdgcn_s_setprio(TPRIO);
  STAMP_AT(8);
  u64 fabv;
  if (FABM) asm volatile("global_load_dwordx2 %0, %1, off sc1" : "=&v"(fabv) : "v"(fab) : "memory"); else fabv = ~tag;
  issueYA(0, 0); issueYA(1, 1);
  if (FABM) wait_v<8>();
  asm volatile("" : "+v"(fabv));
  const bool ab_seen = __all(fabv == tag);
  if (lw == 0) {
    if (!ab_seen) wait_flags(fab, tag);
    if (lane == 0) *(volatile LAS unsigned*)(smem + T_GO) = 1;
  } else if (!ab_seen) {
#pragma unroll 1
    for (int it = 0; it < 4000000; ++it) {
      if (*(volatile LAS unsigned*)(smem + T_GO) != 0) break;
      __builtin_amdgcn_s_sleep(1);
    }
  }
  asm volatile("" ::: "memory");
  STAMP_AT(9);
  const u64* fh = FLAG(C_FH + mt2 * 64 + (lane & 31) * 2 + (split & 1));
  const int l36 = min(lane, 35);
  const int wt = (nt * 240 + ((l36 / 3) * 4 + lw) * 5 + (split >> 1) * 2 + l36 % 3) >> 1;
  const u64* fw = FLAG(C_FH + (wt >> 5) * 64 + (wt & 31));
  u64 fhv = 0, fwv = 0;
  issueYB(0, 0);
  if (B0E) { wait_v<0>(); BAR(); issueYB(1, 1); } else { issueYB(1, 1); wait_v<5>(); BAR(); }
  issueYA(2, 2); issueYB(2, 2);
  wait_v<9>(); BAR();
  __builtin_amdgcn_s_setprio(0);
  issueYA(3, 3); issueYB(3, 3); issueYA(4, 4); issueYB(4, 4);
  const int ph = nt * 32 + lane, pw = (mt2 & 7) * 20 + min(max(lane - 32, 0), 19);
  const unsigned dstr = lane < 32 ? 2 * A_ST : 2 * B_ST;
  unsigned dpf = (lane < 32 ? WS_H + ((mt2 * 24 + (ph >> 6)) * 16 + (split & 1) * 8) * 1024 + (ph & 63) * 128
                            : WS_WD + ((nt * 48 + pw / 40) * 10 + (split >> 1) * 5) * 1024 + (pw % 40) * 128) + (DP0 + lw) * dstr;
#pragma unroll 1
  for (int k = 2; k < NY - 3; ++k) {
    if (k == DFK + 1) {
      wait_v<9>();
      asm volatile("" : "+v"(fhv), "+v"(fwv));
      if (!__all(fhv == tag && fwv == tag)) wait_flags2(fh, fw, tag);
    } else wait_v<18>();
    BAR();
    if (k == DFK) asm volatile("global_load_dwordx2 %0, %2, off sc1\n\tglobal_load_dwordx2 %1, %3, off sc1" : "+v"(fhv), "+v"(fwv) : "v"(fh), "v"(fw) : "memory");
    issueYA(k + 3, k + 3); issueYB(k + 3, k + 3);
    if (DPF) {
      const int n0 = (k - (DFK + 1)) * DPS;
      const int n = n0 + ((lw - n0) & 3);
      if (n0 >= 0 && n < n0 + DPS && DP0 + n < 12) { touch_lines<true>(lds0 + T_PF, dpf, P.ws); dpf += 4 * dstr; }
    }
  }
  bf16x8 GA[4][4], GB[4][5];
  f32x4 dac[4][5];
  auto loadD = [&](int i) {
    const int s = i & 3;
    const char* pa = baseH + (i ^ xg) * (2 * A_ST);
    const char* pb = baseWD + (i ^ xg) * (2 * B_ST);
    asm volatile("global_load_dwordx4 %0, %9, %12 sc1\n\tglobal_load_dwordx4 %4, %9, %11 sc1\n\tglobal_load_dwordx4 %5, %9, %11 offset:2048 sc1\n\t"
                 "global_load_dwordx4 %6, %10, %11 sc1\n\tglobal_load_dwordx4 %7, %10, %11 offset:2048 sc1\n\t"
                 "global_load_dwordx4 %1, %9, %12 offset:1024 sc1\n\tglobal_load_dwordx4 %2, %9, %12 offset:2048 sc1\n\t"
                 "global_load_dwordx4 %3, %9, %12 offset:3072 sc1\n\tglobal_load_dwordx4 %8, %10, %12 sc1"
                 : "=&v"(GB[s][0]), "=&v"(GB[s][1]), "=&v"(GB[s][2]), "=&v"(GB[s][3]), "=&v"(GA[s][0]), "=&v"(GA[s][1]), "=&v"(GA[s][2]), "=&v"(GA[s][3]), "=&v"(GB[s][4])
                 : "v"(vL), "v"(vL4), "s"(pa), "s"(pb) : "memory");
  };
  auto load1 = [&](int i, int n) {
    const int s = i & 3;
    const char* pa = baseH + (i ^ xg) * (2 * A_ST);
    const char* pb = baseWD + (i ^ xg) * (2 * B_ST);
    if (n < 4) asm volatile("global_load_dwordx4 %0, %1, %2 offset:%3 sc1" : "=&v"(GA[s][n]) : "v"(n < 2 ? vL : vL4), "s"(pa), "i"((n & 1) * 2048) : "memory");
    else asm volatile("global_load_dwordx4 %0, %1, %2 offset:%3 sc1" : "=&v"(GB[s][n - 4]) : "v"(n < 8 ? vL : vL4), "s"(pb), "i"(((n - 4) & 3) * 1024) : "memory");
  };
  auto group = [&](auto I) __attribute__((always_inline)) {
    constexpr int i = decltype(I)::v, s = i & 3;
    wait_v<(i == 0 ? 27 : i < 10 ? 18 : (11 - i) * 9)>();
    asm volatile("" : "+v"(GA[s][0]), "+v"(GA[s][1]), "+v"(GA[s][2]), "+v"(GA[s][3]), "+v"(GB[s][0]), "+v"(GB[s][1]), "+v"(GB[s][2]), "+v"(GB[s][3]), "+v"(GB[s][4]));
#pragma unroll
    for (int m = 0; m < 20; ++m) {
      const int r = (m & 4) ? 3 - (m & 3) : (m & 3);
      mf(dac[r][m >> 2], GB[s][m >> 2], GA[s][r], i == 0);
      if (i >= 1 && i + 3 < 12 && (m & 1) == 1 && m < 18) load1(i + 3, m >> 1);
    }
  };
  loadD(0);
  wait_v<27>(); BAR();
  loadD(1);
  wait_v<27>(); BAR();
  loadD(2);
  wait_v<27>(); BAR();
  loadD(3);
  __builtin_amdgcn_s_setprio(DPRIO);
  sfor<0, 12>(group);
  MFMA_DONE();
  after_mfma(dac);
#pragma unroll
  for (int r = 0; r < 4; ++r)
#pragma unroll
    for (int j = 0; j < 5; ++j) *(LAS f32x4*)(smem + f_red(lw) + (r * 5 + j) * 1024 + lane * 16) = dac[r][j];
  WAIT_L0();
  STAMP_AT(11);
  BAR();
  STAMP_AT(10);
}

template <bool TG>
DI void mfma_p1(const Params& P, LAS char* smem, int lane, int w, int mt, int ns, u64& tag) {
  const int n8 = ns & 7, hcol0 = TG ? 128 * n8 : 1024 + 64 * n8;
  const __amdgpu_buffer_rsrc_t rw = __builtin_amdgcn_make_buffer_rsrc(P.ws, 0, WS_END, 0x00020000);
  int q = lane >> 4, rho = lane & 15;
  const int rr = lane >> 3, c8 = lane & 7;
  u32x4 vx[5], vw[5], vm[10];
  if (TG) {
    const char* px = (const char*)P.x + (size_t)(mt * 128 + n8 * 16 + 4 * w + q) * 1280 + rho * 16;
#pragma unroll
    for (int i = 0; i < 5; ++i) vx[i] = *(const u32x4*)(px + i * 256);
    const char* pw = (const char*)((w >> 1) ? P.wu : P.wg) + (size_t)(40 * mt + rr) * 3072 + (hcol0 + (w & 1) * 64) * 2 + c8 * 16;
#pragma unroll
    for (int i = 0; i < 5; ++i) vw[i] = *(const u32x4*)(pw + i * (8 * 3072));
    if (w < 2) {
      const char* pm = (const char*)(w ? P.wu : P.wg) + (size_t)(40 * mt + rr) * 3072 + (1024 + 64 * n8) * 2 + c8 * 16;
#pragma unroll
      for (int i = 0; i < 5; ++i) vm[i] = *(const u32x4*)(pm + i * (8 * 3072));
    } else {
      const int e = (n8 & 3) * 160 + 10 * mt + 5 * (w - 2) + min(rr, 4);
      const char* pd = (const char*)(n8 >= 4 ? P.wdi : P.wdr) + (size_t)e * 1280 + c8 * 16;
#pragma unroll
      for (int i = 0; i < 10; ++i) vm[i] = *(const u32x4*)(pd + i * 128);
    }
  }
  tag = read_tag(P.ws);
  const int wm = w & 1, wn = w >> 1;
  constexpr int NC = TG ? 4 : 2;
  unsigned vX0 = TG ? wm * 8192 + rho * 128 + ((q ^ (rho & 7)) * 16)
                          : wm * 8192 + (8 * (rho >> 2) + (rho & 3)) * 128 + ((q ^ ((rho & 2) | ((rho >> 1) & 4))) * 16);
  unsigned vX1 = vX0 ^ 64;
  unsigned vW[NC];
  {
    const int qq = rho >> 2, p = lane & 3, swz = (qq & 2) | ((q & 1) << 2);
#pragma unroll
    for (int c = 0; c < NC; ++c) {
      const int cc = TG ? c : (2 * wn + c);
      vW[c] = L_W + (TG ? wn * 49152 : 0) + (8 * q + qq) * 128 + ((((2 * cc) ^ swz) + (p >> 1)) * 16) + 8 * (p & 1);
    }
  }
  unsigned vD0 = L_D + (80 * wn + rho) * 128 + ((q ^ (rho & 7)) * 16), vD1 = vD0 ^ 64;
  asm volatile("" : "+v"(vX0), "+v"(vX1));
  if (!TG) asm volatile("" : "+v"(vD0), "+v"(vD1));
#pragma unroll
  for (int c = 0; c < NC; ++c) asm volatile("" : "+v"(vW[c]));
  f32x4 aG[NC][4], aU[NC][4], aA[4][5];
  if (TG) {
    unsigned ONE2 = 0x3f803f80u, SLO = 0x00003f80u, SHI = 0x3f800000u;
    asm volatile("" : "+v"(ONE2), "+v"(SLO), "+v"(SHI));
    float sx[2] = {0, 0}, sq[2] = {0, 0};
#pragma unroll
    for (int i = 0; i < 5; ++i)
#pragma unroll
      for (int e = 0; e < 4; ++e) { sx[e & 1] = dot2(vx[i][e], ONE2, sx[e & 1]); sq[e & 1] = dot2(vx[i][e], vx[i][e], sq[e & 1]); }
    const float mean = sum16(sx[0] + sx[1]) * (1.0f / D);
    const float var = fmaxf(sum16(sq[0] + sq[1]) * (1.0f / D) - mean * mean, 0.f);
    const float sd = rbf(__builtin_amdgcn_sqrtf(rbf(rbf(var) + 1e-5f)));
    float rso = (lane & 1) ? rbf(mean) : __builtin_amdgcn_rcpf(sd);
    auto colsum = [&](const u32x4* v) -> float {
      float c[8] = {0, 0, 0, 0, 0, 0, 0, 0};
#pragma unroll
      for (int i = 0; i < 5; ++i)
#pragma unroll
        for (int e = 0; e < 4; ++e) { c[2 * e] = dot2(v[i][e], SLO, c[2 * e]); c[2 * e + 1] = dot2(v[i][e], SHI, c[2 * e + 1]); }
      float d4[4], d2[2];
#pragma unroll
      for (int e = 0; e < 4; ++e) d4[e] = xchg32(c[e], c[4 + e]);
#pragma unroll
      for (int e = 0; e < 2; ++e) d2[e] = xchg16(d4[e], d4[2 + e]);
      d2[0] = dpp_add<0x128>(d2[0]); d2[1] = dpp_add<0x128>(d2[1]);
      return (lane & 8) ? d2[1] : d2[0];
    };
    float c1 = colsum(vw), c2; unsigned off2;
    if (w < 2) {
      c2 = colsum(vm);
      off2 = WS_CS + ((((8 + n8) * 4 + w) * 16 + mt) * 64 + 8 * c8 + rr) * 4;
    } else {
      float a = 0, b = 0;
#pragma unroll
      for (int i = 0; i < 10; ++i) { a = dot2(vm[i][0], ONE2, a); b = dot2(vm[i][1], ONE2, b); a = dot2(vm[i][2], ONE2, a); b = dot2(vm[i][3], ONE2, b); }
      c2 = sum8(a + b);
      off2 = (c8 == 0 && rr < 5) ? WS_CD + (n8 * 160 + 10 * mt + 5 * (w - 2) + rr) * 4 : 0xfffffff0u;
    }
    asm volatile("" : "+v"(c1), "+v"(c2), "+v"(rso));
    __builtin_amdgcn_raw_buffer_store_b32(__float_as_uint(c1), rw, WS_CS + (((n8 * 4 + w) * 16 + mt) * 64 + 8 * c8 + rr) * 4, 0, 16);
    __builtin_amdgcn_raw_buffer_store_b32(__float_as_uint(c2), rw, off2, 0, 16);
    __builtin_amdgcn_raw_buffer_store_b32(__float_as_uint(rso), rw, rho < 2 ? WS_RS + mt * 1024 + rho * 512 + (n8 * 16 + 4 * w + q) * 4 : 0xfffffff0u, 0, 16);
  }
  bf16x8 FX[2][4], FD[2][5];
  s16x4 FW[2][2 * NC][2];
  constexpr int NR = TG ? 20 : 17, NM = TG ? 32 : 36;
  auto rd1 = [&](int s, int buf, int ks, int r) {
    if (r >= 2 && r < 6) {
      const int i = r - 2;
      FX[s][i] = *(LAS bf16x8*)(smem + (ks ? vX1 : vX0) + buf * 16384 + (TG ? i * 2048 : (i >> 1) * 4096 + (i & 1) * 512));
    } else if (r < 4 + 4 * NC) {
      const int h = r < 2 ? r : r - 4, f = h >> 1;
      FW[s][f][h & 1] = __builtin_amdgcn_ds_read_tr16_b64_v4i16((LAS s16x4*)(smem + vW[f >> 1] + buf * 16384 + ks * 4096 + (f & 1) * 8192 + (h & 1) * 512));
    } else {
      const int jj = r - (4 + 4 * NC);
      FD[s][jj] = *(LAS bf16x8*)(smem + (ks ? vD1 : vD0) + buf * 20480 + jj * 2048);
    }
  };
  auto mm1 = [&](int s, int m, bool first) {
    const int f = m >> 2, i = (f & 1) ? 3 - (m & 3) : (m & 3);
    if (f < 2 * NC) {
      const bf16x8 wf = __builtin_shufflevector(FW[s][f][0], FW[s][f][1], 0, 1, 2, 3, 4, 5, 6, 7);
      mf((f & 1) ? aU[f >> 1][i] : aG[f >> 1][i], wf, FX[s][i], first);
    } else mf(aA[i][f - 2 * NC], FX[s][i], FD[s][f - 2 * NC], first);
  };
  auto seg = [&](int s, bool first, int m0, int m1, int rk, int buf, int ks) __attribute__((always_inline)) {
    int r = 0;
#pragma unroll
    for (int m = m0; m < m1; ++m) {
      mm1(s, m, first);
      if (rk == 2 && r == 0) r = 2;
      if (rk == 2 && r == 6) r = 4 + 4 * NC;
      const int rend = rk == 0 ? 0 : rk == 3 ? 4 + 4 * NC : NR;
      if (r < rend && (TG ? (m % 5 != 4) : (rk == 3 || m % 3 != 2))) { rd1(s ^ 1, buf, ks, r); ++r; }
      PIN();
    }
  };
  BAR();
  STAMP_AT(1);
#pragma unroll
  for (int r = 0; r < NR; ++r) rd1(0, 0, 0, r);
  PIN();
  unsigned wv[2][8];
  constexpr int KJ = TG ? 10 : 7;
  sfor<0, 10>([&](auto K) __attribute__((always_inline)) {
    constexpr int k = decltype(K)::v;
    constexpr int m0 = k < KJ ? 0 : 8 * NC;
    seg(0, k == 0, m0, NM, k < KJ ? 1 : 2, k % 3, 1);
    if (k == 1) {
      wait_v<0>();
      if (lane == 0) st_flag(FLAG(C_F0 + (mt * 16 + ns) * 4 + w), tag);
    }
  if (TG && k == WK) {
#pragma unroll
    for (int u = 0; u < 2; ++u) {
      const int task = min(((mt * 8 + ns) * 4 + w) * 2 + u, 959);
      const unsigned* src = (const unsigned*)(P.wdn + (size_t)(((task % 240) / 5) * 32 + 4 * q) * D + (task / 240) * 160 + (task % 5) * 32 + 2 * rho);
#pragma unroll
      for (int e = 0; e < 8; ++e) wv[u][e] = src[((e & 3) + (e >> 2) * 16) * (D / 2)];
    }
  }
  if (TG && k == WK + 2 && (mt * 8 + ns) * 4 + w < 480) {
    const bool up = rho & 8;
#pragma unroll
    for (int u = 0; u < 2; ++u) {
      u32x4 wo[2];
#pragma unroll
      for (int e = 0; e < 4; ++e) {
        const unsigned lo = __builtin_amdgcn_perm(wv[u][2 * e + 1], wv[u][2 * e], 0x05040100u), hi = __builtin_amdgcn_perm(wv[u][2 * e + 1], wv[u][2 * e], 0x07060302u);
        const unsigned t = __builtin_amdgcn_update_dpp(0, up ? lo : hi, 0x128, 0xf, 0xf, true);
        wo[0][e] = up ? t : lo;
        wo[1][e] = up ? hi : t;
      }
      const int task = ((mt * 8 + ns) * 4 + w) * 2 + u;
      const unsigned dst = WS_WD + (((task / 240) * 48 + (task % 240) / 5) * 10 + (task % 5) * 2) * 1024 + (2 * (rho & 7) + (rho >> 3) + 16 * q) * 16;
      st16c(rw, dst, wo[0]); st16c(rw, dst + 1024, wo[1]);
    }
  }
    if (k < 9) {
      WAIT_L0();
      BAR();
      PIN();
    }
    if (k < 9) seg(1, false, m0, NM, k + 1 < KJ ? 1 : 2, (k + 1) % 3, 0);
    else seg(1, false, m0, NM, TG ? 0 : 3, KJ % 3, 0);
    WAIT_L0(); PIN();
  });
  if (!TG) {
    MFMA_DONE();
    after_mfma(aA);
    asm volatile("" : "+v"(lane)); q = lane >> 4; rho = lane & 15;
    STAMP_AT(4);
    const float sgn = (n8 >= 4) ? -1.0f : 1.0f;
    float cs[5];
#pragma unroll
    for (int j = 0; j < 5; ++j) cs[j] = *(LAS float*)(smem + T_CD + (80 * wn + 16 * j + rho) * 4);
    const int kt = (n8 >= 4 ? 32 : 0) + mt * 2 + wm;
#pragma unroll
    for (int I = 0; I < 2; ++I) {
      f32x4 r4[2], m4[2];
#pragma unroll
      for (int t = 0; t < 2; ++t) {
        const int row = 64 * wm + 32 * I + 8 * q + 4 * t;
        r4[t] = *(LAS f32x4*)(smem + T_RS + row * 4) * sgn; m4[t] = *(LAS f32x4*)(smem + T_RM + row * 4);
      }
      const unsigned dst = WS_AB + ((((n8 & 3) * 64 + kt) * 2 + I) * 10 + 5 * wn) * 1024 + lane * 16;
#pragma unroll
      for (int j = 0; j < 5; ++j) {
        const f32x4 v0 = (aA[2 * I][j] - m4[0] * cs[j]) * r4[0], v1 = (aA[2 * I + 1][j] - m4[1] * cs[j]) * r4[1];
        const u32x4 o = {pk_bf16(v0[0], v0[1]), pk_bf16(v0[2], v0[3]), pk_bf16(v1[0], v1[1]), pk_bf16(v1[2], v1[3])};
        st16c(rw, dst + j * 1024, o);
      }
    }
    STAMP_AT(7);
    WAIT_L0(); PIN();
#pragma unroll
    for (int k = KJ; k < 10; ++k) {
      seg(0, false, 0, 8 * NC, 3, k % 3, 1);
      WAIT_L0(); PIN();
      if (k == KJ + AFL) {
        wait_v<0>();
        if (lane == 0) st_flag(FLAG(C_FAB + ((n8 & 3) * 64 + kt) * 2 + wn), tag);
        PIN();
      }
      seg(1, false, 0, 8 * NC, k < 9 ? 3 : 0, (k + 1) % 3, 0);
      WAIT_L0(); PIN();
    }
  }
  MFMA_DONE();
  after_mfma(aG); after_mfma(aU);
  asm volatile("" : "+v"(lane)); q = lane >> 4; rho = lane & 15;
  BAR();
  STAMP_AT(2);
  {
    const float K0 = -2.0f * 0.7978845608028654f * 1.4426950408889634f, K1 = K0 * 0.044715f;
    float mu[4], c0[4], c1[4], v2[4];
#pragma unroll
    for (int i = 0; i < 4; ++i) {
      const int row = TG ? 64 * wm + 16 * i + rho : 64 * wm + 32 * (i >> 1) + 8 * (rho >> 2) + 4 * (i & 1) + (rho & 3);
      const float rs = *(LAS float*)(smem + T_RS + row * 4);
      mu[i] = *(LAS float*)(smem + T_RM + row * 4);
      c0[i] = K0 * rs; c1[i] = K1 * rs * rs * rs; v2[i] = __builtin_amdgcn_rcpf(rs * rs);
    }
#pragma unroll
    for (int J = 0; J < NC / 2; ++J) {
      f32x4 cg[2], cu[2];
#pragma unroll
      for (int t = 0; t < 2; ++t) {
        const int c = 2 * J + t;
        const int o = TG ? (wn * 256 + c * 64 + q * 16) : ((2 * wn + c) * 64 + q * 16);
        cg[t] = *(LAS f32x4*)(smem + T_CS + o);
        cu[t] = *(LAS f32x4*)(smem + T_CS + (TG ? 512 : 256) + o);
      }
#pragma unroll
      for (int i = 0; i < 4; ++i) {
        u32x4 o;
#pragma unroll
        for (int t = 0; t < 2; ++t) {
          const f32x4 g = aG[2 * J + t][i] - cg[t] * mu[i];
          const f32x4 u = aU[2 * J + t][i] - cu[t] * mu[i];
          const f32x4 z = g * (g * g * c1[i] + c0[i]);
          f32x4 e;
#pragma unroll
          for (int r = 0; r < 4; ++r) e[r] = __builtin_amdgcn_exp2f(z[r]);
          e = e * v2[i] + v2[i];
#pragma unroll
          for (int r = 0; r < 4; ++r) e[r] = __builtin_amdgcn_rcpf(e[r]);
          const f32x4 hv = g * u * e;
          o[2 * t] = pk_bf16(hv[0], hv[1]); o[2 * t + 1] = pk_bf16(hv[2], hv[3]);
        }
        const int f0 = hcol0 + (TG ? wn * 64 + J * 32 : wn * 32);
        const int rb = TG ? 4 * wm + i : 4 * wm + 2 * (i >> 1) + (rho >> 3), pos = TG ? lane : 8 * ((rho >> 2) & 1) + 4 * (i & 1) + (rho & 3) + 16 * q;
        st16c(rw, WS_H + (((mt * 24 + (f0 >> 6)) * 8 + rb) * 2 + ((f0 >> 5) & 1)) * 1024 + pos * 16, o);
      }
    }
  }
  STAMP_AT(3);
}

DI void mfma_p2(const Params& P, LAS char* smem, int lane, int w, int split, int mt, int nt, int mt1, int ns, u64 tag) {
  asm volatile("" : "+v"(lane));
  const int wm = w & 1, wn = w >> 1, q = lane >> 4, r15 = lane & 15;
  unsigned vY0 = wm * 8192 + r15 * 128 + ((q ^ (r15 & 7)) * 16), vY1 = vY0 ^ 64;
  unsigned vB0 = L_B + wn * 5120 + lane * 16, vB2 = vB0 + 2 * B_ST;
  asm volatile("" : "+v"(vY0), "+v"(vY1), "+v"(vB0), "+v"(vB2));
  const __amdgpu_buffer_rsrc_t rw = __builtin_amdgcn_make_buffer_rsrc(P.ws, 0, WS_END, 0x00020000);
  const int tile = mt * 4 + nt;
  f32x4 acc[4][5];
  bf16x8 FA[2][4], FB[2][5];
  auto rdy = [&](int s, int k, int ks, int r) {
    const int buf = k & 3;
    if (r >= 1 && r < 5) FA[s][r - 1] = *(LAS bf16x8*)(smem + (ks ? vY1 : vY0) + buf * A_ST + (r - 1) * 2048);
    else { const int j = r ? r - 4 : 0; FB[s][j] = *(LAS bf16x8*)(smem + (buf >= 2 ? vB2 : vB0) + (buf & 1) * B_ST + ks * 10240 + j * 1024); }
  };
  auto seg = [&](auto K, auto HF) __attribute__((always_inline)) {
    constexpr int k = decltype(K)::v, hf = decltype(HF)::v, rk = k + hf, rh = hf ^ 1;
    int r = 0;
#pragma unroll
    for (int m = 0; m < 20; ++m) {
      const int i = (m & 4) ? 3 - (m & 3) : (m & 3);
      mf(acc[i][m >> 2], FB[hf][m >> 2], FA[hf][i], k == 0 && hf == 0);
      if (rk < NY && r < 9 && m % 3 != 2) { rdy(rh, rk, rh, r); ++r; }
      PIN();
    }
  };
  auto publish = [&](int ip) {
    u32x4 o[5];
#pragma unroll
    for (int t = 0; t < 2; ++t) {
      const int i = 2 * ip + t;
#pragma unroll
      for (int j = 0; j < 5; ++j) {
        const int n = j < 4 ? 2 * t + (j >> 1) : 4, e = j < 4 ? (j & 1) * 2 : 2 * t;
        o[n][e] = pk_bf16(acc[i][j][0], acc[i][j][1]); o[n][e + 1] = pk_bf16(acc[i][j][2], acc[i][j][3]);
      }
    }
    if (w == split) {
#pragma unroll
      for (int n = 0; n < 5; ++n) *(LAS u32x4*)(smem + F_Y + (n < 4 ? ip * 4096 + n * 1024 : 8192 + ip * 1024) + lane * 16) = o[n];
    } else {
      const unsigned dst = WS_P + ((tile * 4 + w) * 3 + (split ^ w) - 1) * 10240 + lane * 16;
#pragma unroll
      for (int n = 0; n < 5; ++n) if (!NOPUB) st16c(rw, dst + (n < 4 ? ip * 4096 + n * 1024 : 8192 + ip * 1024), o[n]);
    }
  };
  BAR();
  STAMP_AT(5);
#pragma unroll
  for (int r = 0; r < 9; ++r) rdy(0, 0, 0, r);
  PIN();
  sfor<0, NY>([&](auto K) __attribute__((always_inline)) {
    constexpr int k = decltype(K)::v;
    if (CPK == k) __builtin_amdgcn_s_setprio(3);
    seg(K, IC<0>{});
    if (k == HFL) {
      wait_v<0>();
      if (lane == 0) st_flag(FLAG(C_FH + mt1 * 64 + ns * 4 + w), tag);
    }
    if (k + 1 < NY) {
      WAIT_L0();
      BAR();
      PIN();
    }
    seg(K, IC<1>{});
    WAIT_L0(); PIN();
  });
  MFMA_DONE();
  after_mfma(acc);
  __builtin_amdgcn_s_setprio(3);
  publish(0); publish(1);
  if (HOLD) BAR();
  if (w != split) {
    wait_v<0>();
    if (lane == 0) st_flag(FLAG(C_FP + (tile * 4 + w) * 4 + (split ^ w) - 1), tag);
  }
  const int orow = mt * 128 + (split & 1) * 64 + w * 16, ocol = nt * 160 + (split >> 1) * 80;
  u32x4 xv[3];
  unsigned tr[3], oo[3];
#pragma unroll
  for (int i = 0; i < 3; ++i) {
    const int n = min(i * 64 + lane, 159);
    const unsigned off = ((orow + n / 10) * D + ocol + (n % 10) * 8) * 2;
    xv[i] = *(const u32x4*)((const char*)P.x + off);
    tr[i] = F_T + w * (16 * FPITCH) + (n / 10) * FPITCH + (n % 10) * 16;
    oo[i] = i * 64 + lane < 160 ? off : 0xfffffff0u;
  }
  if (w == split) {
    const u64* fp = FLAG(C_FP + (tile * 4 + split) * 4 + min(lane, 2));
    __builtin_amdgcn_s_sleep(PD0);
#pragma unroll 1
    for (int it = 0; it < 40000; ++it) {
      if (__all(ld_flag(fp) == tag)) break;
      __builtin_amdgcn_s_sleep(PD1);
    }
    WAIT_L0();
    if (lane == 0) *(volatile LAS unsigned*)(smem + T_GO) = 2;
  } else {
    __builtin_amdgcn_s_setprio(0);
#pragma unroll 1
    for (int it = 0; it < 4000000; ++it) {
      if (*(volatile LAS unsigned*)(smem + T_GO) == 2) break;
      __builtin_amdgcn_s_sleep(4);
    }
    __builtin_amdgcn_s_setprio(3);
  }
  asm volatile("" ::: "memory");
  f32x4 ys[5];
  {
    u32x4 pa[3][2]; u32x2 pb[3];
#pragma unroll
    for (int s = 0; s < 3; ++s) {
      const unsigned src = WS_P + ((tile * 4 + split) * 3 + s) * 10240 + lane * 16;
      pa[s][0] = ld16c(rw, src + w * 2048); pa[s][1] = ld16c(rw, src + w * 2048 + 1024);
      pb[s] = __builtin_amdgcn_raw_buffer_load_b64(rw, src + 8192 + (w >> 1) * 1024 + (w & 1) * 8, 0, 16);
    }
#pragma unroll
    for (int j = 0; j < 5; ++j) {
#pragma unroll
      for (int s = 0; s < 3; ++s) {
        const unsigned lo = j < 4 ? pa[s][j >> 1][(j & 1) * 2] : pb[s][0], hi = j < 4 ? pa[s][j >> 1][(j & 1) * 2 + 1] : pb[s][1];
        const f32x4 v = {bflo(lo), bfhi(lo), bflo(hi), bfhi(hi)};
        ys[j] = s ? ys[j] + v : v;
      }
      asm volatile("" : "+v"(ys[j]));
    }
  }
  {
    LAS char* po = smem + F_Y + lane * 16;
    const u32x4 oa0 = *(LAS u32x4*)(po + w * 2048), oa1 = *(LAS u32x4*)(po + w * 2048 + 1024);
    const u32x2 ob = *(LAS u32x2*)(po + 8192 + (w >> 1) * 1024 + (w & 1) * 8);
#pragma unroll
    for (int j = 0; j < 5; ++j) {
      const unsigned l0 = j < 2 ? oa0[(j & 1) * 2] : j < 4 ? oa1[(j & 1) * 2] : ob[0], h0 = j < 2 ? oa0[(j & 1) * 2 + 1] : j < 4 ? oa1[(j & 1) * 2 + 1] : ob[1];
      ys[j] += (f32x4){bflo(l0), bfhi(l0), bflo(h0), bfhi(h0)};
      asm volatile("" : "+v"(ys[j]));
    }
  }
  unsigned tw = F_T + w * (16 * FPITCH) + r15 * FPITCH + q * 8, rd0 = w * 5120 + lane * 16, rd2 = rd0 + L_B;
  asm volatile("" : "+v"(tw), "+v"(rd0), "+v"(rd2), "+v"(tr[0]), "+v"(tr[1]), "+v"(tr[2]), "+v"(oo[0]), "+v"(oo[1]), "+v"(oo[2]));
  STAMP_AT(6);
  BAR();
  f32x4 dd[4][5];
#pragma unroll
  for (int j = 0; j < 5; ++j)
#pragma unroll
    for (int u = 0; u < 4; ++u) dd[u][j] = *(LAS f32x4*)(smem + ((u & 2) ? rd2 : rd0) + (u & 1) * 20480 + j * 1024);
#pragma unroll
  for (int j = 0; j < 5; ++j) {
    const f32x4 d = (dd[0][j] + dd[1][j]) + (dd[2][j] + dd[3][j]);
    f32x4 e = d * -1.4426950408889634f;
#pragma unroll
    for (int r = 0; r < 4; ++r) e[r] = __builtin_amdgcn_exp2f(e[r]);
    e = e + 1.0f;
#pragma unroll
    for (int r = 0; r < 4; ++r) e[r] = __builtin_amdgcn_rcpf(e[r]);
    e = e * d * ys[j];
    *(LAS u32x2*)(smem + tw + j * 32) = (u32x2){pk_bf16(e[0], e[1]), pk_bf16(e[2], e[3])};
  }
  WAIT_L0();
  const __amdgpu_buffer_rsrc_t ro = __builtin_amdgcn_make_buffer_rsrc(P.out, 0, S * D * 2, 0x00020000);
#pragma unroll
  for (int i = 0; i < 3; ++i) {
    const u32x4 v = *(LAS u32x4*)(smem + tr[i]);
    u32x4 o;
#pragma unroll
    for (int d = 0; d < 4; ++d) o[d] = pk_bf16(bflo(v[d]) + bflo(xv[i][d]), bfhi(v[d]) + bfhi(xv[i][d]));
    __builtin_amdgcn_raw_buffer_store_b128(o, ro, oo[i], 0, 0);
  }
}

__global__ __launch_bounds__(512, 2)
void fused(Params P) {
  extern __shared__ __attribute__((aligned(16))) char smem_[];
  LAS char* smem = (LAS char*)smem_;
  const int tid = threadIdx.x, lane = tid & 63, w = __builtin_amdgcn_readfirstlane(tid >> 6);
  const int bid = blockIdx.x, xcd = bid & 7, idx = bid >> 3;
  u64 tag;
  STAMP_AT(0);
  const int mt1 = (xcd & 1) * 8 + (idx & 7), j = idx >> 3, ns = (j & 1) * 8 + (xcd >> 1) * 2 + (j >> 1);
  const int split = xcd >> 1, mt2 = (xcd & 1) * 8 + (idx >> 2), nt = idx & 3;
  if (w >= 4) {
    __builtin_amdgcn_s_setprio(HPRIO);
    helper(P, smem, lane, w & 3, mt1, ns, split, mt2, nt);
  } else {
    if (j & 1) mfma_p1<false>(P, smem, lane, w, mt1, ns, tag); else mfma_p1<true>(P, smem, lane, w, mt1, ns, tag);
    mfma_p2(P, smem, lane, w, split, mt2, nt, mt1, ns, tag);
    __builtin_amdgcn_s_waitcnt(0);
  }
}

extern "C" void kernel_launch(void* const* d_in, const int* in_sizes, int n_in, void* d_out, int out_size, void* d_ws, size_t ws_size, hipStream_t stream) {
  Params P;
  P.x = (const bf16_t*)d_in[0]; P.wsr = (const bf16_t*)d_in[1]; P.wsi = (const bf16_t*)d_in[2];
  P.wdr = (const bf16_t*)d_in[3]; P.wdi = (const bf16_t*)d_in[4];
  P.wg = (const bf16_t*)d_in[6]; P.wu = (const bf16_t*)d_in[7]; P.wdn = (const bf16_t*)d_in[8];
  P.out = (bf16_t*)d_out; P.ws = (char*)d_ws;
  (void)hipFuncSetAttribute((const void*)fused, hipFuncAttributeMaxDynamicSharedMemorySize, LDS_BYTES);
  fused<<<256, 512, LDS_BYTES, stream>>>(P);
}
```

```cpp
#include <hip/hip_runtime.h>
#include <hip/hip_bf16.h>
typedef unsigned short bf16_t;
typedef unsigned long long u64;
typedef short bf16x8 __attribute__((ext_vector_type(8)));
typedef short s16x4 __attribute__((ext_vector_type(4)));
typedef float f32x4 __attribute__((ext_vector_type(4)));
typedef float f32x2 __attribute__((ext_vector_type(2)));
typedef unsigned u32x4 __attribute__((ext_vector_type(4)));
typedef unsigned u32x2 __attribute__((ext_vector_type(2)));
typedef __bf16 bf16x2_t __attribute__((ext_vector_type(2)));
#define LAS __attribute__((address_space(3)))
#define GAS __attribute__((address_space(1)))
#define DI __device__ __forceinline__

#define STAMP 0
#define NPRE 2
#define ROT2 0
#define YFL 4
#define FABSL 1
#define AFL 2
#define HFL 2
#define PSPL 1
#define XG 2
#define XY 1
#define XD 1
#define YSB 2
#define WK 2
#define FABM 0
#define SK 5
#define HOLD 0
#define PD0 60
#define PD1 8
#define DPF 0
#define DFK 8
#define DP0 4
#define DPS 2
#define NOPUB 0
#define DPRIO 2
#define CPK 99
#define S0E 2
#define TPRIO 3
#define B0E 0
#define PFG 1
#define PFM 2
#define PH0 0
#define PHS 4
#define PFS 10
#define YPL 8
#define XROT 0
#define HPRIO 0
#define PFN 4

constexpr int S = 2048, D = 640, F = 1536;
constexpr unsigned WS_AB = 0, WS_H = WS_AB + 2 * S * D * 2, WS_P = WS_H + S * F * 2, WS_WD = WS_P + 3 * S * D * 2, WS_RS = WS_WD + F * D * 2,
                   WS_CS = WS_RS + S * 8, WS_CD = WS_CS + 16 * 4 * 16 * 64 * 4, WS_CTRL = WS_CD + 8192, WS_END = WS_CTRL + 5120 * 128;
constexpr int C_E = 0, C_F0 = 1, C_FAB = C_F0 + 1024, C_FH = C_FAB + 512, C_FW = C_FH + 1024, C_FP = C_FW + 512;
#define FLAG(i) ((u64*)(P.ws + WS_CTRL) + (i) * 16)

DI unsigned pk_bf16(float lo, float hi) { f32x2 v = {lo, hi}; return __builtin_bit_cast(unsigned, __builtin_convertvector(v, bf16x2_t)); }
DI float rbf(float f) { return __uint_as_float(pk_bf16(f, f) << 16); }
DI float bflo(unsigned u) { return __uint_as_float(u << 16); }
DI float bfhi(unsigned u) { return __uint_as_float(u & 0xffff0000u); }
DI float dot2(unsigned a, unsigned b, float c) { return __builtin_amdgcn_fdot2_f32_bf16(__builtin_bit_cast(bf16x2_t, a), __builtin_bit_cast(bf16x2_t, b), c, false); }
#define GLDS4(POL) asm volatile("s_mov_b32 m0, %0\n\ts_nop 0\n\tglobal_load_lds_dwordx4 %2, %1" POL "\n\tglobal_load_lds_dwordx4 %3, %1 offset:1024" POL \
    "\n\tglobal_load_lds_dwordx4 %4, %1 offset:2048" POL "\n\tglobal_load_lds_dwordx4 %5, %1 offset:3072" POL \
    :: "s"(lds), "s"(base), "v"(v0), "v"(v1), "v"(v2), "v"(v3) : "memory")
#define GLDS5(POL) asm volatile("s_mov_b32 m0, %0\n\ts_nop 0\n\tglobal_load_lds_dwordx4 %2, %1" POL "\n\tglobal_load_lds_dwordx4 %3, %1 offset:1024" POL \
    "\n\tglobal_load_lds_dwordx4 %4, %1 offset:2048" POL "\n\tglobal_load_lds_dwordx4 %5, %1 offset:3072" POL \
    "\n\ts_add_u32 m0, %0, 0x1000\n\ts_nop 0\n\tglobal_load_lds_dwordx4 %6, %1" POL \
    :: "s"(lds), "s"(base), "v"(v0), "v"(v1), "v"(v2), "v"(v3), "v"(v4) : "memory", "scc")
template <bool DEV> DI void glds4(unsigned lds, const char* base, unsigned v0, unsigned v1, unsigned v2, unsigned v3) {
  if (DEV) GLDS4(" sc1"); else GLDS4("");
}
template <bool DEV> DI void glds5(unsigned lds, const char* base, unsigned v0, unsigned v1, unsigned v2, unsigned v3, unsigned v4) {
  if (DEV) GLDS5(" sc1"); else GLDS5("");
}
template <bool DEV = false> DI void touch_lines(unsigned lds, unsigned voff, const char* base) {
  if (DEV) asm volatile("s_mov_b32 m0, %0\n\ts_nop 0\n\tglobal_load_lds_dword %1, %2 sc1" :: "s"(lds), "v"(voff), "s"(base) : "memory");
  else asm volatile("s_mov_b32 m0, %0\n\ts_nop 0\n\tglobal_load_lds_dword %1, %2" :: "s"(lds), "v"(voff), "s"(base) : "memory");
}
template <int N> DI void wait_v() { asm volatile("s_waitcnt vmcnt(%0)" :: "i"(N) : "memory"); }
#define WAIT_L0() do { __builtin_amdgcn_s_waitcnt(0xC07F); asm volatile("" ::: "memory"); } while (0)
#define BAR() do { asm volatile("" ::: "memory"); __builtin_amdgcn_s_barrier(); asm volatile("" ::: "memory"); } while (0)
#define SGB(m, n) __builtin_amdgcn_sched_group_barrier(m, n, 0)
#define PIN() __builtin_amdgcn_sched_barrier(0)
template <int V> struct IC { static constexpr int v = V; };
template <int I, int N, class Fn> DI void sfor(Fn&& f) { if constexpr (I < N) { f(IC<I>{}); sfor<I + 1, N>(f); } }
DI void mf(f32x4& c, const bf16x8& a, const bf16x8& b, bool first) {
  if (first) asm volatile("v_mfma_f32_16x16x32_bf16 %0, %1, %2, 0" : "=&v"(c) : "v"(a), "v"(b));
  else asm volatile("v_mfma_f32_16x16x32_bf16 %0, %1, %2, %0" : "+v"(c) : "v"(a), "v"(b));
}
#define MFMA_DONE() asm volatile("s_nop 15")
template <int A, int B> DI void after_mfma(f32x4 (&c)[A][B]) {
#pragma unroll
  for (int i = 0; i < A; ++i)
#pragma unroll
    for (int j = 0; j < B; ++j) asm volatile("" : "+v"(c[i][j]));
}
#define STAMP_AT(i)

template <int CTRL> DI float dpp_add(float v) { return v + __builtin_bit_cast(float, __builtin_amdgcn_update_dpp(0, __builtin_bit_cast(int, v), CTRL, 0xf, 0xf, true)); }
DI float sum8(float v) { v = dpp_add<0xB1>(v); v = dpp_add<0x4E>(v); return dpp_add<0x141>(v); }
DI float sum16(float v) { return dpp_add<0x140>(sum8(v)); }
DI float xchg16(float keep_lo, float keep_hi) {
  const auto r = __builtin_amdgcn_permlane16_swap(__float_as_uint(keep_lo), __float_as_uint(keep_hi), false, false);
  return __uint_as_float(r[0]) + __uint_as_float(r[1]);
}
DI float xchg32(float keep_lo, float keep_hi) {
  const auto r = __builtin_amdgcn_permlane32_swap(__float_as_uint(keep_lo), __float_as_uint(keep_hi), false, false);
  return __uint_as_float(r[0]) + __uint_as_float(r[1]);
}
#define TIC(v)
#define ACC(a, x, y)
#define PUT(slot, a)
DI u64 ld_flag(const u64* p) { return __hip_atomic_load((const GAS u64*)p, __ATOMIC_RELAXED, __HIP_MEMORY_SCOPE_AGENT); }
DI void st_flag(u64* p, u64 v) { __hip_atomic_store((GAS u64*)p, v, __ATOMIC_RELAXED, __HIP_MEMORY_SCOPE_AGENT); }
DI void wait_flags(const u64* p, u64 tag) {
#pragma unroll 1
  for (int it = 0; it < 60000; ++it) {
    if (__all(ld_flag(p) == tag)) break;
    __builtin_amdgcn_s_sleep(4);
  }
  asm volatile("" ::: "memory");
}
DI void wait_flags2(const u64* p, const u64* p2, u64 tag) {
#pragma unroll 1
  for (int it = 0; it < 60000; ++it) {
    const u64 a = ld_flag(p), b = ld_flag(p2);
    if (__all(a == tag && b == tag)) break;
    __builtin_amdgcn_s_sleep(4);
  }
  asm volatile("" ::: "memory");
}
DI u64 read_tag(const char* ws) {
  u64 e;
  asm volatile("s_load_dwordx2 %0, %1, 0x0\n\ts_waitcnt lgkmcnt(0)" : "=s"(e) : "s"(ws + WS_CTRL) : "memory");
  return e * 6364136223846793005ull + 1442695040888963407ull;
}
DI void st16c(__amdgpu_buffer_rsrc_t r, unsigned off, u32x4 v) { __builtin_amdgcn_raw_buffer_store_b128(v, r, off, 0, 16); }
DI u32x4 ld16c(__amdgpu_buffer_rsrc_t r, unsigned off) { return __builtin_amdgcn_raw_buffer_load_b128(r, off, 0, 16); }

struct Params {
  const bf16_t *x, *wsr, *wsi, *wdr, *wdi, *wg, *wu, *wdn;
  bf16_t* out; char* ws;
};

constexpr int L_W = 49152, L_D = 98304, K1_TAB = 159744, LDS_BYTES = K1_TAB + 4096;
constexpr int T_RS = K1_TAB, T_RM = K1_TAB + 512, T_CS = K1_TAB + 1024, T_CD = K1_TAB + 2048, T_GO = K1_TAB + 3584, T_PF = K1_TAB + 3072;
constexpr int A_ST = 16384, B_ST = 20480, L_B = 4 * A_ST;

constexpr int NY = 16;
constexpr int F_Y = 147456, F_T = 106496, FPITCH = 176;

DI int f_red(int lw) { return (lw >> 1) * L_B + (lw & 1) * 20480; }

DI void helper(const Params& P, LAS char* smem, int lane, int lw, int mt, int ns, int split, int mt2, int nt) {
  constexpr int NL = 12;
  const bool TG = ns < 8;
  const int n8 = ns & 7, hcol0 = TG ? 128 * n8 : 1024 + 64 * n8;
  const int rr = lane >> 3, c8 = lane & 7;
  const unsigned lds0 = (unsigned)(size_t)smem;
  unsigned vX[4], vW[4], vD[5];
#pragma unroll
  for (int j = 0; j < 4; ++j) {
    vX[j] = (32 * lw + 8 * j + rr) * 1280 + ((c8 ^ (TG ? rr : ((rr & 2) | ((j & 2) << 1)))) * 16) - j * 1024;
    vW[j] = (8 * j + rr) * 3072 + ((c8 ^ ((rr & 2) | ((j & 1) << 2))) * 16) - j * 1024;
  }
#pragma unroll
  for (int j = 0; j < 5; ++j) vD[j] = (40 * lw + 8 * j + rr) * 1280 + ((c8 ^ rr) * 16) - (j & 3) * 1024;
  const char* bx = (const char*)P.x + (size_t)mt * 128 * 1280;
  const char* bw = (const char*)((lw >> 1) ? P.wu : P.wg) + hcol0 * 2 + (TG ? (lw & 1) * 128 : (lw & 1) * (32 * 3072));
  const char* bd = (const char*)(n8 >= 4 ? P.wdi : P.wdr) + (size_t)(n8 & 3) * 160 * 1280;
  const unsigned ldsX = __builtin_amdgcn_readfirstlane(lds0 + lw * 4096);
  const unsigned ldsW = __builtin_amdgcn_readfirstlane(lds0 + L_W + (TG ? (lw & 1) * 49152 + (lw >> 1) * 8192 : lw * 4096));
  const unsigned ldsD = __builtin_amdgcn_readfirstlane(lds0 + L_D + lw * 5120);
  auto issue_a = [&](int kt) {
    const int buf = kt % 3;
    glds4<false>(ldsX + buf * 16384, bx + kt * 128, vX[0], vX[1], vX[2], vX[3]);
    glds4<false>(ldsW + buf * 16384, bw + kt * (64 * 3072), vW[0], vW[1], vW[2], vW[3]);
  };
  auto issue_b = [&](int kt) {
    const int buf = kt % 3;
    if (TG) glds4<false>(ldsW + buf * 16384 + 4096, bw + kt * (64 * 3072) + 32 * 3072, vW[0], vW[1], vW[2], vW[3]);
    else glds5<false>(ldsD + buf * 20480, bd + kt * 128, vD[0], vD[1], vD[2], vD[3], vD[4]);
  };
  auto issue = [&](int kt) { issue_a(kt); issue_b(kt); };
  issue(0);
  issue_a(1);
  u64 tag;
  if (!S0E) issue_b(1);
  const char* pfb = (const char*)(split < 2 ? P.wsr : P.wsi) + (size_t)(mt2 * 128 + nt * 32) * 4096 + (split & 1) * 2048;
  const unsigned pfv = (lane & 31) * 4096 + (lw * 2 + (lane >> 5)) * 128;
  const u64* fa = FLAG(C_F0 + (lw == 0 ? mt * 64 + (lane & 31) : (((lane >> 1) & 15) * 16 + n8) * 4 + 2 + (lane & 1)));
  u64 fav = 0;
  const int xg = ((mt2 ^ nt) & 1) * XG;
  const unsigned vL = lane * 16, vL4 = vL + 4096;
  const char* baseWD = P.ws + WS_WD + ((nt * 48 + lw) * 10 + (split >> 1) * 5) * 1024;
  const int wtask = (mt * 8 + n8) * 4 + lw;
  const bool wdo = TG && wtask < 480;
  unsigned wv[16];
#pragma unroll
  for (int e = 0; e < 16; ++e) asm volatile("" : "=v"(wv[e]));
  sfor<0, 8>([&](auto K) __attribute__((always_inline)) {
    constexpr int k = decltype(K)::v;
    if (S0E && k == 0) wait_v<8>(); else if (S0E == 2 && k == 1) wait_v<8>(); else if (k == WK + 1 && wdo) wait_v<12 + 16>(); else if (k == WK + 3 && wdo) wait_v<12 + 4>(); else if (TG) wait_v<12>(); else wait_v<13>();
    BAR();
    if (S0E && k == 0) issue_b(1);
    if (S0E == 2 && k == 1) issue_b(2);
    if (lw < (TG ? 1 : 2)) {
      if (k == SK) asm volatile("global_load_dwordx2 %0, %1, off sc1" : "+v"(fav) : "v"(fa) : "memory");
      if (k == SK + 1) {
        asm volatile("" : "+v"(fav));
        if (!__all(fav == tag)) wait_flags(fa, tag);
        const char* src = P.ws + (lw == 0 ? WS_RS + mt * 1024 : WS_CD + n8 * 640);
        asm volatile("s_mov_b32 m0, %0\n\ts_nop 0\n\tglobal_load_lds_dwordx4 %1, %2 sc1" :: "s"(lds0 + (lw == 0 ? T_RS : T_CD)), "v"(vL), "s"(src) : "memory");
      }
    }
    if (S0E == 2 && k == 0) issue_a(2); else issue(k + 2);
    if (k == WK && wdo) {
#pragma unroll
      for (int u = 0; u < 4; ++u) {
        const int task = wtask * 2 + (u >> 1);
        const unsigned od = (((task % 240) / 5) * 32 + (lane >> 4) * 4) * 1280 + ((task / 240) * 160 + (task % 5) * 32) * 2 + (lane & 15) * 4;
        const char* pd = (const char*)P.wdn + (u & 1) * (16 * 1280);
        asm volatile("global_load_dword %0, %4, %5\n\tglobal_load_dword %1, %4, %5 offset:1280\n\tglobal_load_dword %2, %4, %5 offset:2560\n\tglobal_load_dword %3, %4, %5 offset:3840"
                     : "+v"(wv[4 * u]), "+v"(wv[4 * u + 1]), "+v"(wv[4 * u + 2]), "+v"(wv[4 * u + 3]) : "v"(od), "s"(pd) : "memory");
      }
    }
    if (k == WK + 2 && wdo) {
      const __amdgpu_buffer_rsrc_t rw = __builtin_amdgcn_make_buffer_rsrc(P.ws, 0, WS_END, 0x00020000);
      const bool up = lane & 8;
#pragma unroll
      for (int u = 0; u < 2; ++u) {
        u32x4 wo[2];
#pragma unroll
        for (int e = 0; e < 4; ++e) {
          asm volatile("" : "+v"(wv[8 * u + 2 * e]), "+v"(wv[8 * u + 2 * e + 1]));
          const unsigned lo = __builtin_amdgcn_perm(wv[8 * u + 2 * e + 1], wv[8 * u + 2 * e], 0x05040100u), hi = __builtin_amdgcn_perm(wv[8 * u + 2 * e + 1], wv[8 * u + 2 * e], 0x07060302u);
          const unsigned t = __builtin_amdgcn_update_dpp(0, up ? lo : hi, 0x128, 0xf, 0xf, true);
          wo[0][e] = up ? t : lo;
          wo[1][e] = up ? hi : t;
        }
        const int task = wtask * 2 + u;
        const unsigned dst = WS_WD + (((task / 240) * 48 + (task % 240) / 5) * 10 + (task % 5) * 2) * 1024 + (2 * (lane & 7) + ((lane >> 3) & 1) + (lane & 48)) * 16;
        st16c(rw, dst, wo[0]); st16c(rw, dst + 1024, wo[1]);
      }
    }
    if (k == 0) tag = read_tag(P.ws);
    if ((TG || PFM == 0) && k == PH0 + PHS * (lw >> 1) + (mt & 1)) touch_lines(lds0 + T_PF, pfv, pfb);
    if (k == 7) touch_lines(lds0 + T_PF, pfv, pfb + ((TG || PFM == 0) ? 1024 : 0));
  });
  wait_v<NL>();
  BAR();
  wait_v<1>();
  BAR();
  if (wdo && lane == 0) st_flag(FLAG(C_FW + wtask), tag);
  if (TG || lw < 2) {
    wait_flags(FLAG(C_F0 + ((lane & 15) * 16 + n8) * 4 + lw), tag);
    const __amdgpu_buffer_rsrc_t rw = __builtin_amdgcn_make_buffer_rsrc(P.ws, 0, WS_END, 0x00020000);
    f32x4 c = {0, 0, 0, 0};
#pragma unroll
    for (int i = 0; i < 4; ++i) c += __builtin_bit_cast(f32x4, ld16c(rw, WS_CS + (ns * 4 + lw) * 4096 + i * 1024 + lane * 16));
#pragma unroll
    for (int e = 0; e < 4; ++e) { c[e] = xchg16(c[e], c[e]); c[e] = xchg32(c[e], c[e]); }
    if (lane < 16) *(LAS f32x4*)(smem + T_CS + lw * 256 + lane * 16) = c;
  }
  if (lw == 0 && lane == 0) *(volatile LAS unsigned*)(smem + T_GO) = 0;
  WAIT_L0();
  if (!TG && PFM == 1) { if (lw < 2) __builtin_amdgcn_s_sleep(PFS); else __builtin_amdgcn_s_sleep(PFS + 24); touch_lines(lds0 + T_PF, pfv, pfb + 1024); }
  if (blockIdx.x == 64 && lw == 3) {
    wait_flags2(FLAG(C_F0 + lane * 4), FLAG(C_F0 + (64 + lane) * 4), tag);
    wait_flags2(FLAG(C_F0 + (128 + lane) * 4), FLAG(C_F0 + (192 + lane) * 4), tag);
    if (lane == 0) st_flag(FLAG(C_E), tag);
  }
  const u64* fab = FLAG(C_FAB + (nt * 64 + split * 16) * 2 + (lane & 31));
  unsigned vY[4];
#pragma unroll
  for (int j = 0; j < 4; ++j) vY[j] = (32 * lw + 8 * j + rr) * 4096 + ((c8 ^ rr) * 16) - j * 1024;
  const char* baseY = (const char*)(split < 2 ? P.wsr : P.wsi) + (size_t)mt2 * 128 * 4096 + (split & 1) * 2048;
  const char* baseAB = P.ws + WS_AB + (nt * 64 + split * 16) * B_ST + lw * 5120;
  const char* baseH = P.ws + WS_H + ((mt2 * 24 + (lw >> 1)) * 16 + (split & 1) * 8 + (lw & 1)) * 1024;
  const unsigned ldsA = ldsX, ldsB = __builtin_amdgcn_readfirstlane(lds0 + L_B + lw * 5120);
  auto issueYA = [&](int k, int yi) { glds4<false>(ldsA + (k & 3) * A_ST, baseY + (yi ^ xg) * 128, vY[0], vY[1], vY[2], vY[3]); };
  auto issueYB = [&](int k, int yi) { glds5<true>(ldsB + (k & 3) * B_ST, baseAB + (yi ^ xg) * B_ST, vL, vL, vL, vL, vL4); };
  BAR();
  __builtin_amdgcn_s_setprio(TPRIO);
  STAMP_AT(8);
  u64 fabv;
  if (FABM) asm volatile("global_load_dwordx2 %0, %1, off sc1" : "=&v"(fabv) : "v"(fab) : "memory"); else fabv = ~tag;
  issueYA(0, 0); issueYA(1, 1);
  if (FABM) wait_v<8>();
  asm volatile("" : "+v"(fabv));
  const bool ab_seen = __all(fabv == tag);
  if (lw == 0) {
    if (!ab_seen) wait_flags(fab, tag);
    if (lane == 0) *(volatile LAS unsigned*)(smem + T_GO) = 1;
  } else if (!ab_seen) {
#pragma unroll 1
    for (int it = 0; it < 4000000; ++it) {
      if (*(volatile LAS unsigned*)(smem + T_GO) != 0) break;
      __builtin_amdgcn_s_sleep(1);
    }
  }
  asm volatile("" ::: "memory");
  STAMP_AT(9);
  const u64* fh = FLAG(C_FH + mt2 * 64 + (lane & 31) * 2 + (split & 1));
  const int l36 = min(lane, 35);
  const int wt = (nt * 240 + ((l36 / 3) * 4 + lw) * 5 + (split >> 1) * 2 + l36 % 3) >> 1;
  const u64* fw = FLAG(C_FW + wt);
  u64 fhv = 0, fwv = 0;
  issueYB(0, 0);
  if (B0E) { wait_v<0>(); BAR(); issueYB(1, 1); } else { issueYB(1, 1); wait_v<5>(); BAR(); }
  issueYA(2, 2); issueYB(2, 2);
  wait_v<9>(); BAR();
  __builtin_amdgcn_s_setprio(0);
  issueYA(3, 3); issueYB(3, 3); issueYA(4, 4); issueYB(4, 4);
  const int ph = nt * 32 + lane, pw = (mt2 & 7) * 20 + min(max(lane - 32, 0), 19);
  const unsigned dstr = lane < 32 ? 2 * A_ST : 2 * B_ST;
  unsigned dpf = (lane < 32 ? WS_H + ((mt2 * 24 + (ph >> 6)) * 16 + (split & 1) * 8) * 1024 + (ph & 63) * 128
                            : WS_WD + ((nt * 48 + pw / 40) * 10 + (split >> 1) * 5) * 1024 + (pw % 40) * 128) + (DP0 + lw) * dstr;
#pragma unroll 1
  for (int k = 2; k < NY - 3; ++k) {
    if (k == DFK + 1) {
      wait_v<9>();
      asm volatile("" : "+v"(fhv), "+v"(fwv));
      if (!__all(fhv == tag && fwv == tag)) wait_flags2(fh, fw, tag);
    } else wait_v<18>();
    BAR();
    if (k == DFK) asm volatile("global_load_dwordx2 %0, %2, off sc1\n\tglobal_load_dwordx2 %1, %3, off sc1" : "+v"(fhv), "+v"(fwv) : "v"(fh), "v"(fw) : "memory");
    issueYA(k + 3, k + 3); issueYB(k + 3, k + 3);
    if (DPF) {
      const int n0 = (k - (DFK + 1)) * DPS;
      const int n = n0 + ((lw - n0) & 3);
      if (n0 >= 0 && n < n0 + DPS && DP0 + n < 12) { touch_lines<true>(lds0 + T_PF, dpf, P.ws); dpf += 4 * dstr; }
    }
  }
  bf16x8 GA[4][4], GB[4][5];
  f32x4 dac[4][5];
  auto loadD = [&](int i) {
    const int s = i & 3;
    const char* pa = baseH + (i ^ xg) * (2 * A_ST);
    const char* pb = baseWD + (i ^ xg) * (2 * B_ST);
    asm volatile("global_load_dwordx4 %0, %9, %12 sc1\n\tglobal_load_dwordx4 %4, %9, %11 sc1\n\tglobal_load_dwordx4 %5, %9, %11 offset:2048 sc1\n\t"
                 "global_load_dwordx4 %6, %10, %11 sc1\n\tglobal_load_dwordx4 %7, %10, %11 offset:2048 sc1\n\t"
                 "global_load_dwordx4 %1, %9, %12 offset:1024 sc1\n\tglobal_load_dwordx4 %2, %9, %12 offset:2048 sc1\n\t"
                 "global_load_dwordx4 %3, %9, %12 offset:3072 sc1\n\tglobal_load_dwordx4 %8, %10, %12 sc1"
                 : "=&v"(GB[s][0]), "=&v"(GB[s][1]), "=&v"(GB[s][2]), "=&v"(GB[s][3]), "=&v"(GA[s][0]), "=&v"(GA[s][1]), "=&v"(GA[s][2]), "=&v"(GA[s][3]), "=&v"(GB[s][4])
                 : "v"(vL), "v"(vL4), "s"(pa), "s"(pb) : "memory");
  };
  auto load1 = [&](int i, int n) {
    const int s = i & 3;
    const char* pa = baseH + (i ^ xg) * (2 * A_ST);
    const char* pb = baseWD + (i ^ xg) * (2 * B_ST);
    if (n < 4) asm volatile("global_load_dwordx4 %0, %1, %2 offset:%3 sc1" : "=&v"(GA[s][n]) : "v"(n < 2 ? vL : vL4), "s"(pa), "i"((n & 1) * 2048) : "memory");
    else asm volatile("global_load_dwordx4 %0, %1, %2 offset:%3 sc1" : "=&v"(GB[s][n - 4]) : "v"(n < 8 ? vL : vL4), "s"(pb), "i"(((n - 4) & 3) * 1024) : "memory");
  };
  auto group = [&](auto I) __attribute__((always_inline)) {
    constexpr int i = decltype(I)::v, s = i & 3;
    wait_v<(i == 0 ? 27 : i < 10 ? 18 : (11 - i) * 9)>();
    asm volatile("" : "+v"(GA[s][0]), "+v"(GA[s][1]), "+v"(GA[s][2]), "+v"(GA[s][3]), "+v"(GB[s][0]), "+v"(GB[s][1]), "+v"(GB[s][2]), "+v"(GB[s][3]), "+v"(GB[s][4]));
#pragma unroll
    for (int m = 0; m < 20; ++m) {
      const int r = (m & 4) ? 3 - (m & 3) : (m & 3);
      mf(dac[r][m >> 2], GB[s][m >> 2], GA[s][r], i == 0);
      if (i >= 1 && i + 3 < 12 && (m & 1) == 1 && m < 18) load1(i + 3, m >> 1);
    }
  };
  loadD(0);
  wait_v<27>(); BAR();
  loadD(1);
  wait_v<27>(); BAR();
  loadD(2);
  wait_v<27>(); BAR();
  loadD(3);
  __builtin_amdgcn_s_setprio(DPRIO);
  sfor<0, 12>(group);
  MFMA_DONE();
  after_mfma(dac);
#pragma unroll
  for (int r = 0; r < 4; ++r)
#pragma unroll
    for (int j = 0; j < 5; ++j) *(LAS f32x4*)(smem + f_red(lw) + (r * 5 + j) * 1024 + lane * 16) = dac[r][j];
  WAIT_L0();
  STAMP_AT(11);
  BAR();
  STAMP_AT(10);
}

template <bool TG>
DI void mfma_p1(const Params& P, LAS char* smem, int lane, int w, int mt, int ns, u64& tag) {
  const int n8 = ns & 7, hcol0 = TG ? 128 * n8 : 1024 + 64 * n8;
  const __amdgpu_buffer_rsrc_t rw = __builtin_amdgcn_make_buffer_rsrc(P.ws, 0, WS_END, 0x00020000);
  int q = lane >> 4, rho = lane & 15;
  const int rr = lane >> 3, c8 = lane & 7;
  u32x4 vx[5], vw[5], vm[10];
  if (TG) {
    const char* px = (const char*)P.x + (size_t)(mt * 128 + n8 * 16 + 4 * w + q) * 1280 + rho * 16;
#pragma unroll
    for (int i = 0; i < 5; ++i) vx[i] = *(const u32x4*)(px + i * 256);
    const char* pw = (const char*)((w >> 1) ? P.wu : P.wg) + (size_t)(40 * mt + rr) * 3072 + (hcol0 + (w & 1) * 64) * 2 + c8 * 16;
#pragma unroll
    for (int i = 0; i < 5; ++i) vw[i] = *(const u32x4*)(pw + i * (8 * 3072));
    if (w < 2) {
      const char* pm = (const char*)(w ? P.wu : P.wg) + (size_t)(40 * mt + rr) * 3072 + (1024 + 64 * n8) * 2 + c8 * 16;
#pragma unroll
      for (int i = 0; i < 5; ++i) vm[i] = *(const u32x4*)(pm + i * (8 * 3072));
    } else {
      const int e = (n8 & 3) * 160 + 10 * mt + 5 * (w - 2) + min(rr, 4);
      const char* pd = (const char*)(n8 >= 4 ? P.wdi : P.wdr) + (size_t)e * 1280 + c8 * 16;
#pragma unroll
      for (int i = 0; i < 10; ++i) vm[i] = *(const u32x4*)(pd + i * 128);
    }
  }
  tag = read_tag(P.ws);
  const int wm = w & 1, wn = w >> 1;
  constexpr int NC = TG ? 4 : 2;
  unsigned vX0 = TG ? wm * 8192 + rho * 128 + ((q ^ (rho & 7)) * 16)
                          : wm * 8192 + (8 * (rho >> 2) + (rho & 3)) * 128 + ((q ^ ((rho & 2) | ((rho >> 1) & 4))) * 16);
  unsigned vX1 = vX0 ^ 64;
  unsigned vW[NC];
  {
    const int qq = rho >> 2, p = lane & 3, swz = (qq & 2) | ((q & 1) << 2);
#pragma unroll
    for (int c = 0; c < NC; ++c) {
      const int cc = TG ? c : (2 * wn + c);
      vW[c] = L_W + (TG ? wn * 49152 : 0) + (8 * q + qq) * 128 + ((((2 * cc) ^ swz) + (p >> 1)) * 16) + 8 * (p & 1);
    }
  }
  unsigned vD0 = L_D + (80 * wn + rho) * 128 + ((q ^ (rho & 7)) * 16), vD1 = vD0 ^ 64;
  asm volatile("" : "+v"(vX0), "+v"(vX1));
  if (!TG) asm volatile("" : "+v"(vD0), "+v"(vD1));
#pragma unroll
  for (int c = 0; c < NC; ++c) asm volatile("" : "+v"(vW[c]));
  f32x4 aG[NC][4], aU[NC][4], aA[4][5];
  if (TG) {
    unsigned ONE2 = 0x3f803f80u, SLO = 0x00003f80u, SHI = 0x3f800000u;
    asm volatile("" : "+v"(ONE2), "+v"(SLO), "+v"(SHI));
    float sx[2] = {0, 0}, sq[2] = {0, 0};
#pragma unroll
    for (int i = 0; i < 5; ++i)
#pragma unroll
      for (int e = 0; e < 4; ++e) { sx[e & 1] = dot2(vx[i][e], ONE2, sx[e & 1]); sq[e & 1] = dot2(vx[i][e], vx[i][e], sq[e & 1]); }
    const float mean = sum16(sx[0] + sx[1]) * (1.0f / D);
    const float var = fmaxf(sum16(sq[0] + sq[1]) * (1.0f / D) - mean * mean, 0.f);
    const float sd = rbf(__builtin_amdgcn_sqrtf(rbf(rbf(var) + 1e-5f)));
    float rso = (lane & 1) ? rbf(mean) : __builtin_amdgcn_rcpf(sd);
    auto colsum = [&](const u32x4* v) -> float {
      float c[8] = {0, 0, 0, 0, 0, 0, 0, 0};
#pragma unroll
      for (int i = 0; i < 5; ++i)
#pragma unroll
        for (int e = 0; e < 4; ++e) { c[2 * e] = dot2(v[i][e], SLO, c[2 * e]); c[2 * e + 1] = dot2(v[i][e], SHI, c[2 * e + 1]); }
      float d4[4], d2[2];
#pragma unroll
      for (int e = 0; e < 4; ++e) d4[e] = xchg32(c[e], c[4 + e]);
#pragma unroll
      for (int e = 0; e < 2; ++e) d2[e] = xchg16(d4[e], d4[2 + e]);
      d2[0] = dpp_add<0x128>(d2[0]); d2[1] = dpp_add<0x128>(d2[1]);
      return (lane & 8) ? d2[1] : d2[0];
    };
    float c1 = colsum(vw), c2; unsigned off2;
    if (w < 2) {
      c2 = colsum(vm);
      off2 = WS_CS + ((((8 + n8) * 4 + w) * 16 + mt) * 64 + 8 * c8 + rr) * 4;
    } else {
      float a = 0, b = 0;
#pragma unroll
      for (int i = 0; i < 10; ++i) { a = dot2(vm[i][0], ONE2, a); b = dot2(vm[i][1], ONE2, b); a = dot2(vm[i][2], ONE2, a); b = dot2(vm[i][3], ONE2, b); }
      c2 = sum8(a + b);
      off2 = (c8 == 0 && rr < 5) ? WS_CD + (n8 * 160 + 10 * mt + 5 * (w - 2) + rr) * 4 : 0xfffffff0u;
    }
    asm volatile("" : "+v"(c1), "+v"(c2), "+v"(rso));
    __builtin_amdgcn_raw_buffer_store_b32(__float_as_uint(c1), rw, WS_CS + (((n8 * 4 + w) * 16 + mt) * 64 + 8 * c8 + rr) * 4, 0, 16);
    __builtin_amdgcn_raw_buffer_store_b32(__float_as_uint(c2), rw, off2, 0, 16);
    __builtin_amdgcn_raw_buffer_store_b32(__float_as_uint(rso), rw, rho < 2 ? WS_RS + mt * 1024 + rho * 512 + (n8 * 16 + 4 * w + q) * 4 : 0xfffffff0u, 0, 16);
  }
  bf16x8 FX[2][4], FD[2][5];
  s16x4 FW[2][2 * NC][2];
  constexpr int NR = TG ? 20 : 17, NM = TG ? 32 : 36;
  auto rd1 = [&](int s, int buf, int ks, int r) {
    if (r >= 2 && r < 6) {
      const int i = r - 2;
      FX[s][i] = *(LAS bf16x8*)(smem + (ks ? vX1 : vX0) + buf * 16384 + (TG ? i * 2048 : (i >> 1) * 4096 + (i & 1) * 512));
    } else if (r < 4 + 4 * NC) {
      const int h = r < 2 ? r : r - 4, f = h >> 1;
      FW[s][f][h & 1] = __builtin_amdgcn_ds_read_tr16_b64_v4i16((LAS s16x4*)(smem + vW[f >> 1] + buf * 16384 + ks * 4096 + (f & 1) * 8192 + (h & 1) * 512));
    } else {
      const int jj = r - (4 + 4 * NC);
      FD[s][jj] = *(LAS bf16x8*)(smem + (ks ? vD1 : vD0) + buf * 20480 + jj * 2048);
    }
  };
  auto mm1 = [&](int s, int m, bool first) {
    const int f = m >> 2, i = (f & 1) ? 3 - (m & 3) : (m & 3);
    if (f < 2 * NC) {
      const bf16x8 wf = __builtin_shufflevector(FW[s][f][0], FW[s][f][1], 0, 1, 2, 3, 4, 5, 6, 7);
      mf((f & 1) ? aU[f >> 1][i] : aG[f >> 1][i], wf, FX[s][i], first);
    } else mf(aA[i][f - 2 * NC], FX[s][i], FD[s][f - 2 * NC], first);
  };
  auto seg = [&](int s, bool first, int m0, int m1, int rk, int buf, int ks) __attribute__((always_inline)) {
    int r = 0;
#pragma unroll
    for (int m = m0; m < m1; ++m) {
      mm1(s, m, first);
      if (rk == 2 && r == 0) r = 2;
      if (rk == 2 && r == 6) r = 4 + 4 * NC;
      const int rend = rk == 0 ? 0 : rk == 3 ? 4 + 4 * NC : NR;
      if (r < rend && (TG ? (m % 5 != 4) : (rk == 3 || m % 3 != 2))) { rd1(s ^ 1, buf, ks, r); ++r; }
      PIN();
    }
  };
  BAR();
  STAMP_AT(1);
#pragma unroll
  for (int r = 0; r < NR; ++r) rd1(0, 0, 0, r);
  PIN();
  constexpr int KJ = TG ? 10 : 7;
  sfor<0, 10>([&](auto K) __attribute__((always_inline)) {
    constexpr int k = decltype(K)::v;
    constexpr int m0 = k < KJ ? 0 : 8 * NC;
    seg(0, k == 0, m0, NM, k < KJ ? 1 : 2, k % 3, 1);
    if (k == 1) {
      wait_v<0>();
      if (lane == 0) st_flag(FLAG(C_F0 + (mt * 16 + ns) * 4 + w), tag);
    }
    if (k < 9) {
      WAIT_L0();
      BAR();
      PIN();
    }
    if (k < 9) seg(1, false, m0, NM, k + 1 < KJ ? 1 : 2, (k + 1) % 3, 0);
    else seg(1, false, m0, NM, TG ? 0 : 3, KJ % 3, 0);
    WAIT_L0(); PIN();
  });
  if (!TG) {
    MFMA_DONE();
    after_mfma(aA);
    asm volatile("" : "+v"(lane)); q = lane >> 4; rho = lane & 15;
    STAMP_AT(4);
    const float sgn = (n8 >= 4) ? -1.0f : 1.0f;
    float cs[5];
#pragma unroll
    for (int j = 0; j < 5; ++j) cs[j] = *(LAS float*)(smem + T_CD + (80 * wn + 16 * j + rho) * 4);
    const int kt = (n8 >= 4 ? 32 : 0) + mt * 2 + wm;
#pragma unroll
    for (int I = 0; I < 2; ++I) {
      f32x4 r4[2], m4[2];
#pragma unroll
      for (int t = 0; t < 2; ++t) {
        const int row = 64 * wm + 32 * I + 8 * q + 4 * t;
        r4[t] = *(LAS f32x4*)(smem + T_RS + row * 4) * sgn; m4[t] = *(LAS f32x4*)(smem + T_RM + row * 4);
      }
      const unsigned dst = WS_AB + ((((n8 & 3) * 64 + kt) * 2 + I) * 10 + 5 * wn) * 1024 + lane * 16;
#pragma unroll
      for (int j = 0; j < 5; ++j) {
        const f32x4 v0 = (aA[2 * I][j] - m4[0] * cs[j]) * r4[0], v1 = (aA[2 * I + 1][j] - m4[1] * cs[j]) * r4[1];
        const u32x4 o = {pk_bf16(v0[0], v0[1]), pk_bf16(v0[2], v0[3]), pk_bf16(v1[0], v1[1]), pk_bf16(v1[2], v1[3])};
        st16c(rw, dst + j * 1024, o);
      }
    }
    STAMP_AT(7);
    WAIT_L0(); PIN();
#pragma unroll
    for (int k = KJ; k < 10; ++k) {
      seg(0, false, 0, 8 * NC, 3, k % 3, 1);
      WAIT_L0(); PIN();
      if (k == KJ + AFL) {
        wait_v<0>();
        if (lane == 0) st_flag(FLAG(C_FAB + ((n8 & 3) * 64 + kt) * 2 + wn), tag);
        PIN();
      }
      seg(1, false, 0, 8 * NC, k < 9 ? 3 : 0, (k + 1) % 3, 0);
      WAIT_L0(); PIN();
    }
  }
  MFMA_DONE();
  after_mfma(aG); after_mfma(aU);
  asm volatile("" : "+v"(lane)); q = lane >> 4; rho = lane & 15;
  BAR();
  STAMP_AT(2);
  {
    const float K0 = -2.0f * 0.7978845608028654f * 1.4426950408889634f, K1 = K0 * 0.044715f;
    float mu[4], c0[4], c1[4], v2[4];
#pragma unroll
    for (int i = 0; i < 4; ++i) {
      const int row = TG ? 64 * wm + 16 * i + rho : 64 * wm + 32 * (i >> 1) + 8 * (rho >> 2) + 4 * (i & 1) + (rho & 3);
      const float rs = *(LAS float*)(smem + T_RS + row * 4);
      mu[i] = *(LAS float*)(smem + T_RM + row * 4);
      c0[i] = K0 * rs; c1[i] = K1 * rs * rs * rs; v2[i] = __builtin_amdgcn_rcpf(rs * rs);
    }
#pragma unroll
    for (int J = 0; J < NC / 2; ++J) {
      f32x4 cg[2], cu[2];
#pragma unroll
      for (int t = 0; t < 2; ++t) {
        const int c = 2 * J + t;
        const int o = TG ? (wn * 256 + c * 64 + q * 16) : ((2 * wn + c) * 64 + q * 16);
        cg[t] = *(LAS f32x4*)(smem + T_CS + o);
        cu[t] = *(LAS f32x4*)(smem + T_CS + (TG ? 512 : 256) + o);
      }
#pragma unroll
      for (int i = 0; i < 4; ++i) {
        u32x4 o;
#pragma unroll
        for (int t = 0; t < 2; ++t) {
          const f32x4 g = aG[2 * J + t][i] - cg[t] * mu[i];
          const f32x4 u = aU[2 * J + t][i] - cu[t] * mu[i];
          const f32x4 z = g * (g * g * c1[i] + c0[i]);
          f32x4 e;
#pragma unroll
          for (int r = 0; r < 4; ++r) e[r] = __builtin_amdgcn_exp2f(z[r]);
          e = e * v2[i] + v2[i];
#pragma unroll
          for (int r = 0; r < 4; ++r) e[r] = __builtin_amdgcn_rcpf(e[r]);
          const f32x4 hv = g * u * e;
          o[2 * t] = pk_bf16(hv[0], hv[1]); o[2 * t + 1] = pk_bf16(hv[2], hv[3]);
        }
        const int f0 = hcol0 + (TG ? wn * 64 + J * 32 : wn * 32);
        const int rb = TG ? 4 * wm + i : 4 * wm + 2 * (i >> 1) + (rho >> 3), pos = TG ? lane : 8 * ((rho >> 2) & 1) + 4 * (i & 1) + (rho & 3) + 16 * q;
        st16c(rw, WS_H + (((mt * 24 + (f0 >> 6)) * 8 + rb) * 2 + ((f0 >> 5) & 1)) * 1024 + pos * 16, o);
      }
    }
  }
  STAMP_AT(3);
}

DI void mfma_p2(const Params& P, LAS char* smem, int lane, int w, int split, int mt, int nt, int mt1, int ns, u64 tag) {
  asm volatile("" : "+v"(lane));
  const int wm = w & 1, wn = w >> 1, q = lane >> 4, r15 = lane & 15;
  unsigned vY0 = wm * 8192 + r15 * 128 + ((q ^ (r15 & 7)) * 16), vY1 = vY0 ^ 64;
  unsigned vB0 = L_B + wn * 5120 + lane * 16, vB2 = vB0 + 2 * B_ST;
  asm volatile("" : "+v"(vY0), "+v"(vY1), "+v"(vB0), "+v"(vB2));
  const __amdgpu_buffer_rsrc_t rw = __builtin_amdgcn_make_buffer_rsrc(P.ws, 0, WS_END, 0x00020000);
  const int tile = mt * 4 + nt;
  f32x4 acc[4][5];
  bf16x8 FA[2][4], FB[2][5];
  auto rdy = [&](int s, int k, int ks, int r) {
    const int buf = k & 3;
    if (r >= 1 && r < 5) FA[s][r - 1] = *(LAS bf16x8*)(smem + (ks ? vY1 : vY0) + buf * A_ST + (r - 1) * 2048);
    else { const int j = r ? r - 4 : 0; FB[s][j] = *(LAS bf16x8*)(smem + (buf >= 2 ? vB2 : vB0) + (buf & 1) * B_ST + ks * 10240 + j * 1024); }
  };
  auto seg = [&](auto K, auto HF) __attribute__((always_inline)) {
    constexpr int k = decltype(K)::v, hf = decltype(HF)::v, rk = k + hf, rh = hf ^ 1;
    int r = 0;
#pragma unroll
    for (int m = 0; m < 20; ++m) {
      const int i = (m & 4) ? 3 - (m & 3) : (m & 3);
      mf(acc[i][m >> 2], FB[hf][m >> 2], FA[hf][i], k == 0 && hf == 0);
      if (rk < NY && r < 9 && m % 3 != 2) { rdy(rh, rk, rh, r); ++r; }
      PIN();
    }
  };
  auto publish = [&](int ip) {
    u32x4 o[5];
#pragma unroll
    for (int t = 0; t < 2; ++t) {
      const int i = 2 * ip + t;
#pragma unroll
      for (int j = 0; j < 5; ++j) {
        const int n = j < 4 ? 2 * t + (j >> 1) : 4, e = j < 4 ? (j & 1) * 2 : 2 * t;
        o[n][e] = pk_bf16(acc[i][j][0], acc[i][j][1]); o[n][e + 1] = pk_bf16(acc[i][j][2], acc[i][j][3]);
      }
    }
    if (w == split) {
#pragma unroll
      for (int n = 0; n < 5; ++n) *(LAS u32x4*)(smem + F_Y + (n < 4 ? ip * 4096 + n * 1024 : 8192 + ip * 1024) + lane * 16) = o[n];
    } else {
      const unsigned dst = WS_P + ((tile * 4 + w) * 3 + (split ^ w) - 1) * 10240 + lane * 16;
#pragma unroll
      for (int n = 0; n < 5; ++n) if (!NOPUB) st16c(rw, dst + (n < 4 ? ip * 4096 + n * 1024 : 8192 + ip * 1024), o[n]);
    }
  };
  BAR();
  STAMP_AT(5);
#pragma unroll
  for (int r = 0; r < 9; ++r) rdy(0, 0, 0, r);
  PIN();
  sfor<0, NY>([&](auto K) __attribute__((always_inline)) {
    constexpr int k = decltype(K)::v;
    if (CPK == k) __builtin_amdgcn_s_setprio(3);
    seg(K, IC<0>{});
    if (k == HFL) {
      wait_v<0>();
      if (lane == 0) st_flag(FLAG(C_FH + mt1 * 64 + ns * 4 + w), tag);
    }
    if (k + 1 < NY) {
      WAIT_L0();
      BAR();
      PIN();
    }
    seg(K, IC<1>{});
    WAIT_L0(); PIN();
  });
  MFMA_DONE();
  after_mfma(acc);
  __builtin_amdgcn_s_setprio(3);
  publish(0); publish(1);
  if (HOLD) BAR();
  if (w != split) {
    wait_v<0>();
    if (lane == 0) st_flag(FLAG(C_FP + (tile * 4 + w) * 4 + (split ^ w) - 1), tag);
  }
  const int orow = mt * 128 + (split & 1) * 64 + w * 16, ocol = nt * 160 + (split >> 1) * 80;
  u32x4 xv[3];
  unsigned tr[3], oo[3];
#pragma unroll
  for (int i = 0; i < 3; ++i) {
    const int n = min(i * 64 + lane, 159);
    const unsigned off = ((orow + n / 10) * D + ocol + (n % 10) * 8) * 2;
    xv[i] = *(const u32x4*)((const char*)P.x + off);
    tr[i] = F_T + w * (16 * FPITCH) + (n / 10) * FPITCH + (n % 10) * 16;
    oo[i] = i * 64 + lane < 160 ? off : 0xfffffff0u;
  }
  if (w == split) {
    const u64* fp = FLAG(C_FP + (tile * 4 + split) * 4 + min(lane, 2));
    __builtin_amdgcn_s_sleep(PD0);
#pragma unroll 1
    for (int it = 0; it < 40000; ++it) {
      if (__all(ld_flag(fp) == tag)) break;
      __builtin_amdgcn_s_sleep(PD1);
    }
    WAIT_L0();
    if (lane == 0) *(volatile LAS unsigned*)(smem + T_GO) = 2;
  } else {
    __builtin_amdgcn_s_setprio(0);
#pragma unroll 1
    for (int it = 0; it < 4000000; ++it) {
      if (*(volatile LAS unsigned*)(smem + T_GO) == 2) break;
      __builtin_amdgcn_s_sleep(4);
    }
    __builtin_amdgcn_s_setprio(3);
  }
  asm volatile("" ::: "memory");
  f32x4 ys[5];
  {
    u32x4 pa[3][2]; u32x2 pb[3];
#pragma unroll
    for (int s = 0; s < 3; ++s) {
      const unsigned src = WS_P + ((tile * 4 + split) * 3 + s) * 10240 + lane * 16;
      pa[s][0] = ld16c(rw, src + w * 2048); pa[s][1] = ld16c(rw, src + w * 2048 + 1024);
      pb[s] = __builtin_amdgcn_raw_buffer_load_b64(rw, src + 8192 + (w >> 1) * 1024 + (w & 1) * 8, 0, 16);
    }
#pragma unroll
    for (int j = 0; j < 5; ++j) {
#pragma unroll
      for (int s = 0; s < 3; ++s) {
        const unsigned lo = j < 4 ? pa[s][j >> 1][(j & 1) * 2] : pb[s][0], hi = j < 4 ? pa[s][j >> 1][(j & 1) * 2 + 1] : pb[s][1];
        const f32x4 v = {bflo(lo), bfhi(lo), bflo(hi), bfhi(hi)};
        ys[j] = s ? ys[j] + v : v;
      }
      asm volatile("" : "+v"(ys[j]));
    }
  }
  {
    LAS char* po = smem + F_Y + lane * 16;
    const u32x4 oa0 = *(LAS u32x4*)(po + w * 2048), oa1 = *(LAS u32x4*)(po + w * 2048 + 1024);
    const u32x2 ob = *(LAS u32x2*)(po + 8192 + (w >> 1) * 1024 + (w & 1) * 8);
#pragma unroll
    for (int j = 0; j < 5; ++j) {
      const unsigned l0 = j < 2 ? oa0[(j & 1) * 2] : j < 4 ? oa1[(j & 1) * 2] : ob[0], h0 = j < 2 ? oa0[(j & 1) * 2 + 1] : j < 4 ? oa1[(j & 1) * 2 + 1] : ob[1];
      ys[j] += (f32x4){bflo(l0), bfhi(l0), bflo(h0), bfhi(h0)};
      asm volatile("" : "+v"(ys[j]));
    }
  }
  unsigned tw = F_T + w * (16 * FPITCH) + r15 * FPITCH + q * 8, rd0 = w * 5120 + lane * 16, rd2 = rd0 + L_B;
  asm volatile("" : "+v"(tw), "+v"(rd0), "+v"(rd2), "+v"(tr[0]), "+v"(tr[1]), "+v"(tr[2]), "+v"(oo[0]), "+v"(oo[1]), "+v"(oo[2]));
  STAMP_AT(6);
  BAR();
  f32x4 dd[4][5];
#pragma unroll
  for (int j = 0; j < 5; ++j)
#pragma unroll
    for (int u = 0; u < 4; ++u) dd[u][j] = *(LAS f32x4*)(smem + ((u & 2) ? rd2 : rd0) + (u & 1) * 20480 + j * 1024);
#pragma unroll
  for (int j = 0; j < 5; ++j) {
    const f32x4 d = (dd[0][j] + dd[1][j]) + (dd[2][j] + dd[3][j]);
    f32x4 e = d * -1.4426950408889634f;
#pragma unroll
    for (int r = 0; r < 4; ++r) e[r] = __builtin_amdgcn_exp2f(e[r]);
    e = e + 1.0f;
#pragma unroll
    for (int r = 0; r < 4; ++r) e[r] = __builtin_amdgcn_rcpf(e[r]);
    e = e * d * ys[j];
    *(LAS u32x2*)(smem + tw + j * 32) = (u32x2){pk_bf16(e[0], e[1]), pk_bf16(e[2], e[3])};
  }
  WAIT_L0();
  const __amdgpu_buffer_rsrc_t ro = __builtin_amdgcn_make_buffer_rsrc(P.out, 0, S * D * 2, 0x00020000);
#pragma unroll
  for (int i = 0; i < 3; ++i) {
    const u32x4 v = *(LAS u32x4*)(smem + tr[i]);
    u32x4 o;
#pragma unroll
    for (int d = 0; d < 4; ++d) o[d] = pk_bf16(bflo(v[d]) + bflo(xv[i][d]), bfhi(v[d]) + bfhi(xv[i][d]));
    __builtin_amdgcn_raw_buffer_store_b128(o, ro, oo[i], 0, 0);
  }
}

__global__ __launch_bounds__(512, 2)
void fused(Params P) {
  extern __shared__ __attribute__((aligned(16))) char smem_[];
  LAS char* smem = (LAS char*)smem_;
  const int tid = threadIdx.x, lane = tid & 63, w = __builtin_amdgcn_readfirstlane(tid >> 6);
  const int bid = blockIdx.x, xcd = bid & 7, idx = bid >> 3;
  u64 tag;
  STAMP_AT(0);
  const int mt1 = (xcd & 1) * 8 + (idx & 7), j = idx >> 3, ns = (j & 1) * 8 + (xcd >> 1) * 2 + (j >> 1);
  const int split = xcd >> 1, mt2 = (xcd & 1) * 8 + (idx >> 2), nt = idx & 3;
  if (w >= 4) {
    __builtin_amdgcn_s_setprio(HPRIO);
    helper(P, smem, lane, w & 3, mt1, ns, split, mt2, nt);
  } else {
    if (j & 1) mfma_p1<false>(P, smem, lane, w, mt1, ns, tag); else mfma_p1<true>(P, smem, lane, w, mt1, ns, tag);
    mfma_p2(P, smem, lane, w, split, mt2, nt, mt1, ns, tag);
    __builtin_amdgcn_s_waitcnt(0);
  }
}

extern "C" void kernel_launch(void* const* d_in, const int* in_sizes, int n_in, void* d_out, int out_size, void* d_ws, size_t ws_size, hipStream_t stream) {
  Params P;
  P.x = (const bf16_t*)d_in[0]; P.wsr = (const bf16_t*)d_in[1]; P.wsi = (const bf16_t*)d_in[2];
  P.wdr = (const bf16_t*)d_in[3]; P.wdi = (const bf16_t*)d_in[4];
  P.wg = (const bf16_t*)d_in[6]; P.wu = (const bf16_t*)d_in[7]; P.wdn = (const bf16_t*)d_in[8];
  P.out = (bf16_t*)d_out; P.ws = (char*)d_ws;
  (void)hipFuncSetAttribute((const void*)fused, hipFuncAttributeMaxDynamicSharedMemorySize, LDS_BYTES);
  fused<<<256, 512, LDS_BYTES, stream>>>(P);
}
```

```cpp
#include <hip/hip_runtime.h>
#include <hip/hip_bf16.h>
typedef unsigned short bf16_t;
typedef unsigned long long u64;
typedef short bf16x8 __attribute__((ext_vector_type(8)));
typedef short s16x4 __attribute__((ext_vector_type(4)));
typedef float f32x4 __attribute__((ext_vector_type(4)));
typedef float f32x2 __attribute__((ext_vector_type(2)));
typedef unsigned u32x4 __attribute__((ext_vector_type(4)));
typedef unsigned u32x2 __attribute__((ext_vector_type(2)));
typedef __bf16 bf16x2_t __attribute__((ext_vector_type(2)));
#define LAS __attribute__((address_space(3)))
#define GAS __attribute__((address_space(1)))
#define DI __device__ __forceinline__

#define STAMP 1
#define NPRE 2
#define ROT2 0
#define YFL 4
#define FABSL 1
#define AFL 2
#define HFL 2
#define PSPL 1
#define XG 2
#define XY 1
#define XD 1
#define YSB 2
#define WK 2
#define FABM 0
#define SK 5
#define HOLD 0
#define PD0 60
#define PD1 8
#define DPF 0
#define DFK 8
#define DP0 4
#define DPS 2
#define NOPUB 0
#define DPRIO 2
#define CPK 99
#define S0E 2
#define TPRIO 3
#define B0E 0
#define PFG 1
#define PFM 1
#define PH0 0
#define PHS 4
#define PFS 10
#define YPL 8
#define XROT 0
#define HPRIO 0
#define PFN 4

constexpr int S = 2048, D = 640, F = 1536;
constexpr unsigned WS_AB = 0, WS_H = WS_AB + 2 * S * D * 2, WS_P = WS_H + S * F * 2, WS_WD = WS_P + 3 * S * D * 2, WS_RS = WS_WD + F * D * 2,
                   WS_CS = WS_RS + S * 8, WS_CD = WS_CS + 16 * 4 * 16 * 64 * 4, WS_CTRL = WS_CD + 8192, WS_END = WS_CTRL + 5120 * 128;
constexpr int C_E = 0, C_F0 = 1, C_FAB = C_F0 + 1024, C_FH = C_FAB + 512, C_FW = C_FH + 1024, C_FP = C_FW + 512;
#define FLAG(i) ((u64*)(P.ws + WS_CTRL) + (i) * 16)

DI unsigned pk_bf16(float lo, float hi) { f32x2 v = {lo, hi}; return __builtin_bit_cast(unsigned, __builtin_convertvector(v, bf16x2_t)); }
DI float rbf(float f) { return __uint_as_float(pk_bf16(f, f) << 16); }
DI float bflo(unsigned u) { return __uint_as_float(u << 16); }
DI float bfhi(unsigned u) { return __uint_as_float(u & 0xffff0000u); }
DI float dot2(unsigned a, unsigned b, float c) { return __builtin_amdgcn_fdot2_f32_bf16(__builtin_bit_cast(bf16x2_t, a), __builtin_bit_cast(bf16x2_t, b), c, false); }
#define GLDS4(POL) asm volatile("s_mov_b32 m0, %0\n\ts_nop 0\n\tglobal_load_lds_dwordx4 %2, %1" POL "\n\tglobal_load_lds_dwordx4 %3, %1 offset:1024" POL \
    "\n\tglobal_load_lds_dwordx4 %4, %1 offset:2048" POL "\n\tglobal_load_lds_dwordx4 %5, %1 offset:3072" POL \
    :: "s"(lds), "s"(base), "v"(v0), "v"(v1), "v"(v2), "v"(v3) : "memory")
#define GLDS5(POL) asm volatile("s_mov_b32 m0, %0\n\ts_nop 0\n\tglobal_load_lds_dwordx4 %2, %1" POL "\n\tglobal_load_lds_dwordx4 %3, %1 offset:1024" POL \
    "\n\tglobal_load_lds_dwordx4 %4, %1 offset:2048" POL "\n\tglobal_load_lds_dwordx4 %5, %1 offset:3072" POL \
    "\n\ts_add_u32 m0, %0, 0x1000\n\ts_nop 0\n\tglobal_load_lds_dwordx4 %6, %1" POL \
    :: "s"(lds), "s"(base), "v"(v0), "v"(v1), "v"(v2), "v"(v3), "v"(v4) : "memory", "scc")
template <bool DEV> DI void glds4(unsigned lds, const char* base, unsigned v0, unsigned v1, unsigned v2, unsigned v3) {
  if (DEV) GLDS4(" sc1"); else GLDS4("");
}
template <bool DEV> DI void glds5(unsigned lds, const char* base, unsigned v0, unsigned v1, unsigned v2, unsigned v3, unsigned v4) {
  if (DEV) GLDS5(" sc1"); else GLDS5("");
}
template <bool DEV = false> DI void touch_lines(unsigned lds, unsigned voff, const char* base) {
  if (DEV) asm volatile("s_mov_b32 m0, %0\n\ts_nop 0\n\tglobal_load_lds_dword %1, %2 sc1" :: "s"(lds), "v"(voff), "s"(base) : "memory");
  else asm volatile("s_mov_b32 m0, %0\n\ts_nop 0\n\tglobal_load_lds_dword %1, %2" :: "s"(lds), "v"(voff), "s"(base) : "memory");
}
template <int N> DI void wait_v() { asm volatile("s_waitcnt vmcnt(%0)" :: "i"(N) : "memory"); }
#define WAIT_L0() do { __builtin_amdgcn_s_waitcnt(0xC07F); asm volatile("" ::: "memory"); } while (0)
#define BAR() do { asm volatile("" ::: "memory"); __builtin_amdgcn_s_barrier(); asm volatile("" ::: "memory"); } while (0)
#define SGB(m, n) __builtin_amdgcn_sched_group_barrier(m, n, 0)
#define PIN() __builtin_amdgcn_sched_barrier(0)
template <int V> struct IC { static constexpr int v = V; };
template <int I, int N, class Fn> DI void sfor(Fn&& f) { if constexpr (I < N) { f(IC<I>{}); sfor<I + 1, N>(f); } }
DI void mf(f32x4& c, const bf16x8& a, const bf16x8& b, bool first) {
  if (first) asm volatile("v_mfma_f32_16x16x32_bf16 %0, %1, %2, 0" : "=&v"(c) : "v"(a), "v"(b));
  else asm volatile("v_mfma_f32_16x16x32_bf16 %0, %1, %2, %0" : "+v"(c) : "v"(a), "v"(b));
}
#define MFMA_DONE() asm volatile("s_nop 15")
template <int A, int B> DI void after_mfma(f32x4 (&c)[A][B]) {
#pragma unroll
  for (int i = 0; i < A; ++i)
#pragma unroll
    for (int j = 0; j < B; ++j) asm volatile("" : "+v"(c[i][j]));
}
#define STAMP_AT(i) do { if ((threadIdx.x & 63) == 0 && (threadIdx.x >> 6) == ((i) >= 8 ? 4 : 0)) { P.stamps[blockIdx.x * 32 + 2 * (i)] = __builtin_amdgcn_s_memrealtime(); P.stamps[blockIdx.x * 32 + 2 * (i) + 1] = __builtin_amdgcn_s_memtime(); } } while (0)

template <int CTRL> DI float dpp_add(float v) { return v + __builtin_bit_cast(float, __builtin_amdgcn_update_dpp(0, __builtin_bit_cast(int, v), CTRL, 0xf, 0xf, true)); }
DI float sum8(float v) { v = dpp_add<0xB1>(v); v = dpp_add<0x4E>(v); return dpp_add<0x141>(v); }
DI float sum16(float v) { return dpp_add<0x140>(sum8(v)); }
DI float xchg16(float keep_lo, float keep_hi) {
  const auto r = __builtin_amdgcn_permlane16_swap(__float_as_uint(keep_lo), __float_as_uint(keep_hi), false, false);
  return __uint_as_float(r[0]) + __uint_as_float(r[1]);
}
DI float xchg32(float keep_lo, float keep_hi) {
  const auto r = __builtin_amdgcn_permlane32_swap(__float_as_uint(keep_lo), __float_as_uint(keep_hi), false, false);
  return __uint_as_float(r[0]) + __uint_as_float(r[1]);
}
#define TIC(v)
#define ACC(a, x, y)
#define PUT(slot, a)
DI u64 ld_flag(const u64* p) { return __hip_atomic_load((const GAS u64*)p, __ATOMIC_RELAXED, __HIP_MEMORY_SCOPE_AGENT); }
DI void st_flag(u64* p, u64 v) { __hip_atomic_store((GAS u64*)p, v, __ATOMIC_RELAXED, __HIP_MEMORY_SCOPE_AGENT); }
DI void wait_flags(const u64* p, u64 tag) {
#pragma unroll 1
  for (int it = 0; it < 60000; ++it) {
    if (__all(ld_flag(p) == tag)) break;
    __builtin_amdgcn_s_sleep(4);
  }
  asm volatile("" ::: "memory");
}
DI void wait_flags2(const u64* p, const u64* p2, u64 tag) {
#pragma unroll 1
  for (int it = 0; it < 60000; ++it) {
    const u64 a = ld_flag(p), b = ld_flag(p2);
    if (__all(a == tag && b == tag)) break;
    __builtin_amdgcn_s_sleep(4);
  }
  asm volatile("" ::: "memory");
}
DI u64 read_tag(const char* ws) {
  u64 e;
  asm volatile("s_load_dwordx2 %0, %1, 0x0\n\ts_waitcnt lgkmcnt(0)" : "=s"(e) : "s"(ws + WS_CTRL) : "memory");
  return e * 6364136223846793005ull + 1442695040888963407ull;
}
DI void st16c(__amdgpu_buffer_rsrc_t r, unsigned off, u32x4 v) { __builtin_amdgcn_raw_buffer_store_b128(v, r, off, 0, 16); }
DI u32x4 ld16c(__amdgpu_buffer_rsrc_t r, unsigned off) { return __builtin_amdgcn_raw_buffer_load_b128(r, off, 0, 16); }

struct Params {
  const bf16_t *x, *wsr, *wsi, *wdr, *wdi, *wg, *wu, *wdn;
  bf16_t* out; char* ws;
  u64* stamps;
};

constexpr int L_W = 49152, L_D = 98304, K1_TAB = 159744, LDS_BYTES = K1_TAB + 4096;
constexpr int T_RS = K1_TAB, T_RM = K1_TAB + 512, T_CS = K1_TAB + 1024, T_CD = K1_TAB + 2048, T_GO = K1_TAB + 3584, T_PF = K1_TAB + 3072;
constexpr int A_ST = 16384, B_ST = 20480, L_B = 4 * A_ST;

constexpr int NY = 16;
constexpr int F_Y = 147456, F_T = 106496, FPITCH = 176;

DI int f_red(int lw) { return (lw >> 1) * L_B + (lw & 1) * 20480; }

DI void helper(const Params& P, LAS char* smem, int lane, int lw, int mt, int ns, int split, int mt2, int nt) {
  constexpr int NL = 12;
  const bool TG = ns < 8;
  const int n8 = ns & 7, hcol0 = TG ? 128 * n8 : 1024 + 64 * n8;
  const int rr = lane >> 3, c8 = lane & 7;
  const unsigned lds0 = (unsigned)(size_t)smem;
  unsigned vX[4], vW[4], vD[5];
#pragma unroll
  for (int j = 0; j < 4; ++j) {
    vX[j] = (32 * lw + 8 * j + rr) * 1280 + ((c8 ^ (TG ? rr : ((rr & 2) | ((j & 2) << 1)))) * 16) - j * 1024;
    vW[j] = (8 * j + rr) * 3072 + ((c8 ^ ((rr & 2) | ((j & 1) << 2))) * 16) - j * 1024;
  }
#pragma unroll
  for (int j = 0; j < 5; ++j) vD[j] = (40 * lw + 8 * j + rr) * 1280 + ((c8 ^ rr) * 16) - (j & 3) * 1024;
  const char* bx = (const char*)P.x + (size_t)mt * 128 * 1280;
  const char* bw = (const char*)((lw >> 1) ? P.wu : P.wg) + hcol0 * 2 + (TG ? (lw & 1) * 128 : (lw & 1) * (32 * 3072));
  const char* bd = (const char*)(n8 >= 4 ? P.wdi : P.wdr) + (size_t)(n8 & 3) * 160 * 1280;
  const unsigned ldsX = __builtin_amdgcn_readfirstlane(lds0 + lw * 4096);
  const unsigned ldsW = __builtin_amdgcn_readfirstlane(lds0 + L_W + (TG ? (lw & 1) * 49152 + (lw >> 1) * 8192 : lw * 4096));
  const unsigned ldsD = __builtin_amdgcn_readfirstlane(lds0 + L_D + lw * 5120);
  auto issue_a = [&](int kt) {
    const int buf = kt % 3;
    glds4<false>(ldsX + buf * 16384, bx + kt * 128, vX[0], vX[1], vX[2], vX[3]);
    glds4<false>(ldsW + buf * 16384, bw + kt * (64 * 3072), vW[0], vW[1], vW[2], vW[3]);
  };
  auto issue_b = [&](int kt) {
    const int buf = kt % 3;
    if (TG) glds4<false>(ldsW + buf * 16384 + 4096, bw + kt * (64 * 3072) + 32 * 3072, vW[0], vW[1], vW[2], vW[3]);
    else glds5<false>(ldsD + buf * 20480, bd + kt * 128, vD[0], vD[1], vD[2], vD[3], vD[4]);
  };
  auto issue = [&](int kt) { issue_a(kt); issue_b(kt); };
  issue(0);
  issue_a(1);
  u64 tag;
  if (!S0E) issue_b(1);
  const char* pfb = (const char*)(split < 2 ? P.wsr : P.wsi) + (size_t)(mt2 * 128 + nt * 32) * 4096 + (split & 1) * 2048;
  const unsigned pfv = (lane & 31) * 4096 + (lw * 2 + (lane >> 5)) * 128;
  const u64* fa = FLAG(C_F0 + (lw == 0 ? mt * 64 + (lane & 31) : (((lane >> 1) & 15) * 16 + n8) * 4 + 2 + (lane & 1)));
  u64 fav = 0;
  const int xg = ((mt2 ^ nt) & 1) * XG;
  const unsigned vL = lane * 16, vL4 = vL + 4096;
  const char* baseWD = P.ws + WS_WD + ((nt * 48 + lw) * 10 + (split >> 1) * 5) * 1024;
  const int wtask = (mt * 8 + n8) * 4 + lw;
  const bool wdo = TG && wtask < 480;
  unsigned wv[16];
#pragma unroll
  for (int e = 0; e < 16; ++e) asm volatile("" : "=v"(wv[e]));
  sfor<0, 8>([&](auto K) __attribute__((always_inline)) {
    constexpr int k = decltype(K)::v;
    if (S0E && k == 0) wait_v<8>(); else if (S0E == 2 && k == 1) wait_v<8>(); else if (k == WK + 1 && wdo) wait_v<12 + 16>(); else if (k == WK + 3 && wdo) wait_v<12 + 4>(); else if (TG) wait_v<12>(); else wait_v<13>();
    BAR();
    if (S0E && k == 0) issue_b(1);
    if (S0E == 2 && k == 1) issue_b(2);
    if (lw < (TG ? 1 : 2)) {
      if (k == SK) asm volatile("global_load_dwordx2 %0, %1, off sc1" : "+v"(fav) : "v"(fa) : "memory");
      if (k == SK + 1) {
        asm volatile("" : "+v"(fav));
        if (!__all(fav == tag)) wait_flags(fa, tag);
        const char* src = P.ws + (lw == 0 ? WS_RS + mt * 1024 : WS_CD + n8 * 640);
        asm volatile("s_mov_b32 m0, %0\n\ts_nop 0\n\tglobal_load_lds_dwordx4 %1, %2 sc1" :: "s"(lds0 + (lw == 0 ? T_RS : T_CD)), "v"(vL), "s"(src) : "memory");
      }
    }
    if (S0E == 2 && k == 0) issue_a(2); else issue(k + 2);
    if (k == WK && wdo) {
#pragma unroll
      for (int u = 0; u < 4; ++u) {
        const int task = wtask * 2 + (u >> 1);
        const unsigned od = (((task % 240) / 5) * 32 + (lane >> 4) * 4) * 1280 + ((task / 240) * 160 + (task % 5) * 32) * 2 + (lane & 15) * 4;
        const char* pd = (const char*)P.wdn + (u & 1) * (16 * 1280);
        asm volatile("global_load_dword %0, %4, %5\n\tglobal_load_dword %1, %4, %5 offset:1280\n\tglobal_load_dword %2, %4, %5 offset:2560\n\tglobal_load_dword %3, %4, %5 offset:3840"
                     : "+v"(wv[4 * u]), "+v"(wv[4 * u + 1]), "+v"(wv[4 * u + 2]), "+v"(wv[4 * u + 3]) : "v"(od), "s"(pd) : "memory");
      }
    }
    if (k == WK + 2 && wdo) {
      const __amdgpu_buffer_rsrc_t rw = __builtin_amdgcn_make_buffer_rsrc(P.ws, 0, WS_END, 0x00020000);
      const bool up = lane & 8;
#pragma unroll
      for (int u = 0; u < 2; ++u) {
        u32x4 wo[2];
#pragma unroll
        for (int e = 0; e < 4; ++e) {
          asm volatile("" : "+v"(wv[8 * u + 2 * e]), "+v"(wv[8 * u + 2 * e + 1]));
          const unsigned lo = __builtin_amdgcn_perm(wv[8 * u + 2 * e + 1], wv[8 * u + 2 * e], 0x05040100u), hi = __builtin_amdgcn_perm(wv[8 * u + 2 * e + 1], wv[8 * u + 2 * e], 0x07060302u);
          const unsigned t = __builtin_amdgcn_update_dpp(0, up ? lo : hi, 0x128, 0xf, 0xf, true);
          wo[0][e] = up ? t : lo;
          wo[1][e] = up ? hi : t;
        }
        const int task = wtask * 2 + u;
        const unsigned dst = WS_WD + (((task / 240) * 48 + (task % 240) / 5) * 10 + (task % 5) * 2) * 1024 + (2 * (lane & 7) + ((lane >> 3) & 1) + (lane & 48)) * 16;
        st16c(rw, dst, wo[0]); st16c(rw, dst + 1024, wo[1]);
      }
    }
    if (k == 0) tag = read_tag(P.ws);
    if ((TG || PFM == 0) && k == PH0 + PHS * (lw >> 1) + (mt & 1)) touch_lines(lds0 + T_PF, pfv, pfb);
    if (k == 7) touch_lines(lds0 + T_PF, pfv, pfb + ((TG || PFM == 0) ? 1024 : 0));
  });
  wait_v<NL>();
  BAR();
  wait_v<1>();
  BAR();
  if (wdo && lane == 0) st_flag(FLAG(C_FW + wtask), tag);
  if (TG || lw < 2) {
    wait_flags(FLAG(C_F0 + ((lane & 15) * 16 + n8) * 4 + lw), tag);
    const __amdgpu_buffer_rsrc_t rw = __builtin_amdgcn_make_buffer_rsrc(P.ws, 0, WS_END, 0x00020000);
    f32x4 c = {0, 0, 0, 0};
#pragma unroll
    for (int i = 0; i < 4; ++i) c += __builtin_bit_cast(f32x4, ld16c(rw, WS_CS + (ns * 4 + lw) * 4096 + i * 1024 + lane * 16));
#pragma unroll
    for (int e = 0; e < 4; ++e) { c[e] = xchg16(c[e], c[e]); c[e] = xchg32(c[e], c[e]); }
    if (lane < 16) *(LAS f32x4*)(smem + T_CS + lw * 256 + lane * 16) = c;
  }
  if (lw == 0 && lane == 0) *(volatile LAS unsigned*)(smem + T_GO) = 0;
  WAIT_L0();
  if (!TG && PFM == 1) { if (lw < 2) __builtin_amdgcn_s_sleep(PFS); else __builtin_amdgcn_s_sleep(PFS + 24); touch_lines(lds0 + T_PF, pfv, pfb + 1024); }
  if (blockIdx.x == 64 && lw == 3) {
    wait_flags2(FLAG(C_F0 + lane * 4), FLAG(C_F0 + (64 + lane) * 4), tag);
    wait_flags2(FLAG(C_F0 + (128 + lane) * 4), FLAG(C_F0 + (192 + lane) * 4), tag);
    if (lane == 0) st_flag(FLAG(C_E), tag);
  }
  const u64* fab = FLAG(C_FAB + (nt * 64 + split * 16) * 2 + (lane & 31));
  unsigned vY[4];
#pragma unroll
  for (int j = 0; j < 4; ++j) vY[j] = (32 * lw + 8 * j + rr) * 4096 + ((c8 ^ rr) * 16) - j * 1024;
  const char* baseY = (const char*)(split < 2 ? P.wsr : P.wsi) + (size_t)mt2 * 128 * 4096 + (split & 1) * 2048;
  const char* baseAB = P.ws + WS_AB + (nt * 64 + split * 16) * B_ST + lw * 5120;
  const char* baseH = P.ws + WS_H + ((mt2 * 24 + (lw >> 1)) * 16 + (split & 1) * 8 + (lw & 1)) * 1024;
  const unsigned ldsA = ldsX, ldsB = __builtin_amdgcn_readfirstlane(lds0 + L_B + lw * 5120);
  auto issueYA = [&](int k, int yi) { glds4<false>(ldsA + (k & 3) * A_ST, baseY + (yi ^ xg) * 128, vY[0], vY[1], vY[2], vY[3]); };
  auto issueYB = [&](int k, int yi) { glds5<true>(ldsB + (k & 3) * B_ST, baseAB + (yi ^ xg) * B_ST, vL, vL, vL, vL, vL4); };
  BAR();
  __builtin_amdgcn_s_setprio(TPRIO);
  STAMP_AT(8);
  u64 fabv;
  if (FABM) asm volatile("global_load_dwordx2 %0, %1, off sc1" : "=&v"(fabv) : "v"(fab) : "memory"); else fabv = ~tag;
  issueYA(0, 0); issueYA(1, 1);
  if (FABM) wait_v<8>();
  asm volatile("" : "+v"(fabv));
  const bool ab_seen = __all(fabv == tag);
  if (lw == 0) {
    if (!ab_seen) wait_flags(fab, tag);
    if (lane == 0) *(volatile LAS unsigned*)(smem + T_GO) = 1;
  } else if (!ab_seen) {
#pragma unroll 1
    for (int it = 0; it < 4000000; ++it) {
      if (*(volatile LAS unsigned*)(smem + T_GO) != 0) break;
      __builtin_amdgcn_s_sleep(1);
    }
  }
  asm volatile("" ::: "memory");
  STAMP_AT(9);
  const u64* fh = FLAG(C_FH + mt2 * 64 + (lane & 31) * 2 + (split & 1));
  const int l36 = min(lane, 35);
  const int wt = (nt * 240 + ((l36 / 3) * 4 + lw) * 5 + (split >> 1) * 2 + l36 % 3) >> 1;
  const u64* fw = FLAG(C_FW + wt);
  u64 fhv = 0, fwv = 0;
  issueYB(0, 0);
  if (B0E) { wait_v<0>(); BAR(); issueYB(1, 1); } else { issueYB(1, 1); wait_v<5>(); BAR(); }
  issueYA(2, 2); issueYB(2, 2);
  wait_v<9>(); BAR();
  __builtin_amdgcn_s_setprio(0);
  issueYA(3, 3); issueYB(3, 3); issueYA(4, 4); issueYB(4, 4);
  const int ph = nt * 32 + lane, pw = (mt2 & 7) * 20 + min(max(lane - 32, 0), 19);
  const unsigned dstr = lane < 32 ? 2 * A_ST : 2 * B_ST;
  unsigned dpf = (lane < 32 ? WS_H + ((mt2 * 24 + (ph >> 6)) * 16 + (split & 1) * 8) * 1024 + (ph & 63) * 128
                            : WS_WD + ((nt * 48 + pw / 40) * 10 + (split >> 1) * 5) * 1024 + (pw % 40) * 128) + (DP0 + lw) * dstr;
#pragma unroll 1
  for (int k = 2; k < NY - 3; ++k) {
    if (k == DFK + 1) {
      wait_v<9>();
      asm volatile("" : "+v"(fhv), "+v"(fwv));
      if (!__all(fhv == tag && fwv == tag)) wait_flags2(fh, fw, tag);
    } else wait_v<18>();
    BAR();
    if (k == DFK) asm volatile("global_load_dwordx2 %0, %2, off sc1\n\tglobal_load_dwordx2 %1, %3, off sc1" : "+v"(fhv), "+v"(fwv) : "v"(fh), "v"(fw) : "memory");
    issueYA(k + 3, k + 3); issueYB(k + 3, k + 3);
    if (DPF) {
      const int n0 = (k - (DFK + 1)) * DPS;
      const int n = n0 + ((lw - n0) & 3);
      if (n0 >= 0 && n < n0 + DPS && DP0 + n < 12) { touch_lines<true>(lds0 + T_PF, dpf, P.ws); dpf += 4 * dstr; }
    }
  }
  bf16x8 GA[4][4], GB[4][5];
  f32x4 dac[4][5];
  auto loadD = [&](int i) {
    const int s = i & 3;
    const char* pa = baseH + (i ^ xg) * (2 * A_ST);
    const char* pb = baseWD + (i ^ xg) * (2 * B_ST);
    asm volatile("global_load_dwordx4 %0, %9, %12 sc1\n\tglobal_load_dwordx4 %4, %9, %11 sc1\n\tglobal_load_dwordx4 %5, %9, %11 offset:2048 sc1\n\t"
                 "global_load_dwordx4 %6, %10, %11 sc1\n\tglobal_load_dwordx4 %7, %10, %11 offset:2048 sc1\n\t"
                 "global_load_dwordx4 %1, %9, %12 offset:1024 sc1\n\tglobal_load_dwordx4 %2, %9, %12 offset:2048 sc1\n\t"
                 "global_load_dwordx4 %3, %9, %12 offset:3072 sc1\n\tglobal_load_dwordx4 %8, %10, %12 sc1"
                 : "=&v"(GB[s][0]), "=&v"(GB[s][1]), "=&v"(GB[s][2]), "=&v"(GB[s][3]), "=&v"(GA[s][0]), "=&v"(GA[s][1]), "=&v"(GA[s][2]), "=&v"(GA[s][3]), "=&v"(GB[s][4])
                 : "v"(vL), "v"(vL4), "s"(pa), "s"(pb) : "memory");
  };
  auto load1 = [&](int i, int n) {
    const int s = i & 3;
    const char* pa = baseH + (i ^ xg) * (2 * A_ST);
    const char* pb = baseWD + (i ^ xg) * (2 * B_ST);
    if (n < 4) asm volatile("global_load_dwordx4 %0, %1, %2 offset:%3 sc1" : "=&v"(GA[s][n]) : "v"(n < 2 ? vL : vL4), "s"(pa), "i"((n & 1) * 2048) : "memory");
    else asm volatile("global_load_dwordx4 %0, %1, %2 offset:%3 sc1" : "=&v"(GB[s][n - 4]) : "v"(n < 8 ? vL : vL4), "s"(pb), "i"(((n - 4) & 3) * 1024) : "memory");
  };
  auto group = [&](auto I) __attribute__((always_inline)) {
    constexpr int i = decltype(I)::v, s = i & 3;
    wait_v<(i == 0 ? 27 : i < 10 ? 18 : (11 - i) * 9)>();
    asm volatile("" : "+v"(GA[s][0]), "+v"(GA[s][1]), "+v"(GA[s][2]), "+v"(GA[s][3]), "+v"(GB[s][0]), "+v"(GB[s][1]), "+v"(GB[s][2]), "+v"(GB[s][3]), "+v"(GB[s][4]));
#pragma unroll
    for (int m = 0; m < 20; ++m) {
      const int r = (m & 4) ? 3 - (m & 3) : (m & 3);
      mf(dac[r][m >> 2], GB[s][m >> 2], GA[s][r], i == 0);
      if (i >= 1 && i + 3 < 12 && (m & 1) == 1 && m < 18) load1(i + 3, m >> 1);
    }
  };
  loadD(0);
  wait_v<27>(); BAR();
  loadD(1);
  wait_v<27>(); BAR();
  loadD(2);
  wait_v<27>(); BAR();
  loadD(3);
  __builtin_amdgcn_s_setprio(DPRIO);
  sfor<0, 12>(group);
  MFMA_DONE();
  after_mfma(dac);
#pragma unroll
  for (int r = 0; r < 4; ++r)
#pragma unroll
    for (int j = 0; j < 5; ++j) *(LAS f32x4*)(smem + f_red(lw) + (r * 5 + j) * 1024 + lane * 16) = dac[r][j];
  WAIT_L0();
  STAMP_AT(11);
  BAR();
  STAMP_AT(10);
}

template <bool TG>
DI void mfma_p1(const Params& P, LAS char* smem, int lane, int w, int mt, int ns, u64& tag) {
  const int n8 = ns & 7, hcol0 = TG ? 128 * n8 : 1024 + 64 * n8;
  const __amdgpu_buffer_rsrc_t rw = __builtin_amdgcn_make_buffer_rsrc(P.ws, 0, WS_END, 0x00020000);
  int q = lane >> 4, rho = lane & 15;
  const int rr = lane >> 3, c8 = lane & 7;
  u32x4 vx[5], vw[5], vm[10];
  if (TG) {
    const char* px = (const char*)P.x + (size_t)(mt * 128 + n8 * 16 + 4 * w + q) * 1280 + rho * 16;
#pragma unroll
    for (int i = 0; i < 5; ++i) vx[i] = *(const u32x4*)(px + i * 256);
    const char* pw = (const char*)((w >> 1) ? P.wu : P.wg) + (size_t)(40 * mt + rr) * 3072 + (hcol0 + (w & 1) * 64) * 2 + c8 * 16;
#pragma unroll
    for (int i = 0; i < 5; ++i) vw[i] = *(const u32x4*)(pw + i * (8 * 3072));
    if (w < 2) {
      const char* pm = (const char*)(w ? P.wu : P.wg) + (size_t)(40 * mt + rr) * 3072 + (1024 + 64 * n8) * 2 + c8 * 16;
#pragma unroll
      for (int i = 0; i < 5; ++i) vm[i] = *(const u32x4*)(pm + i * (8 * 3072));
    } else {
      const int e = (n8 & 3) * 160 + 10 * mt + 5 * (w - 2) + min(rr, 4);
      const char* pd = (const char*)(n8 >= 4 ? P.wdi : P.wdr) + (size_t)e * 1280 + c8 * 16;
#pragma unroll
      for (int i = 0; i < 10; ++i) vm[i] = *(const u32x4*)(pd + i * 128);
    }
  }
  tag = read_tag(P.ws);
  const int wm = w & 1, wn = w >> 1;
  constexpr int NC = TG ? 4 : 2;
  unsigned vX0 = TG ? wm * 8192 + rho * 128 + ((q ^ (rho & 7)) * 16)
                          : wm * 8192 + (8 * (rho >> 2) + (rho & 3)) * 128 + ((q ^ ((rho & 2) | ((rho >> 1) & 4))) * 16);
  unsigned vX1 = vX0 ^ 64;
  unsigned vW[NC];
  {
    const int qq = rho >> 2, p = lane & 3, swz = (qq & 2) | ((q & 1) << 2);
#pragma unroll
    for (int c = 0; c < NC; ++c) {
      const int cc = TG ? c : (2 * wn + c);
      vW[c] = L_W + (TG ? wn * 49152 : 0) + (8 * q + qq) * 128 + ((((2 * cc) ^ swz) + (p >> 1)) * 16) + 8 * (p & 1);
    }
  }
  unsigned vD0 = L_D + (80 * wn + rho) * 128 + ((q ^ (rho & 7)) * 16), vD1 = vD0 ^ 64;
  asm volatile("" : "+v"(vX0), "+v"(vX1));
  if (!TG) asm volatile("" : "+v"(vD0), "+v"(vD1));
#pragma unroll
  for (int c = 0; c < NC; ++c) asm volatile("" : "+v"(vW[c]));
  f32x4 aG[NC][4], aU[NC][4], aA[4][5];
  if (TG) {
    unsigned ONE2 = 0x3f803f80u, SLO = 0x00003f80u, SHI = 0x3f800000u;
    asm volatile("" : "+v"(ONE2), "+v"(SLO), "+v"(SHI));
    float sx[2] = {0, 0}, sq[2] = {0, 0};
#pragma unroll
    for (int i = 0; i < 5; ++i)
#pragma unroll
      for (int e = 0; e < 4; ++e) { sx[e & 1] = dot2(vx[i][e], ONE2, sx[e & 1]); sq[e & 1] = dot2(vx[i][e], vx[i][e], sq[e & 1]); }
    const float mean = sum16(sx[0] + sx[1]) * (1.0f / D);
    const float var = fmaxf(sum16(sq[0] + sq[1]) * (1.0f / D) - mean * mean, 0.f);
    const float sd = rbf(__builtin_amdgcn_sqrtf(rbf(rbf(var) + 1e-5f)));
    float rso = (lane & 1) ? rbf(mean) : __builtin_amdgcn_rcpf(sd);
    auto colsum = [&](const u32x4* v) -> float {
      float c[8] = {0, 0, 0, 0, 0, 0, 0, 0};
#pragma unroll
      for (int i = 0; i < 5; ++i)
#pragma unroll
        for (int e = 0; e < 4; ++e) { c[2 * e] = dot2(v[i][e], SLO, c[2 * e]); c[2 * e + 1] = dot2(v[i][e], SHI, c[2 * e + 1]); }
      float d4[4], d2[2];
#pragma unroll
      for (int e = 0; e < 4; ++e) d4[e] = xchg32(c[e], c[4 + e]);
#pragma unroll
      for (int e = 0; e < 2; ++e) d2[e] = xchg16(d4[e], d4[2 + e]);
      d2[0] = dpp_add<0x128>(d2[0]); d2[1] = dpp_add<0x128>(d2[1]);
      return (lane & 8) ? d2[1] : d2[0];
    };
    float c1 = colsum(vw), c2; unsigned off2;
    if (w < 2) {
      c2 = colsum(vm);
      off2 = WS_CS + ((((8 + n8) * 4 + w) * 16 + mt) * 64 + 8 * c8 + rr) * 4;
    } else {
      float a = 0, b = 0;
#pragma unroll
      for (int i = 0; i < 10; ++i) { a = dot2(vm[i][0], ONE2, a); b = dot2(vm[i][1], ONE2, b); a = dot2(vm[i][2], ONE2, a); b = dot2(vm[i][3], ONE2, b); }
      c2 = sum8(a + b);
      off2 = (c8 == 0 && rr < 5) ? WS_CD + (n8 * 160 + 10 * mt + 5 * (w - 2) + rr) * 4 : 0xfffffff0u;
    }
    asm volatile("" : "+v"(c1), "+v"(c2), "+v"(rso));
    __builtin_amdgcn_raw_buffer_store_b32(__float_as_uint(c1), rw, WS_CS + (((n8 * 4 + w) * 16 + mt) * 64 + 8 * c8 + rr) * 4, 0, 16);
    __builtin_amdgcn_raw_buffer_store_b32(__float_as_uint(c2), rw, off2, 0, 16);
    __builtin_amdgcn_raw_buffer_store_b32(__float_as_uint(rso), rw, rho < 2 ? WS_RS + mt * 1024 + rho * 512 + (n8 * 16 + 4 * w + q) * 4 : 0xfffffff0u, 0, 16);
  }
  bf16x8 FX[2][4], FD[2][5];
  s16x4 FW[2][2 * NC][2];
  constexpr int NR = TG ? 20 : 17, NM = TG ? 32 : 36;
  auto rd1 = [&](int s, int buf, int ks, int r) {
    if (r >= 2 && r < 6) {
      const int i = r - 2;
      FX[s][i] = *(LAS bf16x8*)(smem + (ks ? vX1 : vX0) + buf * 16384 + (TG ? i * 2048 : (i >> 1) * 4096 + (i & 1) * 512));
    } else if (r < 4 + 4 * NC) {
      const int h = r < 2 ? r : r - 4, f = h >> 1;
      FW[s][f][h & 1] = __builtin_amdgcn_ds_read_tr16_b64_v4i16((LAS s16x4*)(smem + vW[f >> 1] + buf * 16384 + ks * 4096 + (f & 1) * 8192 + (h & 1) * 512));
    } else {
      const int jj = r - (4 + 4 * NC);
      FD[s][jj] = *(LAS bf16x8*)(smem + (ks ? vD1 : vD0) + buf * 20480 + jj * 2048);
    }
  };
  auto mm1 = [&](int s, int m, bool first) {
    const int f = m >> 2, i = (f & 1) ? 3 - (m & 3) : (m & 3);
    if (f < 2 * NC) {
      const bf16x8 wf = __builtin_shufflevector(FW[s][f][0], FW[s][f][1], 0, 1, 2, 3, 4, 5, 6, 7);
      mf((f & 1) ? aU[f >> 1][i] : aG[f >> 1][i], wf, FX[s][i], first);
    } else mf(aA[i][f - 2 * NC], FX[s][i], FD[s][f - 2 * NC], first);
  };
  auto seg = [&](int s, bool first, int m0, int m1, int rk, int buf, int ks) __attribute__((always_inline)) {
    int r = 0;
#pragma unroll
    for (int m = m0; m < m1; ++m) {
      mm1(s, m, first);
      if (rk == 2 && r == 0) r = 2;
      if (rk == 2 && r == 6) r = 4 + 4 * NC;
      const int rend = rk == 0 ? 0 : rk == 3 ? 4 + 4 * NC : NR;
      if (r < rend && (TG ? (m % 5 != 4) : (rk == 3 || m % 3 != 2))) { rd1(s ^ 1, buf, ks, r); ++r; }
      PIN();
    }
  };
  BAR();
  STAMP_AT(1);
#pragma unroll
  for (int r = 0; r < NR; ++r) rd1(0, 0, 0, r);
  PIN();
  constexpr int KJ = TG ? 10 : 7;
  sfor<0, 10>([&](auto K) __attribute__((always_inline)) {
    constexpr int k = decltype(K)::v;
    constexpr int m0 = k < KJ ? 0 : 8 * NC;
    seg(0, k == 0, m0, NM, k < KJ ? 1 : 2, k % 3, 1);
    if (k == 1) {
      wait_v<0>();
      if (lane == 0) st_flag(FLAG(C_F0 + (mt * 16 + ns) * 4 + w), tag);
    }
    if (k < 9) {
      WAIT_L0();
      BAR();
      PIN();
    }
    if (k < 9) seg(1, false, m0, NM, k + 1 < KJ ? 1 : 2, (k + 1) % 3, 0);
    else seg(1, false, m0, NM, TG ? 0 : 3, KJ % 3, 0);
    WAIT_L0(); PIN();
  });
  if (!TG) {
    MFMA_DONE();
    after_mfma(aA);
    asm volatile("" : "+v"(lane)); q = lane >> 4; rho = lane & 15;
    STAMP_AT(4);
    const float sgn = (n8 >= 4) ? -1.0f : 1.0f;
    float cs[5];
#pragma unroll
    for (int j = 0; j < 5; ++j) cs[j] = *(LAS float*)(smem + T_CD + (80 * wn + 16 * j + rho) * 4);
    const int kt = (n8 >= 4 ? 32 : 0) + mt * 2 + wm;
#pragma unroll
    for (int I = 0; I < 2; ++I) {
      f32x4 r4[2], m4[2];
#pragma unroll
      for (int t = 0; t < 2; ++t) {
        const int row = 64 * wm + 32 * I + 8 * q + 4 * t;
        r4[t] = *(LAS f32x4*)(smem + T_RS + row * 4) * sgn; m4[t] = *(LAS f32x4*)(smem + T_RM + row * 4);
      }
      const unsigned dst = WS_AB + ((((n8 & 3) * 64 + kt) * 2 + I) * 10 + 5 * wn) * 1024 + lane * 16;
#pragma unroll
      for (int j = 0; j < 5; ++j) {
        const f32x4 v0 = (aA[2 * I][j] - m4[0] * cs[j]) * r4[0], v1 = (aA[2 * I + 1][j] - m4[1] * cs[j]) * r4[1];
        const u32x4 o = {pk_bf16(v0[0], v0[1]), pk_bf16(v0[2], v0[3]), pk_bf16(v1[0], v1[1]), pk_bf16(v1[2], v1[3])};
        st16c(rw, dst + j * 1024, o);
      }
    }
    STAMP_AT(7);
    WAIT_L0(); PIN();
#pragma unroll
    for (int k = KJ; k < 10; ++k) {
      seg(0, false, 0, 8 * NC, 3, k % 3, 1);
      WAIT_L0(); PIN();
      if (k == KJ + AFL) {
        wait_v<0>();
        if (lane == 0) st_flag(FLAG(C_FAB + ((n8 & 3) * 64 + kt) * 2 + wn), tag);
        PIN();
      }
      seg(1, false, 0, 8 * NC, k < 9 ? 3 : 0, (k + 1) % 3, 0);
      WAIT_L0(); PIN();
    }
  }
  MFMA_DONE();
  after_mfma(aG); after_mfma(aU);
  asm volatile("" : "+v"(lane)); q = lane >> 4; rho = lane & 15;
  BAR();
  STAMP_AT(2);
  {
    const float K0 = -2.0f * 0.7978845608028654f * 1.4426950408889634f, K1 = K0 * 0.044715f;
    float mu[4], c0[4], c1[4], v2[4];
#pragma unroll
    for (int i = 0; i < 4; ++i) {
      const int row = TG ? 64 * wm + 16 * i + rho : 64 * wm + 32 * (i >> 1) + 8 * (rho >> 2) + 4 * (i & 1) + (rho & 3);
      const float rs = *(LAS float*)(smem + T_RS + row * 4);
      mu[i] = *(LAS float*)(smem + T_RM + row * 4);
      c0[i] = K0 * rs; c1[i] = K1 * rs * rs * rs; v2[i] = __builtin_amdgcn_rcpf(rs * rs);
    }
#pragma unroll
    for (int J = 0; J < NC / 2; ++J) {
      f32x4 cg[2], cu[2];
#pragma unroll
      for (int t = 0; t < 2; ++t) {
        const int c = 2 * J + t;
        const int o = TG ? (wn * 256 + c * 64 + q * 16) : ((2 * wn + c) * 64 + q * 16);
        cg[t] = *(LAS f32x4*)(smem + T_CS + o);
        cu[t] = *(LAS f32x4*)(smem + T_CS + (TG ? 512 : 256) + o);
      }
#pragma unroll
      for (int i = 0; i < 4; ++i) {
        u32x4 o;
#pragma unroll
        for (int t = 0; t < 2; ++t) {
          const f32x4 g = aG[2 * J + t][i] - cg[t] * mu[i];
          const f32x4 u = aU[2 * J + t][i] - cu[t] * mu[i];
          const f32x4 z = g * (g * g * c1[i] + c0[i]);
          f32x4 e;
#pragma unroll
          for (int r = 0; r < 4; ++r) e[r] = __builtin_amdgcn_exp2f(z[r]);
          e = e * v2[i] + v2[i];
#pragma unroll
          for (int r = 0; r < 4; ++r) e[r] = __builtin_amdgcn_rcpf(e[r]);
          const f32x4 hv = g * u * e;
          o[2 * t] = pk_bf16(hv[0], hv[1]); o[2 * t + 1] = pk_bf16(hv[2], hv[3]);
        }
        const int f0 = hcol0 + (TG ? wn * 64 + J * 32 : wn * 32);
        const int rb = TG ? 4 * wm + i : 4 * wm + 2 * (i >> 1) + (rho >> 3), pos = TG ? lane : 8 * ((rho >> 2) & 1) + 4 * (i & 1) + (rho & 3) + 16 * q;
        st16c(rw, WS_H + (((mt * 24 + (f0 >> 6)) * 8 + rb) * 2 + ((f0 >> 5) & 1)) * 1024 + pos * 16, o);
      }
    }
  }
  STAMP_AT(3);
}

DI void mfma_p2(const Params& P, LAS char* smem, int lane, int w, int split, int mt, int nt, int mt1, int ns, u64 tag) {
  asm volatile("" : "+v"(lane));
  const int wm = w & 1, wn = w >> 1, q = lane >> 4, r15 = lane & 15;
  unsigned vY0 = wm * 8192 + r15 * 128 + ((q ^ (r15 & 7)) * 16), vY1 = vY0 ^ 64;
  unsigned vB0 = L_B + wn * 5120 + lane * 16, vB2 = vB0 + 2 * B_ST;
  asm volatile("" : "+v"(vY0), "+v"(vY1), "+v"(vB0), "+v"(vB2));
  const __amdgpu_buffer_rsrc_t rw = __builtin_amdgcn_make_buffer_rsrc(P.ws, 0, WS_END, 0x00020000);
  const int tile = mt * 4 + nt;
  f32x4 acc[4][5];
  bf16x8 FA[2][4], FB[2][5];
  auto rdy = [&](int s, int k, int ks, int r) {
    const int buf = k & 3;
    if (r >= 1 && r < 5) FA[s][r - 1] = *(LAS bf16x8*)(smem + (ks ? vY1 : vY0) + buf * A_ST + (r - 1) * 2048);
    else { const int j = r ? r - 4 : 0; FB[s][j] = *(LAS bf16x8*)(smem + (buf >= 2 ? vB2 : vB0) + (buf & 1) * B_ST + ks * 10240 + j * 1024); }
  };
  auto seg = [&](auto K, auto HF) __attribute__((always_inline)) {
    constexpr int k = decltype(K)::v, hf = decltype(HF)::v, rk = k + hf, rh = hf ^ 1;
    int r = 0;
#pragma unroll
    for (int m = 0; m < 20; ++m) {
      const int i = (m & 4) ? 3 - (m & 3) : (m & 3);
      mf(acc[i][m >> 2], FB[hf][m >> 2], FA[hf][i], k == 0 && hf == 0);
      if (rk < NY && r < 9 && m % 3 != 2) { rdy(rh, rk, rh, r); ++r; }
      PIN();
    }
  };
  auto publish = [&](int ip) {
    u32x4 o[5];
#pragma unroll
    for (int t = 0; t < 2; ++t) {
      const int i = 2 * ip + t;
#pragma unroll
      for (int j = 0; j < 5; ++j) {
        const int n = j < 4 ? 2 * t + (j >> 1) : 4, e = j < 4 ? (j & 1) * 2 : 2 * t;
        o[n][e] = pk_bf16(acc[i][j][0], acc[i][j][1]); o[n][e + 1] = pk_bf16(acc[i][j][2], acc[i][j][3]);
      }
    }
    if (w == split) {
#pragma unroll
      for (int n = 0; n < 5; ++n) *(LAS u32x4*)(smem + F_Y + (n < 4 ? ip * 4096 + n * 1024 : 8192 + ip * 1024) + lane * 16) = o[n];
    } else {
      const unsigned dst = WS_P + ((tile * 4 + w) * 3 + (split ^ w) - 1) * 10240 + lane * 16;
#pragma unroll
      for (int n = 0; n < 5; ++n) if (!NOPUB) st16c(rw, dst + (n < 4 ? ip * 4096 + n * 1024 : 8192 + ip * 1024), o[n]);
    }
  };
  BAR();
  STAMP_AT(5);
#pragma unroll
  for (int r = 0; r < 9; ++r) rdy(0, 0, 0, r);
  PIN();
  sfor<0, NY>([&](auto K) __attribute__((always_inline)) {
    constexpr int k = decltype(K)::v;
    if (CPK == k) __builtin_amdgcn_s_setprio(3);
    seg(K, IC<0>{});
    if (k == HFL) {
      wait_v<0>();
      if (lane == 0) st_flag(FLAG(C_FH + mt1 * 64 + ns * 4 + w), tag);
    }
    if (k + 1 < NY) {
      WAIT_L0();
      BAR();
      PIN();
    }
    seg(K, IC<1>{});
    WAIT_L0(); PIN();
  });
  MFMA_DONE();
  after_mfma(acc);
  if (threadIdx.x == 0) { P.stamps[blockIdx.x * 32 + 2 * 14] = __builtin_amdgcn_s_memrealtime(); }
  __builtin_amdgcn_s_setprio(3);
  publish(0); publish(1);
  if (HOLD) BAR();
  if (w != split) {
    wait_v<0>();
    if (lane == 0) st_flag(FLAG(C_FP + (tile * 4 + w) * 4 + (split ^ w) - 1), tag);
  }
  if (threadIdx.x == 0) { P.stamps[blockIdx.x * 32 + 2 * 15] = __builtin_amdgcn_s_memrealtime(); }
  const int orow = mt * 128 + (split & 1) * 64 + w * 16, ocol = nt * 160 + (split >> 1) * 80;
  u32x4 xv[3];
  unsigned tr[3], oo[3];
#pragma unroll
  for (int i = 0; i < 3; ++i) {
    const int n = min(i * 64 + lane, 159);
    const unsigned off = ((orow + n / 10) * D + ocol + (n % 10) * 8) * 2;
    xv[i] = *(const u32x4*)((const char*)P.x + off);
    tr[i] = F_T + w * (16 * FPITCH) + (n / 10) * FPITCH + (n % 10) * 16;
    oo[i] = i * 64 + lane < 160 ? off : 0xfffffff0u;
  }
  if (w == split) {
    const u64* fp = FLAG(C_FP + (tile * 4 + split) * 4 + min(lane, 2));
    __builtin_amdgcn_s_sleep(PD0);
#pragma unroll 1
    for (int it = 0; it < 40000; ++it) {
      if (__all(ld_flag(fp) == tag)) break;
      __builtin_amdgcn_s_sleep(PD1);
    }
    WAIT_L0();
    if (lane == 0) *(volatile LAS unsigned*)(smem + T_GO) = 2;
  } else {
    __builtin_amdgcn_s_setprio(0);
#pragma unroll 1
    for (int it = 0; it < 4000000; ++it) {
      if (*(volatile LAS unsigned*)(smem + T_GO) == 2) break;
      __builtin_amdgcn_s_sleep(4);
    }
    __builtin_amdgcn_s_setprio(3);
  }
  asm volatile("" ::: "memory");
  if (threadIdx.x == 0) { P.stamps[blockIdx.x * 32 + 2 * 13] = __builtin_amdgcn_s_memrealtime(); }
  f32x4 ys[5];
  {
    u32x4 pa[3][2]; u32x2 pb[3];
#pragma unroll
    for (int s = 0; s < 3; ++s) {
      const unsigned src = WS_P + ((tile * 4 + split) * 3 + s) * 10240 + lane * 16;
      pa[s][0] = ld16c(rw, src + w * 2048); pa[s][1] = ld16c(rw, src + w * 2048 + 1024);
      pb[s] = __builtin_amdgcn_raw_buffer_load_b64(rw, src + 8192 + (w >> 1) * 1024 + (w & 1) * 8, 0, 16);
    }
#pragma unroll
    for (int j = 0; j < 5; ++j) {
#pragma unroll
      for (int s = 0; s < 3; ++s) {
        const unsigned lo = j < 4 ? pa[s][j >> 1][(j & 1) * 2] : pb[s][0], hi = j < 4 ? pa[s][j >> 1][(j & 1) * 2 + 1] : pb[s][1];
        const f32x4 v = {bflo(lo), bfhi(lo), bflo(hi), bfhi(hi)};
        ys[j] = s ? ys[j] + v : v;
      }
      asm volatile("" : "+v"(ys[j]));
    }
  }
  {
    LAS char* po = smem + F_Y + lane * 16;
    const u32x4 oa0 = *(LAS u32x4*)(po + w * 2048), oa1 = *(LAS u32x4*)(po + w * 2048 + 1024);
    const u32x2 ob = *(LAS u32x2*)(po + 8192 + (w >> 1) * 1024 + (w & 1) * 8);
#pragma unroll
    for (int j = 0; j < 5; ++j) {
      const unsigned l0 = j < 2 ? oa0[(j & 1) * 2] : j < 4 ? oa1[(j & 1) * 2] : ob[0], h0 = j < 2 ? oa0[(j & 1) * 2 + 1] : j < 4 ? oa1[(j & 1) * 2 + 1] : ob[1];
      ys[j] += (f32x4){bflo(l0), bfhi(l0), bflo(h0), bfhi(h0)};
      asm volatile("" : "+v"(ys[j]));
    }
  }
  unsigned tw = F_T + w * (16 * FPITCH) + r15 * FPITCH + q * 8, rd0 = w * 5120 + lane * 16, rd2 = rd0 + L_B;
  asm volatile("" : "+v"(tw), "+v"(rd0), "+v"(rd2), "+v"(tr[0]), "+v"(tr[1]), "+v"(tr[2]), "+v"(oo[0]), "+v"(oo[1]), "+v"(oo[2]));
  STAMP_AT(6);
  BAR();
  f32x4 dd[4][5];
#pragma unroll
  for (int j = 0; j < 5; ++j)
#pragma unroll
    for (int u = 0; u < 4; ++u) dd[u][j] = *(LAS f32x4*)(smem + ((u & 2) ? rd2 : rd0) + (u & 1) * 20480 + j * 1024);
#pragma unroll
  for (int j = 0; j < 5; ++j) {
    const f32x4 d = (dd[0][j] + dd[1][j]) + (dd[2][j] + dd[3][j]);
    f32x4 e = d * -1.4426950408889634f;
#pragma unroll
    for (int r = 0; r < 4; ++r) e[r] = __builtin_amdgcn_exp2f(e[r]);
    e = e + 1.0f;
#pragma unroll
    for (int r = 0; r < 4; ++r) e[r] = __builtin_amdgcn_rcpf(e[r]);
    e = e * d * ys[j];
    *(LAS u32x2*)(smem + tw + j * 32) = (u32x2){pk_bf16(e[0], e[1]), pk_bf16(e[2], e[3])};
  }
  WAIT_L0();
  const __amdgpu_buffer_rsrc_t ro = __builtin_amdgcn_make_buffer_rsrc(P.out, 0, S * D * 2, 0x00020000);
#pragma unroll
  for (int i = 0; i < 3; ++i) {
    const u32x4 v = *(LAS u32x4*)(smem + tr[i]);
    u32x4 o;
#pragma unroll
    for (int d = 0; d < 4; ++d) o[d] = pk_bf16(bflo(v[d]) + bflo(xv[i][d]), bfhi(v[d]) + bfhi(xv[i][d]));
    __builtin_amdgcn_raw_buffer_store_b128(o, ro, oo[i], 0, 0);
  }
  wait_v<0>();
  if (threadIdx.x == 0) { P.stamps[blockIdx.x * 32 + 2 * 12] = __builtin_amdgcn_s_memrealtime(); }
}

__global__ __launch_bounds__(512, 2)
void fused(Params P) {
  extern __shared__ __attribute__((aligned(16))) char smem_[];
  LAS char* smem = (LAS char*)smem_;
  const int tid = threadIdx.x, lane = tid & 63, w = __builtin_amdgcn_readfirstlane(tid >> 6);
  const int bid = blockIdx.x, xcd = bid & 7, idx = bid >> 3;
  u64 tag;
  STAMP_AT(0);
  const int mt1 = (xcd & 1) * 8 + (idx & 7), j = idx >> 3, ns = (j & 1) * 8 + (xcd >> 1) * 2 + (j >> 1);
  const int split = xcd >> 1, mt2 = (xcd & 1) * 8 + (idx >> 2), nt = idx & 3;
  if (w >= 4) {
    __builtin_amdgcn_s_setprio(HPRIO);
    helper(P, smem, lane, w & 3, mt1, ns, split, mt2, nt);
  } else {
    if (j & 1) mfma_p1<false>(P, smem, lane, w, mt1, ns, tag); else mfma_p1<true>(P, smem, lane, w, mt1, ns, tag);
    mfma_p2(P, smem, lane, w, split, mt2, nt, mt1, ns, tag);
    __builtin_amdgcn_s_waitcnt(0);
  }
}

__global__ void diag(const u64* st, int a, int b, int mode) {
  const int lane = threadIdx.x;
  u64 mn = ~0ull, mx = 0, sum = 0;
  for (int i = 0; i < 4; ++i) { const int blk = i * 64 + lane; const u64 x = st[blk * 32 + 2 * a], y = st[blk * 32 + 2 * b];
    mn = x < mn ? x : mn; mx = y > mx ? y : mx; sum += mode == 2 ? (st[blk * 32 + 2 * b + 1] - st[blk * 32 + 2 * a + 1]) / 24 : y - x; }
  for (int o = 1; o < 64; o <<= 1) { const u64 m2 = __shfl_xor(mn, o), x2 = __shfl_xor(mx, o); mn = m2 < mn ? m2 : mn; mx = x2 > mx ? x2 : mx; sum += __shfl_xor(sum, o); }
  u64 d = mode == 0 ? mx - mn : sum / 256;
  if (a < 0 || d > 20000) d = 0;
  const u64 t0 = __builtin_amdgcn_s_memrealtime();
  while (__builtin_amdgcn_s_memrealtime() - t0 < 2 * d) __builtin_amdgcn_s_sleep(1);
}
extern "C" void kernel_launch(void* const* d_in, const int* in_sizes, int n_in, void* d_out, int out_size, void* d_ws, size_t ws_size, hipStream_t stream) {
  Params P;
  P.x = (const bf16_t*)d_in[0]; P.wsr = (const bf16_t*)d_in[1]; P.wsi = (const bf16_t*)d_in[2];
  P.wdr = (const bf16_t*)d_in[3]; P.wdi = (const bf16_t*)d_in[4];
  P.wg = (const bf16_t*)d_in[6]; P.wu = (const bf16_t*)d_in[7]; P.wdn = (const bf16_t*)d_in[8];
  P.out = (bf16_t*)d_out; P.ws = (char*)d_ws;
  P.stamps = (u64*)((char*)d_ws + (128u << 20));
  (void)hipFuncSetAttribute((const void*)fused, hipFuncAttributeMaxDynamicSharedMemorySize, LDS_BYTES);
  fused<<<256, 512, LDS_BYTES, stream>>>(P);
  diag<<<1, 64, 0, stream>>>(P.stamps, 0, 0, 1);
}
```

```cpp
#include <hip/hip_runtime.h>
#include <hip/hip_bf16.h>
typedef unsigned short bf16_t;
typedef unsigned long long u64;
typedef short bf16x8 __attribute__((ext_vector_type(8)));
typedef short s16x4 __attribute__((ext_vector_type(4)));
typedef float f32x4 __attribute__((ext_vector_type(4)));
typedef float f32x2 __attribute__((ext_vector_type(2)));
typedef unsigned u32x4 __attribute__((ext_vector_type(4)));
typedef unsigned u32x2 __attribute__((ext_vector_type(2)));
typedef __bf16 bf16x2_t __attribute__((ext_vector_type(2)));
#define LAS __attribute__((address_space(3)))
#define GAS __attribute__((address_space(1)))
#define DI __device__ __forceinline__

#define STAMP 0
#define NPRE 2
#define ROT2 0
#define YFL 4
#define FABSL 1
#define AFL 2
#define HFL 2
#define PSPL 1
#define XG 2
#define XY 1
#define XD 1
#define YSB 2
#define WK 2
#define FABM 0
#define SK 5
#define HOLD 0
#define PD0 60
#define PD1 8
#define DPF 0
#define DFK 8
#define DP0 4
#define DPS 2
#define NOPUB 0
#define DPRIO 2
#define CPK 99
#define S0E 2
#define TPRIO 3
#define B0E 0
#define PFG 1
#define PFM 1
#define PH0 0
#define PHS 4
#define PFS 10
#define YPL 8
#define XROT 0
#define HPRIO 0
#define PFN 4

constexpr int S = 2048, D = 640, F = 1536;
constexpr unsigned WS_AB = 0, WS_H = WS_AB + 2 * S * D * 2, WS_P = WS_H + S * F * 2, WS_WD = WS_P + 3 * S * D * 2, WS_RS = WS_WD + F * D * 2,
                   WS_CS = WS_RS + S * 8, WS_CD = WS_CS + 16 * 4 * 16 * 64 * 4, WS_CTRL = WS_CD + 8192, WS_END = WS_CTRL + 5120 * 128;
constexpr int C_E = 0, C_F0 = 1, C_FAB = C_F0 + 1024, C_FH = C_FAB + 512, C_FW = C_FH + 1024, C_FP = C_FW + 512;
#define FLAG(i) ((u64*)(P.ws + WS_CTRL) + (i) * 16)

DI unsigned pk_bf16(float lo, float hi) { f32x2 v = {lo, hi}; return __builtin_bit_cast(unsigned, __builtin_convertvector(v, bf16x2_t)); }
DI float rbf(float f) { return __uint_as_float(pk_bf16(f, f) << 16); }
DI float bflo(unsigned u) { return __uint_as_float(u << 16); }
DI float bfhi(unsigned u) { return __uint_as_float(u & 0xffff0000u); }
DI float dot2(unsigned a, unsigned b, float c) { return __builtin_amdgcn_fdot2_f32_bf16(__builtin_bit_cast(bf16x2_t, a), __builtin_bit_cast(bf16x2_t, b), c, false); }
#define GLDS4(POL) asm volatile("s_mov_b32 m0, %0\n\ts_nop 0\n\tglobal_load_lds_dwordx4 %2, %1" POL "\n\tglobal_load_lds_dwordx4 %3, %1 offset:1024" POL \
    "\n\tglobal_load_lds_dwordx4 %4, %1 offset:2048" POL "\n\tglobal_load_lds_dwordx4 %5, %1 offset:3072" POL \
    :: "s"(lds), "s"(base), "v"(v0), "v"(v1), "v"(v2), "v"(v3) : "memory")
#define GLDS5(POL) asm volatile("s_mov_b32 m0, %0\n\ts_nop 0\n\tglobal_load_lds_dwordx4 %2, %1" POL "\n\tglobal_load_lds_dwordx4 %3, %1 offset:1024" POL \
    "\n\tglobal_load_lds_dwordx4 %4, %1 offset:2048" POL "\n\tglobal_load_lds_dwordx4 %5, %1 offset:3072" POL \
    "\n\ts_add_u32 m0, %0, 0x1000\n\ts_nop 0\n\tglobal_load_lds_dwordx4 %6, %1" POL \
    :: "s"(lds), "s"(base), "v"(v0), "v"(v1), "v"(v2), "v"(v3), "v"(v4) : "memory", "scc")
template <bool DEV> DI void glds4(unsigned lds, const char* base, unsigned v0, unsigned v1, unsigned v2, unsigned v3) {
  if (DEV) GLDS4(" sc1"); else GLDS4("");
}
template <bool DEV> DI void glds5(unsigned lds, const char* base, unsigned v0, unsigned v1, unsigned v2, unsigned v3, unsigned v4) {
  if (DEV) GLDS5(" sc1"); else GLDS5("");
}
template <bool DEV = false> DI void touch_lines(unsigned lds, unsigned voff, const char* base) {
  if (DEV) asm volatile("s_mov_b32 m0, %0\n\ts_nop 0\n\tglobal_load_lds_dword %1, %2 sc1" :: "s"(lds), "v"(voff), "s"(base) : "memory");
  else asm volatile("s_mov_b32 m0, %0\n\ts_nop 0\n\tglobal_load_lds_dword %1, %2" :: "s"(lds), "v"(voff), "s"(base) : "memory");
}
template <int N> DI void wait_v() { asm volatile("s_waitcnt vmcnt(%0)" :: "i"(N) : "memory"); }
#define WAIT_L0() do { __builtin_amdgcn_s_waitcnt(0xC07F); asm volatile("" ::: "memory"); } while (0)
#define BAR() do { asm volatile("" ::: "memory"); __builtin_amdgcn_s_barrier(); asm volatile("" ::: "memory"); } while (0)
#define SGB(m, n) __builtin_amdgcn_sched_group_barrier(m, n, 0)
#define PIN() __builtin_amdgcn_sched_barrier(0)
template <int V> struct IC { static constexpr int v = V; };
template <int I, int N, class Fn> DI void sfor(Fn&& f) { if constexpr (I < N) { f(IC<I>{}); sfor<I + 1, N>(f); } }
DI void mf(f32x4& c, const bf16x8& a, const bf16x8& b, bool first) {
  if (first) asm volatile("v_mfma_f32_16x16x32_bf16 %0, %1, %2, 0" : "=&v"(c) : "v"(a), "v"(b));
  else asm volatile("v_mfma_f32_16x16x32_bf16 %0, %1, %2, %0" : "+v"(c) : "v"(a), "v"(b));
}
#define MFMA_DONE() asm volatile("s_nop 15")
template <int A, int B> DI void after_mfma(f32x4 (&c)[A][B]) {
#pragma unroll
  for (int i = 0; i < A; ++i)
#pragma unroll
    for (int j = 0; j < B; ++j) asm volatile("" : "+v"(c[i][j]));
}
#define STAMP_AT(i)

template <int CTRL> DI float dpp_add(float v) { return v + __builtin_bit_cast(float, __builtin_amdgcn_update_dpp(0, __builtin_bit_cast(int, v), CTRL, 0xf, 0xf, true)); }
DI float sum8(float v) { v = dpp_add<0xB1>(v); v = dpp_add<0x4E>(v); return dpp_add<0x141>(v); }
DI float sum16(float v) { return dpp_add<0x140>(sum8(v)); }
DI float xchg16(float keep_lo, float keep_hi) {
  const auto r = __builtin_amdgcn_permlane16_swap(__float_as_uint(keep_lo), __float_as_uint(keep_hi), false, false);
  return __uint_as_float(r[0]) + __uint_as_float(r[1]);
}
DI float xchg32(float keep_lo, float keep_hi) {
  const auto r = __builtin_amdgcn_permlane32_swap(__float_as_uint(keep_lo), __float_as_uint(keep_hi), false, false);
  return __uint_as_float(r[0]) + __uint_as_float(r[1]);
}
#define TIC(v)
#define ACC(a, x, y)
#define PUT(slot, a)
DI u64 ld_flag(const u64* p) { return __hip_atomic_load((const GAS u64*)p, __ATOMIC_RELAXED, __HIP_MEMORY_SCOPE_AGENT); }
DI void st_flag(u64* p, u64 v) { __hip_atomic_store((GAS u64*)p, v, __ATOMIC_RELAXED, __HIP_MEMORY_SCOPE_AGENT); }
DI void wait_flags(const u64* p, u64 tag) {
#pragma unroll 1
  for (int it = 0; it < 60000; ++it) {
    if (__all(ld_flag(p) == tag)) break;
    __builtin_amdgcn_s_sleep(4);
  }
  asm volatile("" ::: "memory");
}
DI void wait_flags2(const u64* p, const u64* p2, u64 tag) {
#pragma unroll 1
  for (int it = 0; it < 60000; ++it) {
    const u64 a = ld_flag(p), b = ld_flag(p2);
    if (__all(a == tag && b == tag)) break;
    __builtin_amdgcn_s_sleep(4);
  }
  asm volatile("" ::: "memory");
}
DI u64 read_tag(const char* ws) {
  u64 e;
  asm volatile("s_load_dwordx2 %0, %1, 0x0\n\ts_waitcnt lgkmcnt(0)" : "=s"(e) : "s"(ws + WS_CTRL) : "memory");
  return e * 6364136223846793005ull + 1442695040888963407ull;
}
DI void st16c(__amdgpu_buffer_rsrc_t r, unsigned off, u32x4 v) { __builtin_amdgcn_raw_buffer_store_b128(v, r, off, 0, 16); }
DI u32x4 ld16c(__amdgpu_buffer_rsrc_t r, unsigned off) { return __builtin_amdgcn_raw_buffer_load_b128(r, off, 0, 16); }

struct Params {
  const bf16_t *x, *wsr, *wsi, *wdr, *wdi, *wg, *wu, *wdn;
  bf16_t* out; char* ws;
};

constexpr int L_W = 49152, L_D = 98304, K1_TAB = 159744, LDS_BYTES = K1_TAB + 4096;
constexpr int T_RS = K1_TAB, T_RM = K1_TAB + 512, T_CS = K1_TAB + 1024, T_CD = K1_TAB + 2048, T_GO = K1_TAB + 3584, T_PF = K1_TAB + 3072;
constexpr int A_ST = 16384, B_ST = 20480, L_B = 4 * A_ST;

constexpr int NY = 16;
constexpr int F_Y = 147456, F_T = 106496, FPITCH = 176;

DI int f_red(int lw) { return (lw >> 1) * L_B + (lw & 1) * 20480; }

DI void helper(const Params& P, LAS char* smem, int lane, int lw, int mt, int ns, int split, int mt2, int nt) {
  constexpr int NL = 12;
  const bool TG = ns < 8;
  const int n8 = ns & 7, hcol0 = TG ? 128 * n8 : 1024 + 64 * n8;
  const int rr = lane >> 3, c8 = lane & 7;
  const unsigned lds0 = (unsigned)(size_t)smem;
  unsigned vX[4], vW[4], vD[5];
#pragma unroll
  for (int j = 0; j < 4; ++j) {
    vX[j] = (32 * lw + 8 * j + rr) * 1280 + ((c8 ^ (TG ? rr : ((rr & 2) | ((j & 2) << 1)))) * 16) - j * 1024;
    vW[j] = (8 * j + rr) * 3072 + ((c8 ^ ((rr & 2) | ((j & 1) << 2))) * 16) - j * 1024;
  }
#pragma unroll
  for (int j = 0; j < 5; ++j) vD[j] = (40 * lw + 8 * j + rr) * 1280 + ((c8 ^ rr) * 16) - (j & 3) * 1024;
  const char* bx = (const char*)P.x + (size_t)mt * 128 * 1280;
  const char* bw = (const char*)((lw >> 1) ? P.wu : P.wg) + hcol0 * 2 + (TG ? (lw & 1) * 128 : (lw & 1) * (32 * 3072));
  const char* bd = (const char*)(n8 >= 4 ? P.wdi : P.wdr) + (size_t)(n8 & 3) * 160 * 1280;
  const unsigned ldsX = __builtin_amdgcn_readfirstlane(lds0 + lw * 4096);
  const unsigned ldsW = __builtin_amdgcn_readfirstlane(lds0 + L_W + (TG ? (lw & 1) * 49152 + (lw >> 1) * 8192 : lw * 4096));
  const unsigned ldsD = __builtin_amdgcn_readfirstlane(lds0 + L_D + lw * 5120);
  auto issue_a = [&](int kt) {
    const int buf = kt % 3;
    glds4<false>(ldsX + buf * 16384, bx + kt * 128, vX[0], vX[1], vX[2], vX[3]);
    glds4<false>(ldsW + buf * 16384, bw + kt * (64 * 3072), vW[0], vW[1], vW[2], vW[3]);
  };
  auto issue_b = [&](int kt) {
    const int buf = kt % 3;
    if (TG) glds4<false>(ldsW + buf * 16384 + 4096, bw + kt * (64 * 3072) + 32 * 3072, vW[0], vW[1], vW[2], vW[3]);
    else glds5<false>(ldsD + buf * 20480, bd + kt * 128, vD[0], vD[1], vD[2], vD[3], vD[4]);
  };
  auto issue = [&](int kt) { issue_a(kt); issue_b(kt); };
  issue(0);
  issue_a(1);
  u64 tag;
  if (!S0E) issue_b(1);
  const char* pfb = (const char*)(split < 2 ? P.wsr : P.wsi) + (size_t)(mt2 * 128 + nt * 32) * 4096 + (split & 1) * 2048;
  const unsigned pfv = (lane & 31) * 4096 + (lw * 2 + (lane >> 5)) * 128;
  const u64* fa = FLAG(C_F0 + (lw == 0 ? mt * 64 + (lane & 31) : (((lane >> 1) & 15) * 16 + n8) * 4 + 2 + (lane & 1)));
  u64 fav = 0;
  const int xg = ((mt2 ^ nt) & 1) * XG;
  const unsigned vL = lane * 16, vL4 = vL + 4096;
  const char* baseWD = P.ws + WS_WD + ((nt * 48 + lw) * 10 + (split >> 1) * 5) * 1024;
  const int wtask = (mt * 8 + n8) * 4 + lw;
  const bool wdo = TG && wtask < 480;
  unsigned wv[16];
#pragma unroll
  for (int e = 0; e < 16; ++e) asm volatile("" : "=v"(wv[e]));
  sfor<0, 8>([&](auto K) __attribute__((always_inline)) {
    constexpr int k = decltype(K)::v;
    if (S0E && k == 0) wait_v<8>(); else if (S0E == 2 && k == 1) wait_v<8>(); else if (k == WK + 1 && wdo) wait_v<12 + 16>(); else if (k == WK + 3 && wdo) wait_v<12 + 4>(); else if (TG) wait_v<12>(); else wait_v<13>();
    BAR();
    if (S0E && k == 0) issue_b(1);
    if (S0E == 2 && k == 1) issue_b(2);
    if (lw < (TG ? 1 : 2)) {
      if (k == SK) asm volatile("global_load_dwordx2 %0, %1, off sc1" : "+v"(fav) : "v"(fa) : "memory");
      if (k == SK + 1) {
        asm volatile("" : "+v"(fav));
        if (!__all(fav == tag)) wait_flags(fa, tag);
        const char* src = P.ws + (lw == 0 ? WS_RS + mt * 1024 : WS_CD + n8 * 640);
        asm volatile("s_mov_b32 m0, %0\n\ts_nop 0\n\tglobal_load_lds_dwordx4 %1, %2 sc1" :: "s"(lds0 + (lw == 0 ? T_RS : T_CD)), "v"(vL), "s"(src) : "memory");
      }
    }
    if (S0E == 2 && k == 0) issue_a(2); else issue(k + 2);
    if (k == WK && wdo) {
#pragma unroll
      for (int u = 0; u < 4; ++u) {
        const int task = wtask * 2 + (u >> 1);
        const unsigned od = (((task % 240) / 5) * 32 + (lane >> 4) * 4) * 1280 + ((task / 240) * 160 + (task % 5) * 32) * 2 + (lane & 15) * 4;
        const char* pd = (const char*)P.wdn + (u & 1) * (16 * 1280);
        asm volatile("global_load_dword %0, %4, %5\n\tglobal_load_dword %1, %4, %5 offset:1280\n\tglobal_load_dword %2, %4, %5 offset:2560\n\tglobal_load_dword %3, %4, %5 offset:3840"
                     : "+v"(wv[4 * u]), "+v"(wv[4 * u + 1]), "+v"(wv[4 * u + 2]), "+v"(wv[4 * u + 3]) : "v"(od), "s"(pd) : "memory");
      }
    }
    if (k == WK + 2 && wdo) {
      const __amdgpu_buffer_rsrc_t rw = __builtin_amdgcn_make_buffer_rsrc(P.ws, 0, WS_END, 0x00020000);
      const bool up = lane & 8;
#pragma unroll
      for (int u = 0; u < 2; ++u) {
        u32x4 wo[2];
#pragma unroll
        for (int e = 0; e < 4; ++e) {
          asm volatile("" : "+v"(wv[8 * u + 2 * e]), "+v"(wv[8 * u + 2 * e + 1]));
          const unsigned lo = __builtin_amdgcn_perm(wv[8 * u + 2 * e + 1], wv[8 * u + 2 * e], 0x05040100u), hi = __builtin_amdgcn_perm(wv[8 * u + 2 * e + 1], wv[8 * u + 2 * e], 0x07060302u);
          const unsigned t = __builtin_amdgcn_update_dpp(0, up ? lo : hi, 0x128, 0xf, 0xf, true);
          wo[0][e] = up ? t : lo;
          wo[1][e] = up ? hi : t;
        }
        const int task = wtask * 2 + u;
        const unsigned dst = WS_WD + (((task / 240) * 48 + (task % 240) / 5) * 10 + (task % 5) * 2) * 1024 + (2 * (lane & 7) + ((lane >> 3) & 1) + (lane & 48)) * 16;
        st16c(rw, dst, wo[0]); st16c(rw, dst + 1024, wo[1]);
      }
    }
    if (k == 0) tag = read_tag(P.ws);
    if ((TG || PFM == 0) && k == PH0 + PHS * (lw >> 1) + (mt & 1)) touch_lines(lds0 + T_PF, pfv, pfb);
    if (k == 7) touch_lines(lds0 + T_PF, pfv, pfb + ((TG || PFM == 0) ? 1024 : 0));
  });
  wait_v<NL>();
  BAR();
  wait_v<1>();
  BAR();
  if (wdo && lane == 0) st_flag(FLAG(C_FW + wtask), tag);
  if (TG || lw < 2) {
    wait_flags(FLAG(C_F0 + ((lane & 15) * 16 + n8) * 4 + lw), tag);
    const __amdgpu_buffer_rsrc_t rw = __builtin_amdgcn_make_buffer_rsrc(P.ws, 0, WS_END, 0x00020000);
    f32x4 c = {0, 0, 0, 0};
#pragma unroll
    for (int i = 0; i < 4; ++i) c += __builtin_bit_cast(f32x4, ld16c(rw, WS_CS + (ns * 4 + lw) * 4096 + i * 1024 + lane * 16));
#pragma unroll
    for (int e = 0; e < 4; ++e) { c[e] = xchg16(c[e], c[e]); c[e] = xchg32(c[e], c[e]); }
    if (lane < 16) *(LAS f32x4*)(smem + T_CS + lw * 256 + lane * 16) = c;
  }
  if (lw == 0 && lane == 0) *(volatile LAS unsigned*)(smem + T_GO) = 0;
  WAIT_L0();
  if (!TG && PFM == 1) { if (lw < 2) __builtin_amdgcn_s_sleep(PFS); else __builtin_amdgcn_s_sleep(PFS + 24); touch_lines(lds0 + T_PF, pfv, pfb + 1024); }
  if (blockIdx.x == 64 && lw == 3) {
    wait_flags2(FLAG(C_F0 + lane * 4), FLAG(C_F0 + (64 + lane) * 4), tag);
    wait_flags2(FLAG(C_F0 + (128 + lane) * 4), FLAG(C_F0 + (192 + lane) * 4), tag);
    if (lane == 0) st_flag(FLAG(C_E), tag);
  }
  const u64* fab = FLAG(C_FAB + (nt * 64 + split * 16) * 2 + (lane & 31));
  unsigned vY[4];
#pragma unroll
  for (int j = 0; j < 4; ++j) vY[j] = (32 * lw + 8 * j + rr) * 4096 + ((c8 ^ rr) * 16) - j * 1024;
  const char* baseY = (const char*)(split < 2 ? P.wsr : P.wsi) + (size_t)mt2 * 128 * 4096 + (split & 1) * 2048;
  const char* baseAB = P.ws + WS_AB + (nt * 64 + split * 16) * B_ST + lw * 5120;
  const char* baseH = P.ws + WS_H + ((mt2 * 24 + (lw >> 1)) * 16 + (split & 1) * 8 + (lw & 1)) * 1024;
  const unsigned ldsA = ldsX, ldsB = __builtin_amdgcn_readfirstlane(lds0 + L_B + lw * 5120);
  auto issueYA = [&](int k, int yi) { glds4<false>(ldsA + (k & 3) * A_ST, baseY + (yi ^ xg) * 128, vY[0], vY[1], vY[2], vY[3]); };
  auto issueYB = [&](int k, int yi) { glds5<true>(ldsB + (k & 3) * B_ST, baseAB + (yi ^ xg) * B_ST, vL, vL, vL, vL, vL4); };
  BAR();
  __builtin_amdgcn_s_setprio(TPRIO);
  STAMP_AT(8);
  u64 fabv;
  if (FABM) asm volatile("global_load_dwordx2 %0, %1, off sc1" : "=&v"(fabv) : "v"(fab) : "memory"); else fabv = ~tag;
  issueYA(0, 0); issueYA(1, 1);
  if (FABM) wait_v<8>();
  asm volatile("" : "+v"(fabv));
  const bool ab_seen = __all(fabv == tag);
  if (lw == 0) {
    if (!ab_seen) wait_flags(fab, tag);
    if (lane == 0) *(volatile LAS unsigned*)(smem + T_GO) = 1;
  } else if (!ab_seen) {
#pragma unroll 1
    for (int it = 0; it < 4000000; ++it) {
      if (*(volatile LAS unsigned*)(smem + T_GO) != 0) break;
      __builtin_amdgcn_s_sleep(1);
    }
  }
  asm volatile("" ::: "memory");
  STAMP_AT(9);
  const u64* fh = FLAG(C_FH + mt2 * 64 + (lane & 31) * 2 + (split & 1));
  const int l36 = min(lane, 35);
  const int wt = (nt * 240 + ((l36 / 3) * 4 + lw) * 5 + (split >> 1) * 2 + l36 % 3) >> 1;
  const u64* fw = FLAG(C_FW + wt);
  u64 fhv = 0, fwv = 0;
  issueYB(0, 0);
  if (B0E) { wait_v<0>(); BAR(); issueYB(1, 1); } else { issueYB(1, 1); wait_v<5>(); BAR(); }
  issueYA(2, 2); issueYB(2, 2);
  wait_v<9>(); BAR();
  __builtin_amdgcn_s_setprio(0);
  issueYA(3, 3); issueYB(3, 3); issueYA(4, 4); issueYB(4, 4);
  const int ph = nt * 32 + lane, pw = (mt2 & 7) * 20 + min(max(lane - 32, 0), 19);
  const unsigned dstr = lane < 32 ? 2 * A_ST : 2 * B_ST;
  unsigned dpf = (lane < 32 ? WS_H + ((mt2 * 24 + (ph >> 6)) * 16 + (split & 1) * 8) * 1024 + (ph & 63) * 128
                            : WS_WD + ((nt * 48 + pw / 40) * 10 + (split >> 1) * 5) * 1024 + (pw % 40) * 128) + (DP0 + lw) * dstr;
#pragma unroll 1
  for (int k = 2; k < NY - 3; ++k) {
    if (k == DFK + 1) {
      wait_v<9>();
      asm volatile("" : "+v"(fhv), "+v"(fwv));
      if (!__all(fhv == tag && fwv == tag)) wait_flags2(fh, fw, tag);
    } else wait_v<18>();
    BAR();
    if (k == DFK) asm volatile("global_load_dwordx2 %0, %2, off sc1\n\tglobal_load_dwordx2 %1, %3, off sc1" : "+v"(fhv), "+v"(fwv) : "v"(fh), "v"(fw) : "memory");
    issueYA(k + 3, k + 3); issueYB(k + 3, k + 3);
    if (DPF) {
      const int n0 = (k - (DFK + 1)) * DPS;
      const int n = n0 + ((lw - n0) & 3);
      if (n0 >= 0 && n < n0 + DPS && DP0 + n < 12) { touch_lines<true>(lds0 + T_PF, dpf, P.ws); dpf += 4 * dstr; }
    }
  }
  bf16x8 GA[4][4], GB[4][5];
  f32x4 dac[4][5];
  auto loadD = [&](int i) {
    const int s = i & 3;
    const char* pa = baseH + (i ^ xg) * (2 * A_ST);
    const char* pb = baseWD + (i ^ xg) * (2 * B_ST);
    asm volatile("global_load_dwordx4 %0, %9, %12 sc1\n\tglobal_load_dwordx4 %4, %9, %11 sc1\n\tglobal_load_dwordx4 %5, %9, %11 offset:2048 sc1\n\t"
                 "global_load_dwordx4 %6, %10, %11 sc1\n\tglobal_load_dwordx4 %7, %10, %11 offset:2048 sc1\n\t"
                 "global_load_dwordx4 %1, %9, %12 offset:1024 sc1\n\tglobal_load_dwordx4 %2, %9, %12 offset:2048 sc1\n\t"
                 "global_load_dwordx4 %3, %9, %12 offset:3072 sc1\n\tglobal_load_dwordx4 %8, %10, %12 sc1"
                 : "=&v"(GB[s][0]), "=&v"(GB[s][1]), "=&v"(GB[s][2]), "=&v"(GB[s][3]), "=&v"(GA[s][0]), "=&v"(GA[s][1]), "=&v"(GA[s][2]), "=&v"(GA[s][3]), "=&v"(GB[s][4])
                 : "v"(vL), "v"(vL4), "s"(pa), "s"(pb) : "memory");
  };
  auto load1 = [&](int i, int n) {
    const int s = i & 3;
    const char* pa = baseH + (i ^ xg) * (2 * A_ST);
    const char* pb = baseWD + (i ^ xg) * (2 * B_ST);
    if (n < 4) asm volatile("global_load_dwordx4 %0, %1, %2 offset:%3 sc1" : "=&v"(GA[s][n]) : "v"(n < 2 ? vL : vL4), "s"(pa), "i"((n & 1) * 2048) : "memory");
    else asm volatile("global_load_dwordx4 %0, %1, %2 offset:%3 sc1" : "=&v"(GB[s][n - 4]) : "v"(n < 8 ? vL : vL4), "s"(pb), "i"(((n - 4) & 3) * 1024) : "memory");
  };
  auto group = [&](auto I) __attribute__((always_inline)) {
    constexpr int i = decltype(I)::v, s = i & 3;
    wait_v<(i == 0 ? 27 : i < 10 ? 18 : (11 - i) * 9)>();
    asm volatile("" : "+v"(GA[s][0]), "+v"(GA[s][1]), "+v"(GA[s][2]), "+v"(GA[s][3]), "+v"(GB[s][0]), "+v"(GB[s][1]), "+v"(GB[s][2]), "+v"(GB[s][3]), "+v"(GB[s][4]));
#pragma unroll
    for (int m = 0; m < 20; ++m) {
      const int r = (m & 4) ? 3 - (m & 3) : (m & 3);
      mf(dac[r][m >> 2], GB[s][m >> 2], GA[s][r], i == 0);
      if (i >= 1 && i + 3 < 12 && (m & 1) == 1 && m < 18) load1(i + 3, m >> 1);
    }
  };
  loadD(0);
  wait_v<27>(); BAR();
  loadD(1);
  wait_v<27>(); BAR();
  loadD(2);
  wait_v<27>(); BAR();
  loadD(3);
  __builtin_amdgcn_s_setprio(DPRIO);
  sfor<0, 12>(group);
  MFMA_DONE();
  after_mfma(dac);
#pragma unroll
  for (int r = 0; r < 4; ++r)
#pragma unroll
    for (int j = 0; j < 5; ++j) *(LAS f32x4*)(smem + f_red(lw) + (r * 5 + j) * 1024 + lane * 16) = dac[r][j];
  WAIT_L0();
  STAMP_AT(11);
  BAR();
  STAMP_AT(10);
}

template <bool TG>
DI void mfma_p1(const Params& P, LAS char* smem, int lane, int w, int mt, int ns, u64& tag) {
  const int n8 = ns & 7, hcol0 = TG ? 128 * n8 : 1024 + 64 * n8;
  const __amdgpu_buffer_rsrc_t rw = __builtin_amdgcn_make_buffer_rsrc(P.ws, 0, WS_END, 0x00020000);
  int q = lane >> 4, rho = lane & 15;
  const int rr = lane >> 3, c8 = lane & 7;
  u32x4 vx[5], vw[5], vm[10];
  if (TG) {
    const char* px = (const char*)P.x + (size_t)(mt * 128 + n8 * 16 + 4 * w + q) * 1280 + rho * 16;
#pragma unroll
    for (int i = 0; i < 5; ++i) vx[i] = *(const u32x4*)(px + i * 256);
    const char* pw = (const char*)((w >> 1) ? P.wu : P.wg) + (size_t)(40 * mt + rr) * 3072 + (hcol0 + (w & 1) * 64) * 2 + c8 * 16;
#pragma unroll
    for (int i = 0; i < 5; ++i) vw[i] = *(const u32x4*)(pw + i * (8 * 3072));
    if (w < 2) {
      const char* pm = (const char*)(w ? P.wu : P.wg) + (size_t)(40 * mt + rr) * 3072 + (1024 + 64 * n8) * 2 + c8 * 16;
#pragma unroll
      for (int i = 0; i < 5; ++i) vm[i] = *(const u32x4*)(pm + i * (8 * 3072));
    } else {
      const int e = (n8 & 3) * 160 + 10 * mt + 5 * (w - 2) + min(rr, 4);
      const char* pd = (const char*)(n8 >= 4 ? P.wdi : P.wdr) + (size_t)e * 1280 + c8 * 16;
#pragma unroll
      for (int i = 0; i < 10; ++i) vm[i] = *(const u32x4*)(pd + i * 128);
    }
  }
  tag = read_tag(P.ws);
  const int wm = w & 1, wn = w >> 1;
  constexpr int NC = TG ? 4 : 2;
  unsigned vX0 = TG ? wm * 8192 + rho * 128 + ((q ^ (rho & 7)) * 16)
                          : wm * 8192 + (8 * (rho >> 2) + (rho & 3)) * 128 + ((q ^ ((rho & 2) | ((rho >> 1) & 4))) * 16);
  unsigned vX1 = vX0 ^ 64;
  unsigned vW[NC];
  {
    const int qq = rho >> 2, p = lane & 3, swz = (qq & 2) | ((q & 1) << 2);
#pragma unroll
    for (int c = 0; c < NC; ++c) {
      const int cc = TG ? c : (2 * wn + c);
      vW[c] = L_W + (TG ? wn * 49152 : 0) + (8 * q + qq) * 128 + ((((2 * cc) ^ swz) + (p >> 1)) * 16) + 8 * (p & 1);
    }
  }
  unsigned vD0 = L_D + (80 * wn + rho) * 128 + ((q ^ (rho & 7)) * 16), vD1 = vD0 ^ 64;
  asm volatile("" : "+v"(vX0), "+v"(vX1));
  if (!TG) asm volatile("" : "+v"(vD0), "+v"(vD1));
#pragma unroll
  for (int c = 0; c < NC; ++c) asm volatile("" : "+v"(vW[c]));
  f32x4 aG[NC][4], aU[NC][4], aA[4][5];
  if (TG) {
    unsigned ONE2 = 0x3f803f80u, SLO = 0x00003f80u, SHI = 0x3f800000u;
    asm volatile("" : "+v"(ONE2), "+v"(SLO), "+v"(SHI));
    float sx[2] = {0, 0}, sq[2] = {0, 0};
#pragma unroll
    for (int i = 0; i < 5; ++i)
#pragma unroll
      for (int e = 0; e < 4; ++e) { sx[e & 1] = dot2(vx[i][e], ONE2, sx[e & 1]); sq[e & 1] = dot2(vx[i][e], vx[i][e], sq[e & 1]); }
    const float mean = sum16(sx[0] + sx[1]) * (1.0f / D);
    const float var = fmaxf(sum16(sq[0] + sq[1]) * (1.0f / D) - mean * mean, 0.f);
    const float sd = rbf(__builtin_amdgcn_sqrtf(rbf(rbf(var) + 1e-5f)));
    float rso = (lane & 1) ? rbf(mean) : __builtin_amdgcn_rcpf(sd);
    auto colsum = [&](const u32x4* v) -> float {
      float c[8] = {0, 0, 0, 0, 0, 0, 0, 0};
#pragma unroll
      for (int i = 0; i < 5; ++i)
#pragma unroll
        for (int e = 0; e < 4; ++e) { c[2 * e] = dot2(v[i][e], SLO, c[2 * e]); c[2 * e + 1] = dot2(v[i][e], SHI, c[2 * e + 1]); }
      float d4[4], d2[2];
#pragma unroll
      for (int e = 0; e < 4; ++e) d4[e] = xchg32(c[e], c[4 + e]);
#pragma unroll
      for (int e = 0; e < 2; ++e) d2[e] = xchg16(d4[e], d4[2 + e]);
      d2[0] = dpp_add<0x128>(d2[0]); d2[1] = dpp_add<0x128>(d2[1]);
      return (lane & 8) ? d2[1] : d2[0];
    };
    float c1 = colsum(vw), c2; unsigned off2;
    if (w < 2) {
      c2 = colsum(vm);
      off2 = WS_CS + ((((8 + n8) * 4 + w) * 16 + mt) * 64 + 8 * c8 + rr) * 4;
    } else {
      float a = 0, b = 0;
#pragma unroll
      for (int i = 0; i < 10; ++i) { a = dot2(vm[i][0], ONE2, a); b = dot2(vm[i][1], ONE2, b); a = dot2(vm[i][2], ONE2, a); b = dot2(vm[i][3], ONE2, b); }
      c2 = sum8(a + b);
      off2 = (c8 == 0 && rr < 5) ? WS_CD + (n8 * 160 + 10 * mt + 5 * (w - 2) + rr) * 4 : 0xfffffff0u;
    }
    asm volatile("" : "+v"(c1), "+v"(c2), "+v"(rso));
    __builtin_amdgcn_raw_buffer_store_b32(__float_as_uint(c1), rw, WS_CS + (((n8 * 4 + w) * 16 + mt) * 64 + 8 * c8 + rr) * 4, 0, 16);
    __builtin_amdgcn_raw_buffer_store_b32(__float_as_uint(c2), rw, off2, 0, 16);
    __builtin_amdgcn_raw_buffer_store_b32(__float_as_uint(rso), rw, rho < 2 ? WS_RS + mt * 1024 + rho * 512 + (n8 * 16 + 4 * w + q) * 4 : 0xfffffff0u, 0, 16);
  }
  bf16x8 FX[2][4], FD[2][5];
  s16x4 FW[2][2 * NC][2];
  constexpr int NR = TG ? 20 : 17, NM = TG ? 32 : 36;
  auto rd1 = [&](int s, int buf, int ks, int r) {
    if (r >= 2 && r < 6) {
      const int i = r - 2;
      FX[s][i] = *(LAS bf16x8*)(smem + (ks ? vX1 : vX0) + buf * 16384 + (TG ? i * 2048 : (i >> 1) * 4096 + (i & 1) * 512));
    } else if (r < 4 + 4 * NC) {
      const int h = r < 2 ? r : r - 4, f = h >> 1;
      FW[s][f][h & 1] = __builtin_amdgcn_ds_read_tr16_b64_v4i16((LAS s16x4*)(smem + vW[f >> 1] + buf * 16384 + ks * 4096 + (f & 1) * 8192 + (h & 1) * 512));
    } else {
      const int jj = r - (4 + 4 * NC);
      FD[s][jj] = *(LAS bf16x8*)(smem + (ks ? vD1 : vD0) + buf * 20480 + jj * 2048);
    }
  };
  auto mm1 = [&](int s, int m, bool first) {
    const int f = m >> 2, i = (f & 1) ? 3 - (m & 3) : (m & 3);
    if (f < 2 * NC) {
      const bf16x8 wf = __builtin_shufflevector(FW[s][f][0], FW[s][f][1], 0, 1, 2, 3, 4, 5, 6, 7);
      mf((f & 1) ? aU[f >> 1][i] : aG[f >> 1][i], wf, FX[s][i], first);
    } else mf(aA[i][f - 2 * NC], FX[s][i], FD[s][f - 2 * NC], first);
  };
  auto seg = [&](int s, bool first, int m0, int m1, int rk, int buf, int ks) __attribute__((always_inline)) {
    int r = 0;
#pragma unroll
    for (int m = m0; m < m1; ++m) {
      mm1(s, m, first);
      if (rk == 2 && r == 0) r = 2;
      if (rk == 2 && r == 6) r = 4 + 4 * NC;
      const int rend = rk == 0 ? 0 : rk == 3 ? 4 + 4 * NC : NR;
      if (r < rend && (TG ? (m % 5 != 4) : (rk == 3 || m % 3 != 2))) { rd1(s ^ 1, buf, ks, r); ++r; }
      PIN();
    }
  };
  BAR();
  STAMP_AT(1);
#pragma unroll
  for (int r = 0; r < NR; ++r) rd1(0, 0, 0, r);
  PIN();
  constexpr int KJ = TG ? 10 : 7;
  sfor<0, 10>([&](auto K) __attribute__((always_inline)) {
    constexpr int k = decltype(K)::v;
    constexpr int m0 = k < KJ ? 0 : 8 * NC;
    seg(0, k == 0, m0, NM, k < KJ ? 1 : 2, k % 3, 1);
    if (k == 1) {
      wait_v<0>();
      if (lane == 0) st_flag(FLAG(C_F0 + (mt * 16 + ns) * 4 + w), tag);
    }
    if (k < 9) {
      WAIT_L0();
      BAR();
      PIN();
    }
    if (k < 9) seg(1, false, m0, NM, k + 1 < KJ ? 1 : 2, (k + 1) % 3, 0);
    else seg(1, false, m0, NM, TG ? 0 : 3, KJ % 3, 0);
    WAIT_L0(); PIN();
  });
  if (!TG) {
    MFMA_DONE();
    after_mfma(aA);
    asm volatile("" : "+v"(lane)); q = lane >> 4; rho = lane & 15;
    STAMP_AT(4);
    const float sgn = (n8 >= 4) ? -1.0f : 1.0f;
    float cs[5];
#pragma unroll
    for (int j = 0; j < 5; ++j) cs[j] = *(LAS float*)(smem + T_CD + (80 * wn + 16 * j + rho) * 4);
    const int kt = (n8 >= 4 ? 32 : 0) + mt * 2 + wm;
#pragma unroll
    for (int I = 0; I < 2; ++I) {
      f32x4 r4[2], m4[2];
#pragma unroll
      for (int t = 0; t < 2; ++t) {
        const int row = 64 * wm + 32 * I + 8 * q + 4 * t;
        r4[t] = *(LAS f32x4*)(smem + T_RS + row * 4) * sgn; m4[t] = *(LAS f32x4*)(smem + T_RM + row * 4);
      }
      const unsigned dst = WS_AB + ((((n8 & 3) * 64 + kt) * 2 + I) * 10 + 5 * wn) * 1024 + lane * 16;
#pragma unroll
      for (int j = 0; j < 5; ++j) {
        const f32x4 v0 = (aA[2 * I][j] - m4[0] * cs[j]) * r4[0], v1 = (aA[2 * I + 1][j] - m4[1] * cs[j]) * r4[1];
        const u32x4 o = {pk_bf16(v0[0], v0[1]), pk_bf16(v0[2], v0[3]), pk_bf16(v1[0], v1[1]), pk_bf16(v1[2], v1[3])};
        st16c(rw, dst + j * 1024, o);
      }
    }
    STAMP_AT(7);
    WAIT_L0(); PIN();
#pragma unroll
    for (int k = KJ; k < 10; ++k) {
      seg(0, false, 0, 8 * NC, 3, k % 3, 1);
      WAIT_L0(); PIN();
      if (k == KJ + AFL) {
        wait_v<0>();
        if (lane == 0) st_flag(FLAG(C_FAB + ((n8 & 3) * 64 + kt) * 2 + wn), tag);
        PIN();
      }
      seg(1, false, 0, 8 * NC, k < 9 ? 3 : 0, (k + 1) % 3, 0);
      WAIT_L0(); PIN();
    }
  }
  MFMA_DONE();
  after_mfma(aG); after_mfma(aU);
  asm volatile("" : "+v"(lane)); q = lane >> 4; rho = lane & 15;
  BAR();
  STAMP_AT(2);
  {
    const float K0 = -2.0f * 0.7978845608028654f * 1.4426950408889634f, K1 = K0 * 0.044715f;
    float mu[4], c0[4], c1[4], v2[4];
#pragma unroll
    for (int i = 0; i < 4; ++i) {
      const int row = TG ? 64 * wm + 16 * i + rho : 64 * wm + 32 * (i >> 1) + 8 * (rho >> 2) + 4 * (i & 1) + (rho & 3);
      const float rs = *(LAS float*)(smem + T_RS + row * 4);
      mu[i] = *(LAS float*)(smem + T_RM + row * 4);
      c0[i] = K0 * rs; c1[i] = K1 * rs * rs * rs; v2[i] = __builtin_amdgcn_rcpf(rs * rs);
    }
#pragma unroll
    for (int J = 0; J < NC / 2; ++J) {
      f32x4 cg[2], cu[2];
#pragma unroll
      for (int t = 0; t < 2; ++t) {
        const int c = 2 * J + t;
        const int o = TG ? (wn * 256 + c * 64 + q * 16) : ((2 * wn + c) * 64 + q * 16);
        cg[t] = *(LAS f32x4*)(smem + T_CS + o);
        cu[t] = *(LAS f32x4*)(smem + T_CS + (TG ? 512 : 256) + o);
      }
#pragma unroll
      for (int i = 0; i < 4; ++i) {
        u32x4 o;
#pragma unroll
        for (int t = 0; t < 2; ++t) {
          const f32x4 g = aG[2 * J + t][i] - cg[t] * mu[i];
          const f32x4 u = aU[2 * J + t][i] - cu[t] * mu[i];
          const f32x4 z = g * (g * g * c1[i] + c0[i]);
          f32x4 e;
#pragma unroll
          for (int r = 0; r < 4; ++r) e[r] = __builtin_amdgcn_exp2f(z[r]);
          e = e * v2[i] + v2[i];
#pragma unroll
          for (int r = 0; r < 4; ++r) e[r] = __builtin_amdgcn_rcpf(e[r]);
          const f32x4 hv = g * u * e;
          o[2 * t] = pk_bf16(hv[0], hv[1]); o[2 * t + 1] = pk_bf16(hv[2], hv[3]);
        }
        const int f0 = hcol0 + (TG ? wn * 64 + J * 32 : wn * 32);
        const int rb = TG ? 4 * wm + i : 4 * wm + 2 * (i >> 1) + (rho >> 3), pos = TG ? lane : 8 * ((rho >> 2) & 1) + 4 * (i & 1) + (rho & 3) + 16 * q;
        st16c(rw, WS_H + (((mt * 24 + (f0 >> 6)) * 8 + rb) * 2 + ((f0 >> 5) & 1)) * 1024 + pos * 16, o);
      }
    }
  }
  STAMP_AT(3);
}

DI void mfma_p2(const Params& P, LAS char* smem, int lane, int w, int split, int mt, int nt, int mt1, int ns, u64 tag) {
  asm volatile("" : "+v"(lane));
  const int wm = w & 1, wn = w >> 1, q = lane >> 4, r15 = lane & 15;
  unsigned vY0 = wm * 8192 + r15 * 128 + ((q ^ (r15 & 7)) * 16), vY1 = vY0 ^ 64;
  unsigned vB0 = L_B + wn * 5120 + lane * 16, vB2 = vB0 + 2 * B_ST;
  asm volatile("" : "+v"(vY0), "+v"(vY1), "+v"(vB0), "+v"(vB2));
  const __amdgpu_buffer_rsrc_t rw = __builtin_amdgcn_make_buffer_rsrc(P.ws, 0, WS_END, 0x00020000);
  const int tile = mt * 4 + nt;
  f32x4 acc[4][5];
  bf16x8 FA[2][4], FB[2][5];
  auto rdy = [&](int s, int k, int ks, int r) {
    const int buf = k & 3;
    if (r >= 1 && r < 5) FA[s][r - 1] = *(LAS bf16x8*)(smem + (ks ? vY1 : vY0) + buf * A_ST + (r - 1) * 2048);
    else { const int j = r ? r - 4 : 0; FB[s][j] = *(LAS bf16x8*)(smem + (buf >= 2 ? vB2 : vB0) + (buf & 1) * B_ST + ks * 10240 + j * 1024); }
  };
  auto seg = [&](auto K, auto HF) __attribute__((always_inline)) {
    constexpr int k = decltype(K)::v, hf = decltype(HF)::v, rk = k + hf, rh = hf ^ 1;
    int r = 0;
#pragma unroll
    for (int m = 0; m < 20; ++m) {
      const int i = (m & 4) ? 3 - (m & 3) : (m & 3);
      mf(acc[i][m >> 2], FB[hf][m >> 2], FA[hf][i], k == 0 && hf == 0);
      if (rk < NY && r < 9 && m % 3 != 2) { rdy(rh, rk, rh, r); ++r; }
      PIN();
    }
  };
  auto publish = [&](int ip) {
    u32x4 o[5];
#pragma unroll
    for (int t = 0; t < 2; ++t) {
      const int i = 2 * ip + t;
#pragma unroll
      for (int j = 0; j < 5; ++j) {
        const int n = j < 4 ? 2 * t + (j >> 1) : 4, e = j < 4 ? (j & 1) * 2 : 2 * t;
        o[n][e] = pk_bf16(acc[i][j][0], acc[i][j][1]); o[n][e + 1] = pk_bf16(acc[i][j][2], acc[i][j][3]);
      }
    }
    if (w == split) {
#pragma unroll
      for (int n = 0; n < 5; ++n) *(LAS u32x4*)(smem + F_Y + (n < 4 ? ip * 4096 + n * 1024 : 8192 + ip * 1024) + lane * 16) = o[n];
    } else {
      const unsigned dst = WS_P + ((tile * 4 + w) * 3 + (split ^ w) - 1) * 10240 + lane * 16;
#pragma unroll
      for (int n = 0; n < 5; ++n) if (!NOPUB) st16c(rw, dst + (n < 4 ? ip * 4096 + n * 1024 : 8192 + ip * 1024), o[n]);
    }
  };
  BAR();
  STAMP_AT(5);
#pragma unroll
  for (int r = 0; r < 9; ++r) rdy(0, 0, 0, r);
  PIN();
  sfor<0, NY>([&](auto K) __attribute__((always_inline)) {
    constexpr int k = decltype(K)::v;
    if (CPK == k) __builtin_amdgcn_s_setprio(3);
    seg(K, IC<0>{});
    if (k == HFL) {
      wait_v<0>();
      if (lane == 0) st_flag(FLAG(C_FH + mt1 * 64 + ns * 4 + w), tag);
    }
    if (k + 1 < NY) {
      WAIT_L0();
      BAR();
      PIN();
    }
    seg(K, IC<1>{});
    WAIT_L0(); PIN();
  });
  MFMA_DONE();
  after_mfma(acc);
  __builtin_amdgcn_s_setprio(3);
  publish(0); publish(1);
  if (HOLD) BAR();
  if (w != split) {
    wait_v<0>();
    if (lane == 0) st_flag(FLAG(C_FP + (tile * 4 + w) * 4 + (split ^ w) - 1), tag);
  }
  const int orow = mt * 128 + (split & 1) * 64 + w * 16, ocol = nt * 160 + (split >> 1) * 80;
  u32x4 xv[3];
  unsigned tr[3], oo[3];
#pragma unroll
  for (int i = 0; i < 3; ++i) {
    const int n = min(i * 64 + lane, 159);
    const unsigned off = ((orow + n / 10) * D + ocol + (n % 10) * 8) * 2;
    xv[i] = *(const u32x4*)((const char*)P.x + off);
    tr[i] = F_T + w * (16 * FPITCH) + (n / 10) * FPITCH + (n % 10) * 16;
    oo[i] = i * 64 + lane < 160 ? off : 0xfffffff0u;
  }
  if (w == split) {
    const u64* fp = FLAG(C_FP + (tile * 4 + split) * 4 + min(lane, 2));
    __builtin_amdgcn_s_sleep(PD0);
#pragma unroll 1
    for (int it = 0; it < 40000; ++it) {
      if (__all(ld_flag(fp) == tag)) break;
      __builtin_amdgcn_s_sleep(PD1);
    }
    WAIT_L0();
    if (lane == 0) *(volatile LAS unsigned*)(smem + T_GO) = 2;
  } else {
    __builtin_amdgcn_s_setprio(0);
#pragma unroll 1
    for (int it = 0; it < 4000000; ++it) {
      if (*(volatile LAS unsigned*)(smem + T_GO) == 2) break;
      __builtin_amdgcn_s_sleep(4);
    }
    __builtin_amdgcn_s_setprio(3);
  }
  asm volatile("" ::: "memory");
  f32x4 ys[5];
  {
    u32x4 pa[3][2]; u32x2 pb[3];
#pragma unroll
    for (int s = 0; s < 3; ++s) {
      const unsigned src = WS_P + ((tile * 4 + split) * 3 + s) * 10240 + lane * 16;
      pa[s][0] = ld16c(rw, src + w * 2048); pa[s][1] = ld16c(rw, src + w * 2048 + 1024);
      pb[s] = __builtin_amdgcn_raw_buffer_load_b64(rw, src + 8192 + (w >> 1) * 1024 + (w & 1) * 8, 0, 16);
    }
#pragma unroll
    for (int j = 0; j < 5; ++j) {
#pragma unroll
      for (int s = 0; s < 3; ++s) {
        const unsigned lo = j < 4 ? pa[s][j >> 1][(j & 1) * 2] : pb[s][0], hi = j < 4 ? pa[s][j >> 1][(j & 1) * 2 + 1] : pb[s][1];
        const f32x4 v = {bflo(lo), bfhi(lo), bflo(hi), bfhi(hi)};
        ys[j] = s ? ys[j] + v : v;
      }
      asm volatile("" : "+v"(ys[j]));
    }
  }
  {
    LAS char* po = smem + F_Y + lane * 16;
    const u32x4 oa0 = *(LAS u32x4*)(po + w * 2048), oa1 = *(LAS u32x4*)(po + w * 2048 + 1024);
    const u32x2 ob = *(LAS u32x2*)(po + 8192 + (w >> 1) * 1024 + (w & 1) * 8);
#pragma unroll
    for (int j = 0; j < 5; ++j) {
      const unsigned l0 = j < 2 ? oa0[(j & 1) * 2] : j < 4 ? oa1[(j & 1) * 2] : ob[0], h0 = j < 2 ? oa0[(j & 1) * 2 + 1] : j < 4 ? oa1[(j & 1) * 2 + 1] : ob[1];
      ys[j] += (f32x4){bflo(l0), bfhi(l0), bflo(h0), bfhi(h0)};
      asm volatile("" : "+v"(ys[j]));
    }
  }
  unsigned tw = F_T + w * (16 * FPITCH) + r15 * FPITCH + q * 8, rd0 = w * 5120 + lane * 16, rd2 = rd0 + L_B;
  asm volatile("" : "+v"(tw), "+v"(rd0), "+v"(rd2), "+v"(tr[0]), "+v"(tr[1]), "+v"(tr[2]), "+v"(oo[0]), "+v"(oo[1]), "+v"(oo[2]));
  STAMP_AT(6);
  BAR();
  f32x4 dd[4][5];
#pragma unroll
  for (int j = 0; j < 5; ++j)
#pragma unroll
    for (int u = 0; u < 4; ++u) dd[u][j] = *(LAS f32x4*)(smem + ((u & 2) ? rd2 : rd0) + (u & 1) * 20480 + j * 1024);
#pragma unroll
  for (int j = 0; j < 5; ++j) {
    const f32x4 d = (dd[0][j] + dd[1][j]) + (dd[2][j] + dd[3][j]);
    f32x4 e = d * -1.4426950408889634f;
#pragma unroll
    for (int r = 0; r < 4; ++r) e[r] = __builtin_amdgcn_exp2f(e[r]);
    e = e + 1.0f;
#pragma unroll
    for (int r = 0; r < 4; ++r) e[r] = __builtin_amdgcn_rcpf(e[r]);
    e = e * d * ys[j];
    *(LAS u32x2*)(smem + tw + j * 32) = (u32x2){pk_bf16(e[0], e[1]), pk_bf16(e[2], e[3])};
  }
  WAIT_L0();
  const __amdgpu_buffer_rsrc_t ro = __builtin_amdgcn_make_buffer_rsrc(P.out, 0, S * D * 2, 0x00020000);
#pragma unroll
  for (int i = 0; i < 3; ++i) {
    const u32x4 v = *(LAS u32x4*)(smem + tr[i]);
    u32x4 o;
#pragma unroll
    for (int d = 0; d < 4; ++d) o[d] = pk_bf16(bflo(v[d]) + bflo(xv[i][d]), bfhi(v[d]) + bfhi(xv[i][d]));
    __builtin_amdgcn_raw_buffer_store_b128(o, ro, oo[i], 0, 0);
  }
}

__global__ __launch_bounds__(512, 2)
void fused(Params P) {
  extern __shared__ __attribute__((aligned(16))) char smem_[];
  LAS char* smem = (LAS char*)smem_;
  const int tid = threadIdx.x, lane = tid & 63, w = __builtin_amdgcn_readfirstlane(tid >> 6);
  const int bid = blockIdx.x, xcd = bid & 7, idx = bid >> 3;
  u64 tag;
  STAMP_AT(0);
  const int mt1 = (xcd & 1) * 8 + (idx & 7), j = idx >> 3, ns = (j & 1) * 8 + (xcd >> 1) * 2 + (j >> 1);
  const int split = xcd >> 1, mt2 = (xcd & 1) * 8 + (idx >> 2), nt = idx & 3;
  if (w >= 4) {
    __builtin_amdgcn_s_setprio(HPRIO);
    helper(P, smem, lane, w & 3, mt1, ns, split, mt2, nt);
  } else {
    if (j & 1) mfma_p1<false>(P, smem, lane, w, mt1, ns, tag); else mfma_p1<true>(P, smem, lane, w, mt1, ns, tag);
    mfma_p2(P, smem, lane, w, split, mt2, nt, mt1, ns, tag);
    __builtin_amdgcn_s_waitcnt(0);
  }
}

extern "C" void kernel_launch(void* const* d_in, const int* in_sizes, int n_in, void* d_out, int out_size, void* d_ws, size_t ws_size, hipStream_t stream) {
  Params P;
  P.x = (const bf16_t*)d_in[0]; P.wsr = (const bf16_t*)d_in[1]; P.wsi = (const bf16_t*)d_in[2];
  P.wdr = (const bf16_t*)d_in[3]; P.wdi = (const bf16_t*)d_in[4];
  P.wg = (const bf16_t*)d_in[6]; P.wu = (const bf16_t*)d_in[7]; P.wdn = (const bf16_t*)d_in[8];
  P.out = (bf16_t*)d_out; P.ws = (char*)d_ws;
  (void)hipFuncSetAttribute((const void*)fused, hipFuncAttributeMaxDynamicSharedMemorySize, LDS_BYTES);
  fused<<<256, 512, LDS_BYTES, stream>>>(P);
}
```

```cpp
#include <hip/hip_runtime.h>
#include <hip/hip_bf16.h>
typedef unsigned short bf16_t;
typedef unsigned long long u64;
typedef short bf16x8 __attribute__((ext_vector_type(8)));
typedef short s16x4 __attribute__((ext_vector_type(4)));
typedef float f32x4 __attribute__((ext_vector_type(4)));
typedef float f32x2 __attribute__((ext_vector_type(2)));
typedef unsigned u32x4 __attribute__((ext_vector_type(4)));
typedef unsigned u32x2 __attribute__((ext_vector_type(2)));
typedef __bf16 bf16x2_t __attribute__((ext_vector_type(2)));
#define LAS __attribute__((address_space(3)))
#define GAS __attribute__((address_space(1)))
#define DI __device__ __forceinline__

#define STAMP 0
#define NPRE 2
#define ROT2 0
#define YFL 4
#define FABSL 1
#define AFL 1
#define HFL 2
#define PSPL 1
#define XG 2
#define XY 1
#define XD 1
#define YSB 2
#define WK 2
#define FABM 2
#define SK 5
#define HOLD 0
#define PD0 60
#define PD1 8
#define DPF 0
#define DFK 8
#define DP0 4
#define DPS 2
#define NOPUB 0
#define DPRIO 2
#define CPK 99
#define S0E 2
#define TPRIO 3
#define B0E 0
#define PFG 1
#define PFM 2
#define PH0 0
#define PHS 4
#define PFS 10
#define CPF 1
#define P0M 0
#define PFQ 1
#define YPL 8
#define XROT 0
#define HPRIO 0
#define PFN 4

constexpr int S = 2048, D = 640, F = 1536;
constexpr unsigned WS_AB = 0, WS_H = WS_AB + 2 * S * D * 2, WS_P = WS_H + S * F * 2, WS_WD = WS_P + 3 * S * D * 2, WS_RS = WS_WD + F * D * 2,
                   WS_CS = WS_RS + S * 8, WS_CD = WS_CS + 16 * 4 * 16 * 64 * 4, WS_CTRL = WS_CD + 8192, WS_END = WS_CTRL + 5120 * 128;
constexpr int C_E = 0, C_F0 = 1, C_FAB = C_F0 + 1024, C_FH = C_FAB + 512, C_FW = C_FH + 1024, C_FP = C_FW + 512;
#define FLAG(i) ((u64*)(P.ws + WS_CTRL) + (i) * 16)

DI unsigned pk_bf16(float lo, float hi) { f32x2 v = {lo, hi}; return __builtin_bit_cast(unsigned, __builtin_convertvector(v, bf16x2_t)); }
DI float rbf(float f) { return __uint_as_float(pk_bf16(f, f) << 16); }
DI float bflo(unsigned u) { return __uint_as_float(u << 16); }
DI float bfhi(unsigned u) { return __uint_as_float(u & 0xffff0000u); }
DI float dot2(unsigned a, unsigned b, float c) { return __builtin_amdgcn_fdot2_f32_bf16(__builtin_bit_cast(bf16x2_t, a), __builtin_bit_cast(bf16x2_t, b), c, false); }
#define GLDS4(POL) asm volatile("s_mov_b32 m0, %0\n\ts_nop 0\n\tglobal_load_lds_dwordx4 %2, %1" POL "\n\tglobal_load_lds_dwordx4 %3, %1 offset:1024" POL \
    "\n\tglobal_load_lds_dwordx4 %4, %1 offset:2048" POL "\n\tglobal_load_lds_dwordx4 %5, %1 offset:3072" POL \
    :: "s"(lds), "s"(base), "v"(v0), "v"(v1), "v"(v2), "v"(v3) : "memory")
#define GLDS5(POL) asm volatile("s_mov_b32 m0, %0\n\ts_nop 0\n\tglobal_load_lds_dwordx4 %2, %1" POL "\n\tglobal_load_lds_dwordx4 %3, %1 offset:1024" POL \
    "\n\tglobal_load_lds_dwordx4 %4, %1 offset:2048" POL "\n\tglobal_load_lds_dwordx4 %5, %1 offset:3072" POL \
    "\n\ts_add_u32 m0, %0, 0x1000\n\ts_nop 0\n\tglobal_load_lds_dwordx4 %6, %1" POL \
    :: "s"(lds), "s"(base), "v"(v0), "v"(v1), "v"(v2), "v"(v3), "v"(v4) : "memory", "scc")
template <bool DEV> DI void glds4(unsigned lds, const char* base, unsigned v0, unsigned v1, unsigned v2, unsigned v3) {
  if (DEV) GLDS4(" sc1"); else GLDS4("");
}
template <bool DEV> DI void glds5(unsigned lds, const char* base, unsigned v0, unsigned v1, unsigned v2, unsigned v3, unsigned v4) {
  if (DEV) GLDS5(" sc1"); else GLDS5("");
}
template <bool DEV = false> DI void touch_lines(unsigned lds, unsigned voff, const char* base) {
  if (DEV) asm volatile("s_mov_b32 m0, %0\n\ts_nop 0\n\tglobal_load_lds_dword %1, %2 sc1" :: "s"(lds), "v"(voff), "s"(base) : "memory");
  else asm volatile("s_mov_b32 m0, %0\n\ts_nop 0\n\tglobal_load_lds_dword %1, %2" :: "s"(lds), "v"(voff), "s"(base) : "memory");
}
template <int N> DI void wait_v() { asm volatile("s_waitcnt vmcnt(%0)" :: "i"(N) : "memory"); }
#define WAIT_L0() do { __builtin_amdgcn_s_waitcnt(0xC07F); asm volatile("" ::: "memory"); } while (0)
#define BAR() do { asm volatile("" ::: "memory"); __builtin_amdgcn_s_barrier(); asm volatile("" ::: "memory"); } while (0)
#define SGB(m, n) __builtin_amdgcn_sched_group_barrier(m, n, 0)
#define PIN() __builtin_amdgcn_sched_barrier(0)
template <int V> struct IC { static constexpr int v = V; };
template <int I, int N, class Fn> DI void sfor(Fn&& f) { if constexpr (I < N) { f(IC<I>{}); sfor<I + 1, N>(f); } }
DI void mf(f32x4& c, const bf16x8& a, const bf16x8& b, bool first) {
  if (first) asm volatile("v_mfma_f32_16x16x32_bf16 %0, %1, %2, 0" : "=&v"(c) : "v"(a), "v"(b));
  else asm volatile("v_mfma_f32_16x16x32_bf16 %0, %1, %2, %0" : "+v"(c) : "v"(a), "v"(b));
}
#define MFMA_DONE() asm volatile("s_nop 15")
template <int A, int B> DI void after_mfma(f32x4 (&c)[A][B]) {
#pragma unroll
  for (int i = 0; i < A; ++i)
#pragma unroll
    for (int j = 0; j < B; ++j) asm volatile("" : "+v"(c[i][j]));
}
#define STAMP_AT(i)

template <int CTRL> DI float dpp_add(float v) { return v + __builtin_bit_cast(float, __builtin_amdgcn_update_dpp(0, __builtin_bit_cast(int, v), CTRL, 0xf, 0xf, true)); }
DI float sum8(float v) { v = dpp_add<0xB1>(v); v = dpp_add<0x4E>(v); return dpp_add<0x141>(v); }
DI float sum16(float v) { return dpp_add<0x140>(sum8(v)); }
DI float xchg16(float keep_lo, float keep_hi) {
  const auto r = __builtin_amdgcn_permlane16_swap(__float_as_uint(keep_lo), __float_as_uint(keep_hi), false, false);
  return __uint_as_float(r[0]) + __uint_as_float(r[1]);
}
DI float xchg32(float keep_lo, float keep_hi) {
  const auto r = __builtin_amdgcn_permlane32_swap(__float_as_uint(keep_lo), __float_as_uint(keep_hi), false, false);
  return __uint_as_float(r[0]) + __uint_as_float(r[1]);
}
#define TIC(v)
#define ACC(a, x, y)
#define PUT(slot, a)
DI u64 ld_flag(const u64* p) { return __hip_atomic_load((const GAS u64*)p, __ATOMIC_RELAXED, __HIP_MEMORY_SCOPE_AGENT); }
DI void st_flag(u64* p, u64 v) { __hip_atomic_store((GAS u64*)p, v, __ATOMIC_RELAXED, __HIP_MEMORY_SCOPE_AGENT); }
DI void wait_flags(const u64* p, u64 tag) {
#pragma unroll 1
  for (int it = 0; it < 60000; ++it) {
    if (__all(ld_flag(p) == tag)) break;
    __builtin_amdgcn_s_sleep(4);
  }
  asm volatile("" ::: "memory");
}
DI void wait_flags2(const u64* p, const u64* p2, u64 tag) {
#pragma unroll 1
  for (int it = 0; it < 60000; ++it) {
    const u64 a = ld_flag(p), b = ld_flag(p2);
    if (__all(a == tag && b == tag)) break;
    __builtin_amdgcn_s_sleep(4);
  }
  asm volatile("" ::: "memory");
}
DI u64 read_tag(const char* ws) {
  u64 e;
  asm volatile("s_load_dwordx2 %0, %1, 0x0\n\ts_waitcnt lgkmcnt(0)" : "=s"(e) : "s"(ws + WS_CTRL) : "memory");
  return e * 6364136223846793005ull + 1442695040888963407ull;
}
DI void st16c(__amdgpu_buffer_rsrc_t r, unsigned off, u32x4 v) { __builtin_amdgcn_raw_buffer_store_b128(v, r, off, 0, 16); }
DI u32x4 ld16c(__amdgpu_buffer_rsrc_t r, unsigned off) { return __builtin_amdgcn_raw_buffer_load_b128(r, off, 0, 16); }

struct Params {
  const bf16_t *x, *wsr, *wsi, *wdr, *wdi, *wg, *wu, *wdn;
  bf16_t* out; char* ws;
};

constexpr int L_W = 49152, L_D = 98304, K1_TAB = 159744, LDS_BYTES = K1_TAB + 4096;
constexpr int T_RS = K1_TAB, T_RM = K1_TAB + 512, T_CS = K1_TAB + 1024, T_CD = K1_TAB + 2048, T_GO = K1_TAB + 3584, T_PF = K1_TAB + 3072;
constexpr int A_ST = 16384, B_ST = 20480, L_B = 4 * A_ST;

constexpr int NY = 16;
constexpr int F_Y = 147456, F_T = 106496, FPITCH = 176;

DI int f_red(int lw) { return (lw >> 1) * L_B + (lw & 1) * 20480; }

DI void helper(const Params& P, LAS char* smem, int lane, int lw, int mt, int ns, int split, int mt2, int nt) {
  constexpr int NL = 12;
  const bool TG = ns < 8;
  const int n8 = ns & 7, hcol0 = TG ? 128 * n8 : 1024 + 64 * n8;
  const int rr = lane >> 3, c8 = lane & 7;
  const unsigned lds0 = (unsigned)(size_t)smem;
  unsigned vX[4], vW[4], vD[5];
#pragma unroll
  for (int j = 0; j < 4; ++j) {
    vX[j] = (32 * lw + 8 * j + rr) * 1280 + ((c8 ^ (TG ? rr : ((rr & 2) | ((j & 2) << 1)))) * 16) - j * 1024;
    vW[j] = (8 * j + rr) * 3072 + ((c8 ^ ((rr & 2) | ((j & 1) << 2))) * 16) - j * 1024;
  }
#pragma unroll
  for (int j = 0; j < 5; ++j) vD[j] = (40 * lw + 8 * j + rr) * 1280 + ((c8 ^ rr) * 16) - (j & 3) * 1024;
  const char* bx = (const char*)P.x + (size_t)mt * 128 * 1280;
  const char* bw = (const char*)((lw >> 1) ? P.wu : P.wg) + hcol0 * 2 + (TG ? (lw & 1) * 128 : (lw & 1) * (32 * 3072));
  const char* bd = (const char*)(n8 >= 4 ? P.wdi : P.wdr) + (size_t)(n8 & 3) * 160 * 1280;
  const unsigned ldsX = __builtin_amdgcn_readfirstlane(lds0 + lw * 4096);
  const unsigned ldsW = __builtin_amdgcn_readfirstlane(lds0 + L_W + (TG ? (lw & 1) * 49152 + (lw >> 1) * 8192 : lw * 4096));
  const unsigned ldsD = __builtin_amdgcn_readfirstlane(lds0 + L_D + lw * 5120);
  auto issue_a = [&](int kt) {
    const int buf = kt % 3;
    glds4<false>(ldsX + buf * 16384, bx + kt * 128, vX[0], vX[1], vX[2], vX[3]);
    glds4<false>(ldsW + buf * 16384, bw + kt * (64 * 3072), vW[0], vW[1], vW[2], vW[3]);
  };
  auto issue_b = [&](int kt) {
    const int buf = kt % 3;
    if (TG) glds4<false>(ldsW + buf * 16384 + 4096, bw + kt * (64 * 3072) + 32 * 3072, vW[0], vW[1], vW[2], vW[3]);
    else glds5<false>(ldsD + buf * 20480, bd + kt * 128, vD[0], vD[1], vD[2], vD[3], vD[4]);
  };
  auto issue = [&](int kt) { issue_a(kt); issue_b(kt); };
  issue(0);
  issue_a(1);
  u64 tag;
  if (!S0E) issue_b(1);
  const char* pfb = (const char*)(split < 2 ? P.wsr : P.wsi) + (size_t)(mt2 * 128 + nt * 32) * 4096 + (split & 1) * 2048;
  const unsigned pfv = (lane & 31) * 4096 + (lw * 2 + (lane >> 5)) * 128;
  const int fcol = ((lane & 15) * 16 + ((P0M & 2) ? ns : n8)) * 4 + lw;
  const u64* fa = FLAG(C_F0 + ((TG && (lw > 0 || lane >= 32)) ? fcol : lw == 0 ? mt * 64 + (P0M & 1) * 32 + (lane & 31) : (((lane >> 1) & 15) * 16 + (P0M & 2) * 4 + n8) * 4 + 2 + (lane & 1)));
  u64 fav = 0;
  u32x4 cs[4];
#pragma unroll
  for (int e = 0; e < 4; ++e) asm volatile("" : "=v"(cs[e]));
  auto put_cs = [&]() {
    f32x4 c = (__builtin_bit_cast(f32x4, cs[0]) + __builtin_bit_cast(f32x4, cs[1])) + (__builtin_bit_cast(f32x4, cs[2]) + __builtin_bit_cast(f32x4, cs[3]));
#pragma unroll
    for (int e = 0; e < 4; ++e) { c[e] = xchg16(c[e], c[e]); c[e] = xchg32(c[e], c[e]); }
    if (lane < 16) *(LAS f32x4*)(smem + T_CS + lw * 256 + lane * 16) = c;
  };
  const int xg = ((mt2 ^ nt) & 1) * XG;
  const unsigned vL = lane * 16, vL4 = vL + 4096;
  const char* baseWD = P.ws + WS_WD + ((nt * 48 + lw) * 10 + (split >> 1) * 5) * 1024;
  const int wtask = (mt * 8 + n8) * 4 + lw;
  const bool wdo = TG && wtask < 480;
  unsigned wv[16];
#pragma unroll
  for (int e = 0; e < 16; ++e) asm volatile("" : "=v"(wv[e]));
  sfor<0, 8>([&](auto K) __attribute__((always_inline)) {
    constexpr int k = decltype(K)::v;
    if (S0E && k == 0) wait_v<8>(); else if (S0E == 2 && k == 1) wait_v<8>(); else if (k == WK + 1 && wdo) wait_v<12 + 16>(); else if (k == WK + 3 && wdo) wait_v<12 + 4>(); else if (TG) wait_v<12>(); else wait_v<13>();
    BAR();
    if (S0E && k == 0) issue_b(1);
    if (S0E == 2 && k == 1) issue_b(2);
    if (TG || lw < 2) {
      if (k == SK) asm volatile("global_load_dwordx2 %0, %1, off sc1" : "+v"(fav) : "v"(fa) : "memory");
      if (k == SK + 1) {
        asm volatile("" : "+v"(fav));
        if (!__all(fav == tag)) wait_flags(fa, tag);
        if (lw < (TG ? 1 : 2)) {
          const char* src = P.ws + (lw == 0 ? WS_RS + mt * 1024 : WS_CD + n8 * 640);
          asm volatile("s_mov_b32 m0, %0\n\ts_nop 0\n\tglobal_load_lds_dwordx4 %1, %2 sc1" :: "s"(lds0 + (lw == 0 ? T_RS : T_CD)), "v"(vL), "s"(src) : "memory");
        }
        if (TG) asm volatile("global_load_dwordx4 %0, %4, %5 sc1\n\tglobal_load_dwordx4 %1, %4, %5 offset:1024 sc1\n\tglobal_load_dwordx4 %2, %4, %5 offset:2048 sc1\n\t"
                             "global_load_dwordx4 %3, %4, %5 offset:3072 sc1"
                             : "+v"(cs[0]), "+v"(cs[1]), "+v"(cs[2]), "+v"(cs[3]) : "v"(vL), "s"(P.ws + WS_CS + (ns * 4 + lw) * 4096) : "memory");
      }
    }
    if (S0E == 2 && k == 0) issue_a(2); else issue(k + 2);
    if (k == WK && wdo) {
#pragma unroll
      for (int u = 0; u < 4; ++u) {
        const int task = wtask * 2 + (u >> 1);
        const unsigned od = (((task % 240) / 5) * 32 + (lane >> 4) * 4) * 1280 + ((task / 240) * 160 + (task % 5) * 32) * 2 + (lane & 15) * 4;
        const char* pd = (const char*)P.wdn + (u & 1) * (16 * 1280);
        asm volatile("global_load_dword %0, %4, %5\n\tglobal_load_dword %1, %4, %5 offset:1280\n\tglobal_load_dword %2, %4, %5 offset:2560\n\tglobal_load_dword %3, %4, %5 offset:3840"
                     : "+v"(wv[4 * u]), "+v"(wv[4 * u + 1]), "+v"(wv[4 * u + 2]), "+v"(wv[4 * u + 3]) : "v"(od), "s"(pd) : "memory");
      }
    }
    if (k == WK + 2 && wdo) {
      const __amdgpu_buffer_rsrc_t rw = __builtin_amdgcn_make_buffer_rsrc(P.ws, 0, WS_END, 0x00020000);
      const bool up = lane & 8;
#pragma unroll
      for (int u = 0; u < 2; ++u) {
        u32x4 wo[2];
#pragma unroll
        for (int e = 0; e < 4; ++e) {
          asm volatile("" : "+v"(wv[8 * u + 2 * e]), "+v"(wv[8 * u + 2 * e + 1]));
          const unsigned lo = __builtin_amdgcn_perm(wv[8 * u + 2 * e + 1], wv[8 * u + 2 * e], 0x05040100u), hi = __builtin_amdgcn_perm(wv[8 * u + 2 * e + 1], wv[8 * u + 2 * e], 0x07060302u);
          const unsigned t = __builtin_amdgcn_update_dpp(0, up ? lo : hi, 0x128, 0xf, 0xf, true);
          wo[0][e] = up ? t : lo;
          wo[1][e] = up ? hi : t;
        }
        const int task = wtask * 2 + u;
        const unsigned dst = WS_WD + (((task / 240) * 48 + (task % 240) / 5) * 10 + (task % 5) * 2) * 1024 + (2 * (lane & 7) + ((lane >> 3) & 1) + (lane & 48)) * 16;
        st16c(rw, dst, wo[0]); st16c(rw, dst + 1024, wo[1]);
      }
    }
    if (k == 0) tag = read_tag(P.ws);
    if ((TG || PFM == 0) && k == PH0 + PHS * (lw >> 1) + (mt & 1)) touch_lines(lds0 + T_PF, pfv, pfb);
    if (k == 7) touch_lines(lds0 + T_PF, pfv, pfb + ((TG || PFM == 0) ? 1024 : 0));
  });
  wait_v<NL>();
  BAR();
  wait_v<1>();
  BAR();
  if (wdo && lane == 0) st_flag(FLAG(C_FW + wtask), tag);
  if (TG) {
    asm volatile("" : "+v"(cs[0]), "+v"(cs[1]), "+v"(cs[2]), "+v"(cs[3]));
    put_cs();
  } else if (lw < 2) {
    wait_flags(FLAG(C_F0 + fcol), tag);
    const __amdgpu_buffer_rsrc_t rw = __builtin_amdgcn_make_buffer_rsrc(P.ws, 0, WS_END, 0x00020000);
#pragma unroll
    for (int i = 0; i < 4; ++i) cs[i] = ld16c(rw, WS_CS + (ns * 4 + lw) * 4096 + i * 1024 + lane * 16);
    put_cs();
  }
  if (lw == 0 && lane == 0) *(volatile LAS unsigned*)(smem + T_GO) = 0;
  WAIT_L0();
  if (!TG && PFM == 1) { if (lw < 2) __builtin_amdgcn_s_sleep(PFS); else __builtin_amdgcn_s_sleep(PFS + 24); touch_lines(lds0 + T_PF, pfv, pfb + 1024); }
  if (blockIdx.x == 64 && lw == 3) {
    wait_flags2(FLAG(C_F0 + lane * 4), FLAG(C_F0 + (64 + lane) * 4), tag);
    wait_flags2(FLAG(C_F0 + (128 + lane) * 4), FLAG(C_F0 + (192 + lane) * 4), tag);
    if (lane == 0) st_flag(FLAG(C_E), tag);
  }
  const u64* fab = FLAG(C_FAB + (nt * 64 + split * 16) * 2 + (lane & 31));
  unsigned vY[4];
#pragma unroll
  for (int j = 0; j < 4; ++j) vY[j] = (32 * lw + 8 * j + rr) * 4096 + ((c8 ^ rr) * 16) - j * 1024;
  const char* baseY = (const char*)(split < 2 ? P.wsr : P.wsi) + (size_t)mt2 * 128 * 4096 + (split & 1) * 2048;
  const char* baseAB = P.ws + WS_AB + (nt * 64 + split * 16) * B_ST + lw * 5120;
  const char* baseH = P.ws + WS_H + ((mt2 * 24 + (lw >> 1)) * 16 + (split & 1) * 8 + (lw & 1)) * 1024;
  const unsigned ldsA = ldsX, ldsB = __builtin_amdgcn_readfirstlane(lds0 + L_B + lw * 5120);
  auto issueYA = [&](int k, int yi) { glds4<false>(ldsA + (k & 3) * A_ST, baseY + (yi ^ xg) * 128, vY[0], vY[1], vY[2], vY[3]); };
  auto issueYB = [&](int k, int yi) { glds5<true>(ldsB + (k & 3) * B_ST, baseAB + (yi ^ xg) * B_ST, vL, vL, vL, vL, vL4); };
  BAR();
  __builtin_amdgcn_s_setprio(TPRIO);
  STAMP_AT(8);
  u64 fabv;
  fabv = ~tag;
  if (FABM == 1 || (FABM == 2 && lw == 0)) asm volatile("global_load_dwordx2 %0, %1, off sc1" : "+v"(fabv) : "v"(fab) : "memory");
  issueYA(0, 0); issueYA(1, 1);
  if (FABM) wait_v<8>();
  asm volatile("" : "+v"(fabv));
  const bool ab_seen = __all(fabv == tag);
  if (lw == 0) {
    if (!ab_seen) wait_flags(fab, tag);
    if (lane == 0) *(volatile LAS unsigned*)(smem + T_GO) = 1;
  } else if (!ab_seen) {
#pragma unroll 1
    for (int it = 0; it < 4000000; ++it) {
      if (*(volatile LAS unsigned*)(smem + T_GO) != 0) break;
      __builtin_amdgcn_s_sleep(1);
    }
  }
  asm volatile("" ::: "memory");
  STAMP_AT(9);
  const u64* fh = FLAG(C_FH + mt2 * 64 + (lane & 31) * 2 + (split & 1));
  const int l36 = min(lane, 35);
  const int wt = (nt * 240 + ((l36 / 3) * 4 + lw) * 5 + (split >> 1) * 2 + l36 % 3) >> 1;
  const u64* fw = FLAG(C_FW + wt);
  u64 fhv = 0, fwv = 0;
  issueYB(0, 0);
  if (B0E) { wait_v<0>(); BAR(); issueYB(1, 1); } else { issueYB(1, 1); wait_v<5>(); BAR(); }
  issueYA(2, 2); issueYB(2, 2);
  wait_v<9>(); BAR();
  __builtin_amdgcn_s_setprio(0);
  issueYA(3, 3); issueYB(3, 3); issueYA(4, 4); issueYB(4, 4);
  const int ph = nt * 32 + lane, pw = (mt2 & 7) * 20 + min(max(lane - 32, 0), 19);
  const unsigned dstr = lane < 32 ? 2 * A_ST : 2 * B_ST;
  unsigned dpf = (lane < 32 ? WS_H + ((mt2 * 24 + (ph >> 6)) * 16 + (split & 1) * 8) * 1024 + (ph & 63) * 128
                            : WS_WD + ((nt * 48 + pw / 40) * 10 + (split >> 1) * 5) * 1024 + (pw % 40) * 128) + (DP0 + lw) * dstr;
#pragma unroll 1
  for (int k = 2; k < NY - 3; ++k) {
    if (k == DFK + 1) {
      wait_v<9>();
      asm volatile("" : "+v"(fhv), "+v"(fwv));
      if (!__all(fhv == tag && fwv == tag)) wait_flags2(fh, fw, tag);
    } else wait_v<18>();
    BAR();
    if (k == DFK) asm volatile("global_load_dwordx2 %0, %2, off sc1\n\tglobal_load_dwordx2 %1, %3, off sc1" : "+v"(fhv), "+v"(fwv) : "v"(fh), "v"(fw) : "memory");
    issueYA(k + 3, k + 3); issueYB(k + 3, k + 3);
    if (PFM == 3 && !TG && k == 2 + (lw >> PFQ)) touch_lines(lds0 + T_PF, pfv, pfb + 1024);
    if (DPF) {
      const int n0 = (k - (DFK + 1)) * DPS;
      const int n = n0 + ((lw - n0) & 3);
      if (n0 >= 0 && n < n0 + DPS && DP0 + n < 12) { touch_lines<true>(lds0 + T_PF, dpf, P.ws); dpf += 4 * dstr; }
    }
  }
  bf16x8 GA[4][4], GB[4][5];
  f32x4 dac[4][5];
  auto loadD = [&](int i) {
    const int s = i & 3;
    const char* pa = baseH + (i ^ xg) * (2 * A_ST);
    const char* pb = baseWD + (i ^ xg) * (2 * B_ST);
    asm volatile("global_load_dwordx4 %0, %9, %12 sc1\n\tglobal_load_dwordx4 %4, %9, %11 sc1\n\tglobal_load_dwordx4 %5, %9, %11 offset:2048 sc1\n\t"
                 "global_load_dwordx4 %6, %10, %11 sc1\n\tglobal_load_dwordx4 %7, %10, %11 offset:2048 sc1\n\t"
                 "global_load_dwordx4 %1, %9, %12 offset:1024 sc1\n\tglobal_load_dwordx4 %2, %9, %12 offset:2048 sc1\n\t"
                 "global_load_dwordx4 %3, %9, %12 offset:3072 sc1\n\tglobal_load_dwordx4 %8, %10, %12 sc1"
                 : "=&v"(GB[s][0]), "=&v"(GB[s][1]), "=&v"(GB[s][2]), "=&v"(GB[s][3]), "=&v"(GA[s][0]), "=&v"(GA[s][1]), "=&v"(GA[s][2]), "=&v"(GA[s][3]), "=&v"(GB[s][4])
                 : "v"(vL), "v"(vL4), "s"(pa), "s"(pb) : "memory");
  };
  auto load1 = [&](int i, int n) {
    const int s = i & 3;
    const char* pa = baseH + (i ^ xg) * (2 * A_ST);
    const char* pb = baseWD + (i ^ xg) * (2 * B_ST);
    if (n < 4) asm volatile("global_load_dwordx4 %0, %1, %2 offset:%3 sc1" : "=&v"(GA[s][n]) : "v"(n < 2 ? vL : vL4), "s"(pa), "i"((n & 1) * 2048) : "memory");
    else asm volatile("global_load_dwordx4 %0, %1, %2 offset:%3 sc1" : "=&v"(GB[s][n - 4]) : "v"(n < 8 ? vL : vL4), "s"(pb), "i"(((n - 4) & 3) * 1024) : "memory");
  };
  auto group = [&](auto I) __attribute__((always_inline)) {
    constexpr int i = decltype(I)::v, s = i & 3;
    wait_v<(i == 0 ? 27 : i < 10 ? 18 : (11 - i) * 9)>();
    asm volatile("" : "+v"(GA[s][0]), "+v"(GA[s][1]), "+v"(GA[s][2]), "+v"(GA[s][3]), "+v"(GB[s][0]), "+v"(GB[s][1]), "+v"(GB[s][2]), "+v"(GB[s][3]), "+v"(GB[s][4]));
#pragma unroll
    for (int m = 0; m < 20; ++m) {
      const int r = (m & 4) ? 3 - (m & 3) : (m & 3);
      mf(dac[r][m >> 2], GB[s][m >> 2], GA[s][r], i == 0);
      if (i >= 1 && i + 3 < 12 && (m & 1) == 1 && m < 18) load1(i + 3, m >> 1);
    }
  };
  loadD(0);
  wait_v<27>(); BAR();
  loadD(1);
  wait_v<27>(); BAR();
  loadD(2);
  wait_v<27>(); BAR();
  loadD(3);
  __builtin_amdgcn_s_setprio(DPRIO);
  sfor<0, 12>(group);
  MFMA_DONE();
  after_mfma(dac);
#pragma unroll
  for (int r = 0; r < 4; ++r)
#pragma unroll
    for (int j = 0; j < 5; ++j) *(LAS f32x4*)(smem + f_red(lw) + (r * 5 + j) * 1024 + lane * 16) = dac[r][j];
  WAIT_L0();
  STAMP_AT(11);
  BAR();
  STAMP_AT(10);
}

template <bool TG>
DI void mfma_p1(const Params& P, LAS char* smem, int lane, int w, int mt, int ns, u64& tag) {
  const int n8 = ns & 7, hcol0 = TG ? 128 * n8 : 1024 + 64 * n8;
  const __amdgpu_buffer_rsrc_t rw = __builtin_amdgcn_make_buffer_rsrc(P.ws, 0, WS_END, 0x00020000);
  int q = lane >> 4, rho = lane & 15;
  const int rr = lane >> 3, c8 = lane & 7;
  u32x4 vx[5], vw[5], vm[10];
  constexpr bool DO_R = TG != ((P0M & 1) != 0), DO_W = TG, DO_M = TG != ((P0M & 2) != 0);
  if (DO_R) {
    const char* px = (const char*)P.x + (size_t)(mt * 128 + n8 * 16 + 4 * w + q) * 1280 + rho * 16;
#pragma unroll
    for (int i = 0; i < 5; ++i) vx[i] = *(const u32x4*)(px + i * 256);
  }
  if (DO_W) {
    const char* pw = (const char*)((w >> 1) ? P.wu : P.wg) + (size_t)(40 * mt + rr) * 3072 + (hcol0 + (w & 1) * 64) * 2 + c8 * 16;
#pragma unroll
    for (int i = 0; i < 5; ++i) vw[i] = *(const u32x4*)(pw + i * (8 * 3072));
  }
  if (DO_M) {
    if (w < 2) {
      const char* pm = (const char*)(w ? P.wu : P.wg) + (size_t)(40 * mt + rr) * 3072 + (1024 + 64 * n8) * 2 + c8 * 16;
#pragma unroll
      for (int i = 0; i < 5; ++i) vm[i] = *(const u32x4*)(pm + i * (8 * 3072));
    } else {
      const int e = (n8 & 3) * 160 + 10 * mt + 5 * (w - 2) + min(rr, 4);
      const char* pd = (const char*)(n8 >= 4 ? P.wdi : P.wdr) + (size_t)e * 1280 + c8 * 16;
#pragma unroll
      for (int i = 0; i < 10; ++i) vm[i] = *(const u32x4*)(pd + i * 128);
    }
  }
  tag = read_tag(P.ws);
  const int wm = w & 1, wn = w >> 1;
  constexpr int NC = TG ? 4 : 2;
  unsigned vX0 = TG ? wm * 8192 + rho * 128 + ((q ^ (rho & 7)) * 16)
                          : wm * 8192 + (8 * (rho >> 2) + (rho & 3)) * 128 + ((q ^ ((rho & 2) | ((rho >> 1) & 4))) * 16);
  unsigned vX1 = vX0 ^ 64;
  unsigned vW[NC];
  {
    const int qq = rho >> 2, p = lane & 3, swz = (qq & 2) | ((q & 1) << 2);
#pragma unroll
    for (int c = 0; c < NC; ++c) {
      const int cc = TG ? c : (2 * wn + c);
      vW[c] = L_W + (TG ? wn * 49152 : 0) + (8 * q + qq) * 128 + ((((2 * cc) ^ swz) + (p >> 1)) * 16) + 8 * (p & 1);
    }
  }
  unsigned vD0 = L_D + (80 * wn + rho) * 128 + ((q ^ (rho & 7)) * 16), vD1 = vD0 ^ 64;
  asm volatile("" : "+v"(vX0), "+v"(vX1));
  if (!TG) asm volatile("" : "+v"(vD0), "+v"(vD1));
#pragma unroll
  for (int c = 0; c < NC; ++c) asm volatile("" : "+v"(vW[c]));
  f32x4 aG[NC][4], aU[NC][4], aA[4][5];
  if (DO_R || DO_W || DO_M) {
    unsigned ONE2 = 0x3f803f80u, SLO = 0x00003f80u, SHI = 0x3f800000u;
    asm volatile("" : "+v"(ONE2), "+v"(SLO), "+v"(SHI));
    float rso = 0, c1 = 0, c2 = 0; unsigned off2 = 0;
    if (DO_R) {
      float sx[2] = {0, 0}, sq[2] = {0, 0};
#pragma unroll
      for (int i = 0; i < 5; ++i)
#pragma unroll
        for (int e = 0; e < 4; ++e) { sx[e & 1] = dot2(vx[i][e], ONE2, sx[e & 1]); sq[e & 1] = dot2(vx[i][e], vx[i][e], sq[e & 1]); }
      const float mean = sum16(sx[0] + sx[1]) * (1.0f / D);
      const float var = fmaxf(sum16(sq[0] + sq[1]) * (1.0f / D) - mean * mean, 0.f);
      const float sd = rbf(__builtin_amdgcn_sqrtf(rbf(rbf(var) + 1e-5f)));
      rso = (lane & 1) ? rbf(mean) : __builtin_amdgcn_rcpf(sd);
    }
    auto colsum = [&](const u32x4* v) -> float {
      float c[8] = {0, 0, 0, 0, 0, 0, 0, 0};
#pragma unroll
      for (int i = 0; i < 5; ++i)
#pragma unroll
        for (int e = 0; e < 4; ++e) { c[2 * e] = dot2(v[i][e], SLO, c[2 * e]); c[2 * e + 1] = dot2(v[i][e], SHI, c[2 * e + 1]); }
      float d4[4], d2[2];
#pragma unroll
      for (int e = 0; e < 4; ++e) d4[e] = xchg32(c[e], c[4 + e]);
#pragma unroll
      for (int e = 0; e < 2; ++e) d2[e] = xchg16(d4[e], d4[2 + e]);
      d2[0] = dpp_add<0x128>(d2[0]); d2[1] = dpp_add<0x128>(d2[1]);
      return (lane & 8) ? d2[1] : d2[0];
    };
    if (DO_W) c1 = colsum(vw);
    if (DO_M) {
      if (w < 2) {
        c2 = colsum(vm);
        off2 = WS_CS + ((((8 + n8) * 4 + w) * 16 + mt) * 64 + 8 * c8 + rr) * 4;
      } else {
        float a = 0, b = 0;
#pragma unroll
        for (int i = 0; i < 10; ++i) { a = dot2(vm[i][0], ONE2, a); b = dot2(vm[i][1], ONE2, b); a = dot2(vm[i][2], ONE2, a); b = dot2(vm[i][3], ONE2, b); }
        c2 = sum8(a + b);
        off2 = (c8 == 0 && rr < 5) ? WS_CD + (n8 * 160 + 10 * mt + 5 * (w - 2) + rr) * 4 : 0xfffffff0u;
      }
    }
    asm volatile("" : "+v"(c1), "+v"(c2), "+v"(rso));
    if (DO_W) __builtin_amdgcn_raw_buffer_store_b32(__float_as_uint(c1), rw, WS_CS + (((n8 * 4 + w) * 16 + mt) * 64 + 8 * c8 + rr) * 4, 0, 16);
    if (DO_M) __builtin_amdgcn_raw_buffer_store_b32(__float_as_uint(c2), rw, off2, 0, 16);
    if (DO_R) __builtin_amdgcn_raw_buffer_store_b32(__float_as_uint(rso), rw, rho < 2 ? WS_RS + mt * 1024 + rho * 512 + (n8 * 16 + 4 * w + q) * 4 : 0xfffffff0u, 0, 16);
  }
  bf16x8 FX[2][4], FD[2][5];
  s16x4 FW[2][2 * NC][2];
  constexpr int NR = TG ? 20 : 17, NM = TG ? 32 : 36;
  auto rd1 = [&](int s, int buf, int ks, int r) {
    if (r >= 2 && r < 6) {
      const int i = r - 2;
      FX[s][i] = *(LAS bf16x8*)(smem + (ks ? vX1 : vX0) + buf * 16384 + (TG ? i * 2048 : (i >> 1) * 4096 + (i & 1) * 512));
    } else if (r < 4 + 4 * NC) {
      const int h = r < 2 ? r : r - 4, f = h >> 1;
      FW[s][f][h & 1] = __builtin_amdgcn_ds_read_tr16_b64_v4i16((LAS s16x4*)(smem + vW[f >> 1] + buf * 16384 + ks * 4096 + (f & 1) * 8192 + (h & 1) * 512));
    } else {
      const int jj = r - (4 + 4 * NC);
      FD[s][jj] = *(LAS bf16x8*)(smem + (ks ? vD1 : vD0) + buf * 20480 + jj * 2048);
    }
  };
  auto mm1 = [&](int s, int m, bool first) {
    const int f = m >> 2, i = (f & 1) ? 3 - (m & 3) : (m & 3);
    if (f < 2 * NC) {
      const bf16x8 wf = __builtin_shufflevector(FW[s][f][0], FW[s][f][1], 0, 1, 2, 3, 4, 5, 6, 7);
      mf((f & 1) ? aU[f >> 1][i] : aG[f >> 1][i], wf, FX[s][i], first);
    } else mf(aA[i][f - 2 * NC], FX[s][i], FD[s][f - 2 * NC], first);
  };
  auto seg = [&](int s, bool first, int m0, int m1, int rk, int buf, int ks) __attribute__((always_inline)) {
    int r = 0;
#pragma unroll
    for (int m = m0; m < m1; ++m) {
      mm1(s, m, first);
      if (rk == 2 && r == 0) r = 2;
      if (rk == 2 && r == 6) r = 4 + 4 * NC;
      const int rend = rk == 0 ? 0 : rk == 3 ? 4 + 4 * NC : NR;
      if (r < rend && (TG ? (m % 5 != 4) : (rk == 3 || m % 3 != 2))) { rd1(s ^ 1, buf, ks, r); ++r; }
      PIN();
    }
  };
  BAR();
  STAMP_AT(1);
#pragma unroll
  for (int r = 0; r < NR; ++r) rd1(0, 0, 0, r);
  PIN();
  constexpr int KJ = TG ? 10 : 7;
  seg(0, true, 0, NM, 1, 0, 1);
  WAIT_L0(); BAR(); PIN();
  seg(1, false, 0, NM, 1, 1, 0);
  wait_v<0>();
  if (lane == 0) st_flag(FLAG(C_F0 + (mt * 16 + ns) * 4 + w), tag);
  WAIT_L0(); PIN();
#pragma unroll 1
  for (int it = 0; it < (KJ - 1) / 3; ++it) {
    sfor<1, 4>([&](auto K) __attribute__((always_inline)) {
      constexpr int k = decltype(K)::v;
      seg(0, false, 0, NM, 1, k % 3, 1);
      WAIT_L0();
      if (!TG || k < 3 || it < 2) BAR();
      PIN();
      seg(1, false, 0, NM, 1, (k + 1) % 3, 0);
      WAIT_L0(); PIN();
    });
  }
  if (!TG) sfor<7, 10>([&](auto K) __attribute__((always_inline)) {
    constexpr int k = decltype(K)::v;
    seg(0, false, 8 * NC, NM, 2, k % 3, 1);
    if (k < 9) { WAIT_L0(); BAR(); PIN(); }
    seg(1, false, 8 * NC, NM, k < 9 ? 2 : 3, (k + 1) % 3, 0);
    WAIT_L0(); PIN();
  });
  if (!TG) {
    MFMA_DONE();
    after_mfma(aA);
    asm volatile("" : "+v"(lane)); q = lane >> 4; rho = lane & 15;
    STAMP_AT(4);
    const float sgn = (n8 >= 4) ? -1.0f : 1.0f;
    float cs[5];
#pragma unroll
    for (int j = 0; j < 5; ++j) cs[j] = *(LAS float*)(smem + T_CD + (80 * wn + 16 * j + rho) * 4);
    const int kt = (n8 >= 4 ? 32 : 0) + mt * 2 + wm;
#pragma unroll
    for (int I = 0; I < 2; ++I) {
      f32x4 r4[2], m4[2];
#pragma unroll
      for (int t = 0; t < 2; ++t) {
        const int row = 64 * wm + 32 * I + 8 * q + 4 * t;
        r4[t] = *(LAS f32x4*)(smem + T_RS + row * 4) * sgn; m4[t] = *(LAS f32x4*)(smem + T_RM + row * 4);
      }
      const unsigned dst = WS_AB + ((((n8 & 3) * 64 + kt) * 2 + I) * 10 + 5 * wn) * 1024 + lane * 16;
#pragma unroll
      for (int j = 0; j < 5; ++j) { aA[2 * I][j] -= m4[0] * cs[j]; aA[2 * I + 1][j] -= m4[1] * cs[j]; }
      PIN();
#pragma unroll
      for (int j = 0; j < 5; ++j) { aA[2 * I][j] *= r4[0]; aA[2 * I + 1][j] *= r4[1]; }
      PIN();
#pragma unroll
      for (int j = 0; j < 5; ++j) {
        const f32x4 v0 = aA[2 * I][j], v1 = aA[2 * I + 1][j];
        const u32x4 o = {pk_bf16(v0[0], v0[1]), pk_bf16(v0[2], v0[3]), pk_bf16(v1[0], v1[1]), pk_bf16(v1[2], v1[3])};
        st16c(rw, dst + j * 1024, o);
      }
    }
    STAMP_AT(7);
    WAIT_L0(); PIN();
#pragma unroll
    for (int k = KJ; k < 10; ++k) {
      seg(0, false, 0, 8 * NC, 3, k % 3, 1);
      WAIT_L0(); PIN();
      if (k == KJ + AFL) {
        wait_v<0>();
        if (lane == 0) st_flag(FLAG(C_FAB + ((n8 & 3) * 64 + kt) * 2 + wn), tag);
        PIN();
      }
      seg(1, false, 0, 8 * NC, k < 9 ? 3 : 0, (k + 1) % 3, 0);
      WAIT_L0(); PIN();
    }
  }
  MFMA_DONE();
  after_mfma(aG); after_mfma(aU);
  asm volatile("" : "+v"(lane)); q = lane >> 4; rho = lane & 15;
  BAR();
  STAMP_AT(2);
  {
    const float K0 = -2.0f * 0.7978845608028654f * 1.4426950408889634f, K1 = K0 * 0.044715f;
    float mu[4], c0[4], c1[4], v2[4];
#pragma unroll
    for (int i = 0; i < 4; ++i) {
      const int row = TG ? 64 * wm + 16 * i + rho : 64 * wm + 32 * (i >> 1) + 8 * (rho >> 2) + 4 * (i & 1) + (rho & 3);
      const float rs = *(LAS float*)(smem + T_RS + row * 4);
      mu[i] = *(LAS float*)(smem + T_RM + row * 4);
      c0[i] = K0 * rs; c1[i] = K1 * rs * rs * rs; v2[i] = __builtin_amdgcn_rcpf(rs * rs);
    }
#pragma unroll
    for (int J = 0; J < NC / 2; ++J) {
      f32x4 cg[2], cu[2];
#pragma unroll
      for (int t = 0; t < 2; ++t) {
        const int c = 2 * J + t;
        const int o = TG ? (wn * 256 + c * 64 + q * 16) : ((2 * wn + c) * 64 + q * 16);
        cg[t] = *(LAS f32x4*)(smem + T_CS + o);
        cu[t] = *(LAS f32x4*)(smem + T_CS + (TG ? 512 : 256) + o);
      }
#pragma unroll
      for (int ip = 0; ip < 2; ++ip) {
        f32x4 g[4], z[4], p[4];
#define EACH(stmt) _Pragma("unroll") for (int n = 0; n < 4; ++n) { const int i = 2 * ip + (n >> 1), t = n & 1; (void)i; (void)t; stmt; } PIN()
        EACH(g[n] = aG[2 * J + t][i] - cg[t] * mu[i]);
        EACH(z[n] = g[n] * g[n]);
        EACH(z[n] = z[n] * c1[i] + c0[i]);
        EACH(z[n] = z[n] * g[n]);
        EACH(p[n] = aU[2 * J + t][i] - cu[t] * mu[i]; z[n][0] = __builtin_amdgcn_exp2f(z[n][0]); z[n][1] = __builtin_amdgcn_exp2f(z[n][1]);
             z[n][2] = __builtin_amdgcn_exp2f(z[n][2]); z[n][3] = __builtin_amdgcn_exp2f(z[n][3]));
        EACH(z[n] = z[n] * v2[i] + v2[i]);
        EACH(p[n] = p[n] * g[n]; z[n][0] = __builtin_amdgcn_rcpf(z[n][0]); z[n][1] = __builtin_amdgcn_rcpf(z[n][1]);
             z[n][2] = __builtin_amdgcn_rcpf(z[n][2]); z[n][3] = __builtin_amdgcn_rcpf(z[n][3]));
        EACH(p[n] = p[n] * z[n]);
#undef EACH
#pragma unroll
        for (int u = 0; u < 2; ++u) {
          const int i = 2 * ip + u;
          const u32x4 o = {pk_bf16(p[2 * u][0], p[2 * u][1]), pk_bf16(p[2 * u][2], p[2 * u][3]), pk_bf16(p[2 * u + 1][0], p[2 * u + 1][1]), pk_bf16(p[2 * u + 1][2], p[2 * u + 1][3])};
          const int f0 = hcol0 + (TG ? wn * 64 + J * 32 : wn * 32);
          const int rb = TG ? 4 * wm + i : 4 * wm + 2 * (i >> 1) + (rho >> 3), pos = TG ? lane : 8 * ((rho >> 2) & 1) + 4 * (i & 1) + (rho & 3) + 16 * q;
          st16c(rw, WS_H + (((mt * 24 + (f0 >> 6)) * 8 + rb) * 2 + ((f0 >> 5) & 1)) * 1024 + pos * 16, o);
        }
      }
    }
  }
  STAMP_AT(3);
}

DI void mfma_p2(const Params& P, LAS char* smem, int lane, int w, int split, int mt, int nt, int mt1, int ns, u64 tag) {
  asm volatile("" : "+v"(lane));
  const int wm = w & 1, wn = w >> 1, q = lane >> 4, r15 = lane & 15;
  unsigned vY0 = wm * 8192 + r15 * 128 + ((q ^ (r15 & 7)) * 16), vY1 = vY0 ^ 64;
  unsigned vB0 = L_B + wn * 5120 + lane * 16, vB2 = vB0 + 2 * B_ST;
  asm volatile("" : "+v"(vY0), "+v"(vY1), "+v"(vB0), "+v"(vB2));
  const __amdgpu_buffer_rsrc_t rw = __builtin_amdgcn_make_buffer_rsrc(P.ws, 0, WS_END, 0x00020000);
  const int tile = mt * 4 + nt;
  f32x4 acc[4][5];
  bf16x8 FA[2][4], FB[2][5];
  auto rdy = [&](int s, int k, int ks, int r) {
    const int buf = k & 3;
    if (r >= 1 && r < 5) FA[s][r - 1] = *(LAS bf16x8*)(smem + (ks ? vY1 : vY0) + buf * A_ST + (r - 1) * 2048);
    else { const int j = r ? r - 4 : 0; FB[s][j] = *(LAS bf16x8*)(smem + (buf >= 2 ? vB2 : vB0) + (buf & 1) * B_ST + ks * 10240 + j * 1024); }
  };
  auto seg = [&](auto K, auto HF) __attribute__((always_inline)) {
    constexpr int k = decltype(K)::v, hf = decltype(HF)::v, rk = k + hf, rh = hf ^ 1;
    int r = 0;
#pragma unroll
    for (int m = 0; m < 20; ++m) {
      const int i = (m & 4) ? 3 - (m & 3) : (m & 3);
      mf(acc[i][m >> 2], FB[hf][m >> 2], FA[hf][i], k == 0 && hf == 0);
      if (rk < NY && r < 9 && m % 3 != 2) { rdy(rh, rk, rh, r); ++r; }
      PIN();
    }
  };
  auto publish = [&](int ip) {
    u32x4 o[5];
#pragma unroll
    for (int t = 0; t < 2; ++t) {
      const int i = 2 * ip + t;
#pragma unroll
      for (int j = 0; j < 5; ++j) {
        const int n = j < 4 ? 2 * t + (j >> 1) : 4, e = j < 4 ? (j & 1) * 2 : 2 * t;
        o[n][e] = pk_bf16(acc[i][j][0], acc[i][j][1]); o[n][e + 1] = pk_bf16(acc[i][j][2], acc[i][j][3]);
      }
    }
    if (w == split) {
#pragma unroll
      for (int n = 0; n < 5; ++n) *(LAS u32x4*)(smem + F_Y + (n < 4 ? ip * 4096 + n * 1024 : 8192 + ip * 1024) + lane * 16) = o[n];
    } else {
      const unsigned dst = WS_P + ((tile * 4 + w) * 3 + (split ^ w) - 1) * 10240 + lane * 16;
#pragma unroll
      for (int n = 0; n < 5; ++n) if (!NOPUB) st16c(rw, dst + (n < 4 ? ip * 4096 + n * 1024 : 8192 + ip * 1024), o[n]);
    }
  };
#pragma unroll
  for (int i = 0; i < 4; ++i)
#pragma unroll
    for (int j = 0; j < 5; ++j) acc[i][j] = (f32x4){0, 0, 0, 0};
  after_mfma(acc);
  BAR();
  STAMP_AT(5);
#pragma unroll
  for (int r = 0; r < 9; ++r) rdy(0, 0, 0, r);
  WAIT_L0(); PIN();
#pragma unroll 1
  for (int it = 0; it < NY / 4; ++it) {
    sfor<4, 8>([&](auto K) __attribute__((always_inline)) {
      constexpr int k = decltype(K)::v;
      seg(K, IC<0>{});
      if (k == 4 + HFL && it == 0) {
        wait_v<0>();
        if (lane == 0) st_flag(FLAG(C_FH + mt1 * 64 + ns * 4 + w), tag);
      }
      WAIT_L0();
      if (k < 7 || it < NY / 4 - 1) BAR();
      PIN();
      seg(K, IC<1>{});
      WAIT_L0(); PIN();
    });
  }
  MFMA_DONE();
  after_mfma(acc);
  __builtin_amdgcn_s_setprio(3);
  publish(0); publish(1);
  if (HOLD) BAR();
  if (w != split) {
    wait_v<0>();
    if (lane == 0) st_flag(FLAG(C_FP + (tile * 4 + w) * 4 + (split ^ w) - 1), tag);
  }
  const int orow = mt * 128 + (split & 1) * 64 + w * 16, ocol = nt * 160 + (split >> 1) * 80;
  u32x4 xv[3];
  unsigned tr[3], oo[3];
#pragma unroll
  for (int i = 0; i < 3; ++i) {
    const int n = min(i * 64 + lane, 159);
    const unsigned off = ((orow + n / 10) * D + ocol + (n % 10) * 8) * 2;
    xv[i] = *(const u32x4*)((const char*)P.x + off);
    tr[i] = F_T + w * (16 * FPITCH) + (n / 10) * FPITCH + (n % 10) * 16;
    oo[i] = i * 64 + lane < 160 ? off : 0xfffffff0u;
  }
  if (w == split) {
    const u64* fp = FLAG(C_FP + (tile * 4 + split) * 4 + min(lane, 2));
    __builtin_amdgcn_s_sleep(PD0);
#pragma unroll 1
    for (int it = 0; it < 40000; ++it) {
      if (__all(ld_flag(fp) == tag)) break;
      __builtin_amdgcn_s_sleep(PD1);
    }
    WAIT_L0();
    if (lane == 0) *(volatile LAS unsigned*)(smem + T_GO) = 2;
  } else {
    __builtin_amdgcn_s_setprio(0);
#pragma unroll 1
    for (int it = 0; it < 4000000; ++it) {
      if (*(volatile LAS unsigned*)(smem + T_GO) == 2) break;
      __builtin_amdgcn_s_sleep(4);
    }
    __builtin_amdgcn_s_setprio(3);
  }
  asm volatile("" ::: "memory");
  f32x4 ys[5];
  {
    u32x4 pa[3][2]; u32x2 pb[3];
#pragma unroll
    for (int s = 0; s < 3; ++s) {
      const unsigned src = WS_P + ((tile * 4 + split) * 3 + s) * 10240 + lane * 16;
      pa[s][0] = ld16c(rw, src + w * 2048); pa[s][1] = ld16c(rw, src + w * 2048 + 1024);
      pb[s] = __builtin_amdgcn_raw_buffer_load_b64(rw, src + 8192 + (w >> 1) * 1024 + (w & 1) * 8, 0, 16);
    }
#pragma unroll
    for (int j = 0; j < 5; ++j) {
#pragma unroll
      for (int s = 0; s < 3; ++s) {
        const unsigned lo = j < 4 ? pa[s][j >> 1][(j & 1) * 2] : pb[s][0], hi = j < 4 ? pa[s][j >> 1][(j & 1) * 2 + 1] : pb[s][1];
        const f32x4 v = {bflo(lo), bfhi(lo), bflo(hi), bfhi(hi)};
        ys[j] = s ? ys[j] + v : v;
      }
      asm volatile("" : "+v"(ys[j]));
    }
  }
  {
    LAS char* po = smem + F_Y + lane * 16;
    const u32x4 oa0 = *(LAS u32x4*)(po + w * 2048), oa1 = *(LAS u32x4*)(po + w * 2048 + 1024);
    const u32x2 ob = *(LAS u32x2*)(po + 8192 + (w >> 1) * 1024 + (w & 1) * 8);
#pragma unroll
    for (int j = 0; j < 5; ++j) {
      const unsigned l0 = j < 2 ? oa0[(j & 1) * 2] : j < 4 ? oa1[(j & 1) * 2] : ob[0], h0 = j < 2 ? oa0[(j & 1) * 2 + 1] : j < 4 ? oa1[(j & 1) * 2 + 1] : ob[1];
      ys[j] += (f32x4){bflo(l0), bfhi(l0), bflo(h0), bfhi(h0)};
      asm volatile("" : "+v"(ys[j]));
    }
  }
  unsigned tw = F_T + w * (16 * FPITCH) + r15 * FPITCH + q * 8, rd0 = w * 5120 + lane * 16, rd2 = rd0 + L_B;
  asm volatile("" : "+v"(tw), "+v"(rd0), "+v"(rd2), "+v"(tr[0]), "+v"(tr[1]), "+v"(tr[2]), "+v"(oo[0]), "+v"(oo[1]), "+v"(oo[2]));
  STAMP_AT(6);
  BAR();
  f32x4 dd[4][5];
#pragma unroll
  for (int j = 0; j < 5; ++j)
#pragma unroll
    for (int u = 0; u < 4; ++u) dd[u][j] = *(LAS f32x4*)(smem + ((u & 2) ? rd2 : rd0) + (u & 1) * 20480 + j * 1024);
#pragma unroll
  for (int j = 0; j < 5; ++j) {
    const f32x4 d = (dd[0][j] + dd[1][j]) + (dd[2][j] + dd[3][j]);
    f32x4 e = d * -1.4426950408889634f;
#pragma unroll
    for (int r = 0; r < 4; ++r) e[r] = __builtin_amdgcn_exp2f(e[r]);
    e = e + 1.0f;
#pragma unroll
    for (int r = 0; r < 4; ++r) e[r] = __builtin_amdgcn_rcpf(e[r]);
    e = e * d * ys[j];
    *(LAS u32x2*)(smem + tw + j * 32) = (u32x2){pk_bf16(e[0], e[1]), pk_bf16(e[2], e[3])};
  }
  WAIT_L0();
  const __amdgpu_buffer_rsrc_t ro = __builtin_amdgcn_make_buffer_rsrc(P.out, 0, S * D * 2, 0x00020000);
#pragma unroll
  for (int i = 0; i < 3; ++i) {
    const u32x4 v = *(LAS u32x4*)(smem + tr[i]);
    u32x4 o;
#pragma unroll
    for (int d = 0; d < 4; ++d) o[d] = pk_bf16(bflo(v[d]) + bflo(xv[i][d]), bfhi(v[d]) + bfhi(xv[i][d]));
    __builtin_amdgcn_raw_buffer_store_b128(o, ro, oo[i], 0, 0);
  }
}

__global__ __launch_bounds__(512, 2)
void fused(Params P) {
  extern __shared__ __attribute__((aligned(16))) char smem_[];
  LAS char* smem = (LAS char*)smem_;
  const int tid = threadIdx.x, lane = tid & 63, w = __builtin_amdgcn_readfirstlane(tid >> 6);
  const int bid = blockIdx.x, xcd = bid & 7, idx = bid >> 3;
  u64 tag;
  STAMP_AT(0);
  if (CPF && w == 3 && (idx & 8)) {
    u64 pc; unsigned len;
    asm volatile("s_getpc_b64 %0\n.Lcodepf:\n\ts_mov_b32 %1, code_end_marker-.Lcodepf" : "=s"(pc), "=s"(len));
    const unsigned off = ((idx & 7) * 64 + lane) * (CPF == 2 ? 64 : 128) + (CPF == 2 ? (idx >> 4) * 32768 : 0);
    if (off + 4 <= len) touch_lines((unsigned)(size_t)smem + T_PF, off, (const char*)pc);
  }
  const int mt1 = (xcd & 1) * 8 + (idx & 7), j = idx >> 3, ns = (j & 1) * 8 + (xcd >> 1) * 2 + (j >> 1);
  const int split = xcd >> 1, mt2 = (xcd & 1) * 8 + (idx >> 2), nt = idx & 3;
  if (w >= 4) {
    __builtin_amdgcn_s_setprio(HPRIO);
    helper(P, smem, lane, w & 3, mt1, ns, split, mt2, nt);
  } else {
    if (j & 1) mfma_p1<false>(P, smem, lane, w, mt1, ns, tag); else mfma_p1<true>(P, smem, lane, w, mt1, ns, tag);
    mfma_p2(P, smem, lane, w, split, mt2, nt, mt1, ns, tag);
    __builtin_amdgcn_s_waitcnt(0);
  }
}

extern "C" __global__ void code_end_marker() {}
extern "C" void kernel_launch(void* const* d_in, const int* in_sizes, int n_in, void* d_out, int out_size, void* d_ws, size_t ws_size, hipStream_t stream) {
  Params P;
  P.x = (const bf16_t*)d_in[0]; P.wsr = (const bf16_t*)d_in[1]; P.wsi = (const bf16_t*)d_in[2];
  P.wdr = (const bf16_t*)d_in[3]; P.wdi = (const bf16_t*)d_in[4];
  P.wg = (const bf16_t*)d_in[6]; P.wu = (const bf16_t*)d_in[7]; P.wdn = (const bf16_t*)d_in[8];
  P.out = (bf16_t*)d_out; P.ws = (char*)d_ws;
  (void)hipFuncSetAttribute((const void*)fused, hipFuncAttributeMaxDynamicSharedMemorySize, LDS_BYTES);
  fused<<<256, 512, LDS_BYTES, stream>>>(P);
}
```

```cpp
#include <hip/hip_runtime.h>
#include <hip/hip_bf16.h>
typedef unsigned short bf16_t;
typedef unsigned long long u64;
typedef short bf16x8 __attribute__((ext_vector_type(8)));
typedef short s16x4 __attribute__((ext_vector_type(4)));
typedef float f32x4 __attribute__((ext_vector_type(4)));
typedef float f32x2 __attribute__((ext_vector_type(2)));
typedef unsigned u32x4 __attribute__((ext_vector_type(4)));
typedef unsigned u32x2 __attribute__((ext_vector_type(2)));
typedef __bf16 bf16x2_t __attribute__((ext_vector_type(2)));
#define LAS __attribute__((address_space(3)))
#define GAS __attribute__((address_space(1)))
#define DI __device__ __forceinline__

constexpr int S = 2048, D = 640, F = 1536;
constexpr unsigned WS_AB = 0, WS_H = WS_AB + 2 * S * D * 2, WS_P = WS_H + S * F * 2, WS_WD = WS_P + 3 * S * D * 2, WS_RS = WS_WD + F * D * 2,
                   WS_CS = WS_RS + S * 8, WS_CD = WS_CS + 16 * 4 * 16 * 64 * 4, WS_CTRL = WS_CD + 8192, WS_END = WS_CTRL + 5120 * 128;
constexpr int C_E = 0, C_F0 = 1, C_FAB = C_F0 + 1024, C_FH = C_FAB + 512, C_FW = C_FH + 1024, C_FP = C_FW + 512;
#define FLAG(i) ((u64*)(P.ws + WS_CTRL) + (i) * 16)

DI unsigned pk_bf16(float lo, float hi) { f32x2 v = {lo, hi}; return __builtin_bit_cast(unsigned, __builtin_convertvector(v, bf16x2_t)); }
DI float rbf(float f) { return __uint_as_float(pk_bf16(f, f) << 16); }
DI float bflo(unsigned u) { return __uint_as_float(u << 16); }
DI float bfhi(unsigned u) { return __uint_as_float(u & 0xffff0000u); }
DI float dot2(unsigned a, unsigned b, float c) { return __builtin_amdgcn_fdot2_f32_bf16(__builtin_bit_cast(bf16x2_t, a), __builtin_bit_cast(bf16x2_t, b), c, false); }
#define GLDS4(POL) asm volatile("s_mov_b32 m0, %0\n\ts_nop 0\n\tglobal_load_lds_dwordx4 %2, %1" POL "\n\tglobal_load_lds_dwordx4 %3, %1 offset:1024" POL \
    "\n\tglobal_load_lds_dwordx4 %4, %1 offset:2048" POL "\n\tglobal_load_lds_dwordx4 %5, %1 offset:3072" POL \
    :: "s"(lds), "s"(base), "v"(v0), "v"(v1), "v"(v2), "v"(v3) : "memory")
#define GLDS5(POL) asm volatile("s_mov_b32 m0, %0\n\ts_nop 0\n\tglobal_load_lds_dwordx4 %2, %1" POL "\n\tglobal_load_lds_dwordx4 %3, %1 offset:1024" POL \
    "\n\tglobal_load_lds_dwordx4 %4, %1 offset:2048" POL "\n\tglobal_load_lds_dwordx4 %5, %1 offset:3072" POL \
    "\n\ts_add_u32 m0, %0, 0x1000\n\ts_nop 0\n\tglobal_load_lds_dwordx4 %6, %1" POL \
    :: "s"(lds), "s"(base), "v"(v0), "v"(v1), "v"(v2), "v"(v3), "v"(v4) : "memory", "scc")
template <bool DEV> DI void glds4(unsigned lds, const char* base, unsigned v0, unsigned v1, unsigned v2, unsigned v3) {
  if (DEV) GLDS4(" sc1"); else GLDS4("");
}
template <bool DEV> DI void glds5(unsigned lds, const char* base, unsigned v0, unsigned v1, unsigned v2, unsigned v3, unsigned v4) {
  if (DEV) GLDS5(" sc1"); else GLDS5("");
}
DI void touch_lines(unsigned lds, unsigned voff, const char* base) {
  asm volatile("s_mov_b32 m0, %0\n\ts_nop 0\n\tglobal_load_lds_dword %1, %2" :: "s"(lds), "v"(voff), "s"(base) : "memory");
}
template <int N> DI void wait_v() { asm volatile("s_waitcnt vmcnt(%0)" :: "i"(N) : "memory"); }
#define WAIT_L0() do { __builtin_amdgcn_s_waitcnt(0xC07F); asm volatile("" ::: "memory"); } while (0)
#define BAR() do { asm volatile("" ::: "memory"); __builtin_amdgcn_s_barrier(); asm volatile("" ::: "memory"); } while (0)
#define PIN() __builtin_amdgcn_sched_barrier(0)
template <int V> struct IC { static constexpr int v = V; };
template <int I, int N, class Fn> DI void sfor(Fn&& f) { if constexpr (I < N) { f(IC<I>{}); sfor<I + 1, N>(f); } }
DI void mf(f32x4& c, const bf16x8& a, const bf16x8& b, bool first) {
  if (first) asm volatile("v_mfma_f32_16x16x32_bf16 %0, %1, %2, 0" : "=&v"(c) : "v"(a), "v"(b));
  else asm volatile("v_mfma_f32_16x16x32_bf16 %0, %1, %2, %0" : "+v"(c) : "v"(a), "v"(b));
}
#define MFMA_DONE() asm volatile("s_nop 15")
template <int A, int B> DI void after_mfma(f32x4 (&c)[A][B]) {
#pragma unroll
  for (int i = 0; i < A; ++i)
#pragma unroll
    for (int j = 0; j < B; ++j) asm volatile("" : "+v"(c[i][j]));
}

template <int CTRL> DI float dpp_add(float v) { return v + __builtin_bit_cast(float, __builtin_amdgcn_update_dpp(0, __builtin_bit_cast(int, v), CTRL, 0xf, 0xf, true)); }
DI float sum8(float v) { v = dpp_add<0xB1>(v); v = dpp_add<0x4E>(v); return dpp_add<0x141>(v); }
DI float sum16(float v) { return dpp_add<0x140>(sum8(v)); }
DI float xchg16(float keep_lo, float keep_hi) {
  const auto r = __builtin_amdgcn_permlane16_swap(__float_as_uint(keep_lo), __float_as_uint(keep_hi), false, false);
  return __uint_as_float(r[0]) + __uint_as_float(r[1]);
}
DI float xchg32(float keep_lo, float keep_hi) {
  const auto r = __builtin_amdgcn_permlane32_swap(__float_as_uint(keep_lo), __float_as_uint(keep_hi), false, false);
  return __uint_as_float(r[0]) + __uint_as_float(r[1]);
}
DI u64 ld_flag(const u64* p) { return __hip_atomic_load((const GAS u64*)p, __ATOMIC_RELAXED, __HIP_MEMORY_SCOPE_AGENT); }
DI void st_flag(u64* p, u64 v) { __hip_atomic_store((GAS u64*)p, v, __ATOMIC_RELAXED, __HIP_MEMORY_SCOPE_AGENT); }
DI void wait_flags(const u64* p, u64 tag) {
#pragma unroll 1
  for (int it = 0; it < 60000; ++it) {
    if (__all(ld_flag(p) == tag)) break;
    __builtin_amdgcn_s_sleep(4);
  }
  asm volatile("" ::: "memory");
}
DI void wait_flags2(const u64* p, const u64* p2, u64 tag) {
#pragma unroll 1
  for (int it = 0; it < 60000; ++it) {
    const u64 a = ld_flag(p), b = ld_flag(p2);
    if (__all(a == tag && b == tag)) break;
    __builtin_amdgcn_s_sleep(4);
  }
  asm volatile("" ::: "memory");
}
DI u64 read_tag(const char* ws) {
  u64 e;
  asm volatile("s_load_dwordx2 %0, %1, 0x0\n\ts_waitcnt lgkmcnt(0)" : "=s"(e) : "s"(ws + WS_CTRL) : "memory");
  return e * 6364136223846793005ull + 1442695040888963407ull;
}
DI void st16c(__amdgpu_buffer_rsrc_t r, unsigned off, u32x4 v) { __builtin_amdgcn_raw_buffer_store_b128(v, r, off, 0, 16); }
DI u32x4 ld16c(__amdgpu_buffer_rsrc_t r, unsigned off) { return __builtin_amdgcn_raw_buffer_load_b128(r, off, 0, 16); }

struct Params {
  const bf16_t *x, *wsr, *wsi, *wdr, *wdi, *wg, *wu, *wdn;
  bf16_t* out; char* ws;
};

constexpr int L_W = 49152, L_D = 98304, K1_TAB = 159744, LDS_BYTES = K1_TAB + 4096;
constexpr int T_RS = K1_TAB, T_RM = K1_TAB + 512, T_CS = K1_TAB + 1024, T_CD = K1_TAB + 2048, T_GO = K1_TAB + 3584, T_PF = K1_TAB + 3072;
constexpr int A_ST = 16384, B_ST = 20480, L_B = 4 * A_ST;

constexpr int NY = 16;
constexpr int F_Y = 147456, F_T = 106496, FPITCH = 176;

DI int f_red(int lw) { return (lw >> 1) * L_B + (lw & 1) * 20480; }

DI void helper(const Params& P, LAS char* smem, int lane, int lw, int mt, int ns, int split, int mt2, int nt) {
  const bool TG = ns < 8;
  const int n8 = ns & 7, hcol0 = TG ? 128 * n8 : 1024 + 64 * n8;
  const int rr = lane >> 3, c8 = lane & 7;
  const unsigned lds0 = (unsigned)(size_t)smem;
  unsigned vX[4], vW[4], vD[5];
#pragma unroll
  for (int j = 0; j < 4; ++j) vX[j] = (32 * lw + 8 * j + rr) * 1280 + ((c8 ^ (TG ? rr : ((rr & 2) | ((j & 2) << 1)))) * 16) - j * 1024;
  const char* bx = (const char*)P.x + (size_t)mt * 128 * 1280;
  const unsigned ldsX = __builtin_amdgcn_readfirstlane(lds0 + lw * 4096);
  glds4<false>(ldsX, bx, vX[0], vX[1], vX[2], vX[3]);
  PIN();
#pragma unroll
  for (int j = 0; j < 4; ++j) vW[j] = (8 * j + rr) * 3072 + ((c8 ^ ((rr & 2) | ((j & 1) << 2))) * 16) - j * 1024;
#pragma unroll
  for (int j = 0; j < 5; ++j) vD[j] = (40 * lw + 8 * j + rr) * 1280 + ((c8 ^ rr) * 16) - (j & 3) * 1024;
  const char* bw = (const char*)((lw >> 1) ? P.wu : P.wg) + hcol0 * 2 + (TG ? (lw & 1) * 128 : (lw & 1) * (32 * 3072));
  const char* bd = (const char*)(n8 >= 4 ? P.wdi : P.wdr) + (size_t)(n8 & 3) * 160 * 1280;
  const unsigned ldsW = __builtin_amdgcn_readfirstlane(lds0 + L_W + (TG ? (lw & 1) * 49152 + (lw >> 1) * 8192 : lw * 4096));
  const unsigned ldsD = __builtin_amdgcn_readfirstlane(lds0 + L_D + lw * 5120);
  auto issue_a = [&](int kt) {
    const int buf = kt % 3;
    if (kt) glds4<false>(ldsX + buf * 16384, bx + kt * 128, vX[0], vX[1], vX[2], vX[3]);
    glds4<false>(ldsW + buf * 16384, bw + kt * (64 * 3072), vW[0], vW[1], vW[2], vW[3]);
  };
  auto issue_b = [&](int kt) {
    const int buf = kt % 3;
    if (TG) glds4<false>(ldsW + buf * 16384 + 4096, bw + kt * (64 * 3072) + 32 * 3072, vW[0], vW[1], vW[2], vW[3]);
    else glds5<false>(ldsD + buf * 20480, bd + kt * 128, vD[0], vD[1], vD[2], vD[3], vD[4]);
  };
  auto issue = [&](int kt) { issue_a(kt); issue_b(kt); };
  issue(0);
  issue_a(1);
  u64 tag;
  const char* pfb = (const char*)(split < 2 ? P.wsr : P.wsi) + (size_t)(mt2 * 128 + nt * 32) * 4096 + (split & 1) * 2048;
  const unsigned pfv = (lane & 31) * 4096 + (lw * 2 + (lane >> 5)) * 128;
  const int fcol = ((lane & 15) * 16 + n8) * 4 + lw;
  const u64* fa = FLAG(C_F0 + ((TG && (lw > 0 || lane >= 32)) ? fcol : lw == 0 ? mt * 64 + (lane & 31) : (((lane >> 1) & 15) * 16 + n8) * 4 + 2 + (lane & 1)));
  u64 fav = 0;
  u32x4 cs[4];
#pragma unroll
  for (int e = 0; e < 4; ++e) asm volatile("" : "=v"(cs[e]));
  auto put_cs = [&]() {
    f32x4 c = (__builtin_bit_cast(f32x4, cs[0]) + __builtin_bit_cast(f32x4, cs[1])) + (__builtin_bit_cast(f32x4, cs[2]) + __builtin_bit_cast(f32x4, cs[3]));
#pragma unroll
    for (int e = 0; e < 4; ++e) { c[e] = xchg16(c[e], c[e]); c[e] = xchg32(c[e], c[e]); }
    if (lane < 16) *(LAS f32x4*)(smem + T_CS + lw * 256 + lane * 16) = c;
  };
  const int xg = ((mt2 ^ nt) & 1) * 2;
  const unsigned vL = lane * 16, vL4 = vL + 4096;
  const char* baseWD = P.ws + WS_WD + ((nt * 48 + lw) * 10 + (split >> 1) * 5) * 1024;
  const int wtask = (mt * 8 + n8) * 4 + lw;
  const bool wdo = TG && wtask < 480;
  unsigned wv[16];
#pragma unroll
  for (int e = 0; e < 16; ++e) asm volatile("" : "=v"(wv[e]));
  constexpr int SK = 5, WK = 2;
  sfor<0, 8>([&](auto K) __attribute__((always_inline)) {
    constexpr int k = decltype(K)::v;
    if (k < 2) wait_v<8>(); else if (k == WK + 1 && wdo) wait_v<12 + 16>(); else if (TG) wait_v<12>(); else wait_v<13>();
    BAR();
    if (k < 2) issue_b(k + 1);
    if (TG || lw < 2) {
      if (k == SK) asm volatile("global_load_dwordx2 %0, %1, off sc1" : "+v"(fav) : "v"(fa) : "memory");
      if (k == SK + 1) {
        asm volatile("" : "+v"(fav));
        if (!__all(fav == tag)) wait_flags(fa, tag);
        if (lw < (TG ? 1 : 2)) {
          const char* src = P.ws + (lw == 0 ? WS_RS + mt * 1024 : WS_CD + n8 * 640);
          asm volatile("s_mov_b32 m0, %0\n\ts_nop 0\n\tglobal_load_lds_dwordx4 %1, %2 sc1" :: "s"(lds0 + (lw == 0 ? T_RS : T_CD)), "v"(vL), "s"(src) : "memory");
        }
        if (TG) asm volatile("global_load_dwordx4 %0, %4, %5 sc1\n\tglobal_load_dwordx4 %1, %4, %5 offset:1024 sc1\n\tglobal_load_dwordx4 %2, %4, %5 offset:2048 sc1\n\t"
                             "global_load_dwordx4 %3, %4, %5 offset:3072 sc1"
                             : "+v"(cs[0]), "+v"(cs[1]), "+v"(cs[2]), "+v"(cs[3]) : "v"(vL), "s"(P.ws + WS_CS + (ns * 4 + lw) * 4096) : "memory");
      }
    }
    if (k == 0) issue_a(2); else issue(k + 2);
    if (k == WK && wdo) {
#pragma unroll
      for (int u = 0; u < 4; ++u) {
        const int task = wtask * 2 + (u >> 1);
        const unsigned od = (((task % 240) / 5) * 32 + (lane >> 4) * 4) * 1280 + ((task / 240) * 160 + (task % 5) * 32) * 2 + (lane & 15) * 4;
        const char* pd = (const char*)P.wdn + (u & 1) * (16 * 1280);
        asm volatile("global_load_dword %0, %4, %5\n\tglobal_load_dword %1, %4, %5 offset:1280\n\tglobal_load_dword %2, %4, %5 offset:2560\n\tglobal_load_dword %3, %4, %5 offset:3840"
                     : "+v"(wv[4 * u]), "+v"(wv[4 * u + 1]), "+v"(wv[4 * u + 2]), "+v"(wv[4 * u + 3]) : "v"(od), "s"(pd) : "memory");
      }
    }
    if (k == WK + 2 && wdo) {
      const __amdgpu_buffer_rsrc_t rw = __builtin_amdgcn_make_buffer_rsrc(P.ws, 0, WS_END, 0x00020000);
      const bool up = lane & 8;
#pragma unroll
      for (int u = 0; u < 2; ++u) {
        u32x4 wo[2];
#pragma unroll
        for (int e = 0; e < 4; ++e) {
          asm volatile("" : "+v"(wv[8 * u + 2 * e]), "+v"(wv[8 * u + 2 * e + 1]));
          const unsigned lo = __builtin_amdgcn_perm(wv[8 * u + 2 * e + 1], wv[8 * u + 2 * e], 0x05040100u), hi = __builtin_amdgcn_perm(wv[8 * u + 2 * e + 1], wv[8 * u + 2 * e], 0x07060302u);
          const unsigned t = __builtin_amdgcn_update_dpp(0, up ? lo : hi, 0x128, 0xf, 0xf, true);
          wo[0][e] = up ? t : lo;
          wo[1][e] = up ? hi : t;
        }
        const int task = wtask * 2 + u;
        const unsigned dst = WS_WD + (((task / 240) * 48 + (task % 240) / 5) * 10 + (task % 5) * 2) * 1024 + (2 * (lane & 7) + ((lane >> 3) & 1) + (lane & 48)) * 16;
        st16c(rw, dst, wo[0]); st16c(rw, dst + 1024, wo[1]);
      }
    }
    if (k == 0) tag = read_tag(P.ws);
    if (TG && k == 4 * (lw >> 1) + (mt & 1)) touch_lines(lds0 + T_PF, pfv, pfb);
    if (k == 7) touch_lines(lds0 + T_PF, pfv, pfb + (TG ? 1024 : 0));
  });
  wait_v<12>();
  BAR();
  wait_v<1>();
  BAR();
  if (TG) {
    asm volatile("" : "+v"(cs[0]), "+v"(cs[1]), "+v"(cs[2]), "+v"(cs[3]));
    put_cs();
  } else if (lw < 2) {
    wait_flags(FLAG(C_F0 + fcol), tag);
    const __amdgpu_buffer_rsrc_t rw = __builtin_amdgcn_make_buffer_rsrc(P.ws, 0, WS_END, 0x00020000);
#pragma unroll
    for (int i = 0; i < 4; ++i) cs[i] = ld16c(rw, WS_CS + (ns * 4 + lw) * 4096 + i * 1024 + lane * 16);
    put_cs();
  }
  if (lw == 0 && lane == 0) *(volatile LAS unsigned*)(smem + T_GO) = 0;
  WAIT_L0();
  if (blockIdx.x == 64 && lw == 3) {
    wait_flags2(FLAG(C_F0 + lane * 4), FLAG(C_F0 + (64 + lane) * 4), tag);
    wait_flags2(FLAG(C_F0 + (128 + lane) * 4), FLAG(C_F0 + (192 + lane) * 4), tag);
    if (lane == 0) st_flag(FLAG(C_E), tag);
  }
  const u64* fab = FLAG(C_FAB + (nt * 64 + split * 16) * 2 + (lane & 31));
  unsigned vY[4];
#pragma unroll
  for (int j = 0; j < 4; ++j) vY[j] = (32 * lw + 8 * j + rr) * 4096 + ((c8 ^ rr) * 16) - j * 1024;
  const char* baseY = (const char*)(split < 2 ? P.wsr : P.wsi) + (size_t)mt2 * 128 * 4096 + (split & 1) * 2048;
  const char* baseAB = P.ws + WS_AB + (nt * 64 + split * 16) * B_ST + lw * 5120;
  const char* baseH = P.ws + WS_H + ((mt2 * 24 + (lw >> 1)) * 16 + (split & 1) * 8 + (lw & 1)) * 1024;
  const unsigned ldsA = ldsX, ldsB = __builtin_amdgcn_readfirstlane(lds0 + L_B + lw * 5120);
  auto issueYA = [&](int k, int yi) { glds4<false>(ldsA + (k & 3) * A_ST, baseY + (yi ^ xg) * 128, vY[0], vY[1], vY[2], vY[3]); };
  auto issueYB = [&](int k, int yi) { glds5<true>(ldsB + (k & 3) * B_ST, baseAB + (yi ^ xg) * B_ST, vL, vL, vL, vL, vL4); };
  BAR();
  __builtin_amdgcn_s_setprio(3);
  issueYA(0, 0); issueYA(1, 1);
  if (lw == 0) {
    wait_flags(fab, tag);
    if (lane == 0) *(volatile LAS unsigned*)(smem + T_GO) = 1;
  } else {
#pragma unroll 1
    for (int it = 0; it < 4000000; ++it) {
      if (*(volatile LAS unsigned*)(smem + T_GO) != 0) break;
      __builtin_amdgcn_s_sleep(1);
    }
  }
  if (wdo) wait_v<0>();
  asm volatile("" ::: "memory");
  const u64* fh = FLAG(C_FH + mt2 * 64 + (lane & 31) * 2 + (split & 1));
  const int l36 = min(lane, 35);
  const int wt = (nt * 240 + ((l36 / 3) * 4 + lw) * 5 + (split >> 1) * 2 + l36 % 3) >> 1;
  const u64* fw = FLAG(C_FW + wt);
  u64 fhv = 0, fwv = 0;
  issueYB(0, 0); issueYB(1, 1);
  wait_v<5>(); BAR();
  issueYA(2, 2); issueYB(2, 2);
  wait_v<9>(); BAR();
  __builtin_amdgcn_s_setprio(0);
  issueYA(3, 3); issueYB(3, 3); issueYA(4, 4); issueYB(4, 4);
  if (wdo && lane == 0) st_flag(FLAG(C_FW + wtask), tag);
#pragma unroll 1
  for (int k = 2; k < NY - 3; ++k) {
    wait_v<18>();
    BAR();
    if (k == 9) asm volatile("global_load_dwordx2 %0, %2, off sc1\n\tglobal_load_dwordx2 %1, %3, off sc1" : "+v"(fhv), "+v"(fwv) : "v"(fh), "v"(fw) : "memory");
    issueYA(k + 3, k + 3); issueYB(k + 3, k + 3);
    if (!TG && k == 2 + (lw >> 1)) touch_lines(lds0 + T_PF, pfv, pfb + 1024);
  }
  asm volatile("" : "+v"(fhv), "+v"(fwv));
  if (!__all(fhv == tag && fwv == tag)) wait_flags2(fh, fw, tag);
  bf16x8 GA[4][4], GB[4][5];
  f32x4 dac[4][5];
  auto load1 = [&](int i, int n) {
    const int s = i & 3;
    const char* pa = baseH + (i ^ xg) * (2 * A_ST);
    const char* pb = baseWD + (i ^ xg) * (2 * B_ST);
    if (n < 4) asm volatile("global_load_dwordx4 %0, %1, %2 offset:%3 sc1" : "=&v"(GA[s][n]) : "v"(n < 2 ? vL : vL4), "s"(pa), "i"((n & 1) * 2048) : "memory");
    else asm volatile("global_load_dwordx4 %0, %1, %2 offset:%3 sc1" : "=&v"(GB[s][n - 4]) : "v"(n < 8 ? vL : vL4), "s"(pb), "i"(((n - 4) & 3) * 1024) : "memory");
  };
  auto loadD = [&](int i) {
#pragma unroll
    for (int n = 0; n < 9; ++n) load1(i, n);
  };
  auto group = [&](auto I) __attribute__((always_inline)) {
    constexpr int i = decltype(I)::v, s = i & 3;
    wait_v<(i == 0 ? 27 : i < 10 ? 18 : (11 - i) * 9)>();
    asm volatile("" : "+v"(GA[s][0]), "+v"(GA[s][1]), "+v"(GA[s][2]), "+v"(GA[s][3]), "+v"(GB[s][0]), "+v"(GB[s][1]), "+v"(GB[s][2]), "+v"(GB[s][3]), "+v"(GB[s][4]));
#pragma unroll
    for (int m = 0; m < 20; ++m) {
      const int r = (m & 4) ? 3 - (m & 3) : (m & 3);
      mf(dac[r][m >> 2], GB[s][m >> 2], GA[s][r], i == 0);
      if (i >= 1 && i + 3 < 12 && (m & 1) == 1 && m < 18) load1(i + 3, m >> 1);
    }
  };
  loadD(0);
  wait_v<27>(); BAR();
  loadD(1);
  wait_v<27>(); BAR();
  loadD(2);
  wait_v<27>(); BAR();
  loadD(3);
  __builtin_amdgcn_s_setprio(2);
  sfor<0, 12>(group);
  MFMA_DONE();
  after_mfma(dac);
#pragma unroll
  for (int r = 0; r < 4; ++r)
#pragma unroll
    for (int j = 0; j < 5; ++j) *(LAS f32x4*)(smem + f_red(lw) + (r * 5 + j) * 1024 + lane * 16) = dac[r][j];
  WAIT_L0();
  BAR();
}

template <bool TG>
DI void mfma_p1(const Params& P, LAS char* smem, int lane, int w, int mt, int ns, u64& tag) {
  const int n8 = ns & 7, hcol0 = TG ? 128 * n8 : 1024 + 64 * n8;
  const __amdgpu_buffer_rsrc_t rw = __builtin_amdgcn_make_buffer_rsrc(P.ws, 0, WS_END, 0x00020000);
  int q = lane >> 4, rho = lane & 15;
  const int rr = lane >> 3, c8 = lane & 7;
  u32x4 vx[5], vw[5], vm[10];
  if (TG) {
    const char* px = (const char*)P.x + (size_t)(mt * 128 + n8 * 16 + 4 * w + q) * 1280 + rho * 16;
#pragma unroll
    for (int i = 0; i < 5; ++i) vx[i] = *(const u32x4*)(px + i * 256);
    const char* pw = (const char*)((w >> 1) ? P.wu : P.wg) + (size_t)(40 * mt + rr) * 3072 + (hcol0 + (w & 1) * 64) * 2 + c8 * 16;
#pragma unroll
    for (int i = 0; i < 5; ++i) vw[i] = *(const u32x4*)(pw + i * (8 * 3072));
    if (w < 2) {
      const char* pm = (const char*)(w ? P.wu : P.wg) + (size_t)(40 * mt + rr) * 3072 + (1024 + 64 * n8) * 2 + c8 * 16;
#pragma unroll
      for (int i = 0; i < 5; ++i) vm[i] = *(const u32x4*)(pm + i * (8 * 3072));
    } else {
      const int e = (n8 & 3) * 160 + 10 * mt + 5 * (w - 2) + min(rr, 4);
      const char* pd = (const char*)(n8 >= 4 ? P.wdi : P.wdr) + (size_t)e * 1280 + c8 * 16;
#pragma unroll
      for (int i = 0; i < 10; ++i) vm[i] = *(const u32x4*)(pd + i * 128);
    }
  }
  tag = read_tag(P.ws);
  const int wm = w & 1, wn = w >> 1;
  constexpr int NC = TG ? 4 : 2;
  unsigned vX0 = TG ? wm * 8192 + rho * 128 + ((q ^ (rho & 7)) * 16)
                          : wm * 8192 + (8 * (rho >> 2) + (rho & 3)) * 128 + ((q ^ ((rho & 2) | ((rho >> 1) & 4))) * 16);
  unsigned vX1 = vX0 ^ 64;
  unsigned vW[NC];
  {
    const int qq = rho >> 2, p = lane & 3, swz = (qq & 2) | ((q & 1) << 2);
#pragma unroll
    for (int c = 0; c < NC; ++c) {
      const int cc = TG ? c : (2 * wn + c);
      vW[c] = L_W + (TG ? wn * 49152 : 0) + (8 * q + qq) * 128 + ((((2 * cc) ^ swz) + (p >> 1)) * 16) + 8 * (p & 1);
    }
  }
  unsigned vD0 = L_D + (80 * wn + rho) * 128 + ((q ^ (rho & 7)) * 16), vD1 = vD0 ^ 64;
  asm volatile("" : "+v"(vX0), "+v"(vX1));
  if (!TG) asm volatile("" : "+v"(vD0), "+v"(vD1));
#pragma unroll
  for (int c = 0; c < NC; ++c) asm volatile("" : "+v"(vW[c]));
  f32x4 aG[NC][4], aU[NC][4], aA[4][5];
  if (TG) {
    unsigned ONE2 = 0x3f803f80u, SLO = 0x00003f80u, SHI = 0x3f800000u;
    asm volatile("" : "+v"(ONE2), "+v"(SLO), "+v"(SHI));
    float rso = 0, c1 = 0, c2 = 0; unsigned off2 = 0;
    {
      float sx[2] = {0, 0}, sq[2] = {0, 0};
#pragma unroll
      for (int i = 0; i < 5; ++i)
#pragma unroll
        for (int e = 0; e < 4; ++e) { sx[e & 1] = dot2(vx[i][e], ONE2, sx[e & 1]); sq[e & 1] = dot2(vx[i][e], vx[i][e], sq[e & 1]); }
      const float mean = sum16(sx[0] + sx[1]) * (1.0f / D);
      const float var = fmaxf(sum16(sq[0] + sq[1]) * (1.0f / D) - mean * mean, 0.f);
      const float sd = rbf(__builtin_amdgcn_sqrtf(rbf(rbf(var) + 1e-5f)));
      rso = (lane & 1) ? rbf(mean) : __builtin_amdgcn_rcpf(sd);
    }
    auto colsum = [&](const u32x4* v) -> float {
      float c[8] = {0, 0, 0, 0, 0, 0, 0, 0};
#pragma unroll
      for (int i = 0; i < 5; ++i)
#pragma unroll
        for (int e = 0; e < 4; ++e) { c[2 * e] = dot2(v[i][e], SLO, c[2 * e]); c[2 * e + 1] = dot2(v[i][e], SHI, c[2 * e + 1]); }
      float d4[4], d2[2];
#pragma unroll
      for (int e = 0; e < 4; ++e) d4[e] = xchg32(c[e], c[4 + e]);
#pragma unroll
      for (int e = 0; e < 2; ++e) d2[e] = xchg16(d4[e], d4[2 + e]);
      d2[0] = dpp_add<0x128>(d2[0]); d2[1] = dpp_add<0x128>(d2[1]);
      return (lane & 8) ? d2[1] : d2[0];
    };
    c1 = colsum(vw);
    {
      if (w < 2) {
        c2 = colsum(vm);
        off2 = WS_CS + ((((8 + n8) * 4 + w) * 16 + mt) * 64 + 8 * c8 + rr) * 4;
      } else {
        float a = 0, b = 0;
#pragma unroll
        for (int i = 0; i < 10; ++i) { a = dot2(vm[i][0], ONE2, a); b = dot2(vm[i][1], ONE2, b); a = dot2(vm[i][2], ONE2, a); b = dot2(vm[i][3], ONE2, b); }
        c2 = sum8(a + b);
        off2 = (c8 == 0 && rr < 5) ? WS_CD + (n8 * 160 + 10 * mt + 5 * (w - 2) + rr) * 4 : 0xfffffff0u;
      }
    }
    asm volatile("" : "+v"(c1), "+v"(c2), "+v"(rso));
    __builtin_amdgcn_raw_buffer_store_b32(__float_as_uint(c1), rw, WS_CS + (((n8 * 4 + w) * 16 + mt) * 64 + 8 * c8 + rr) * 4, 0, 16);
    __builtin_amdgcn_raw_buffer_store_b32(__float_as_uint(c2), rw, off2, 0, 16);
    __builtin_amdgcn_raw_buffer_store_b32(__float_as_uint(rso), rw, rho < 2 ? WS_RS + mt * 1024 + rho * 512 + (n8 * 16 + 4 * w + q) * 4 : 0xfffffff0u, 0, 16);
  }
  bf16x8 FX[2][4], FD[2][5];
  s16x4 FW[2][2 * NC][2];
  constexpr int NR = TG ? 20 : 17, NM = TG ? 32 : 36;
  auto rd1 = [&](int s, int buf, int ks, int r) {
    if (r >= 2 && r < 6) {
      const int i = r - 2;
      FX[s][i] = *(LAS bf16x8*)(smem + (ks ? vX1 : vX0) + buf * 16384 + (TG ? i * 2048 : (i >> 1) * 4096 + (i & 1) * 512));
    } else if (r < 4 + 4 * NC) {
      const int h = r < 2 ? r : r - 4, f = h >> 1;
      FW[s][f][h & 1] = __builtin_amdgcn_ds_read_tr16_b64_v4i16((LAS s16x4*)(smem + vW[f >> 1] + buf * 16384 + ks * 4096 + (f & 1) * 8192 + (h & 1) * 512));
    } else {
      const int jj = r - (4 + 4 * NC);
      FD[s][jj] = *(LAS bf16x8*)(smem + (ks ? vD1 : vD0) + buf * 20480 + jj * 2048);
    }
  };
  auto mm1 = [&](int s, int m, bool first) {
    const int f = m >> 2, i = (f & 1) ? 3 - (m & 3) : (m & 3);
    if (f < 2 * NC) {
      const bf16x8 wf = __builtin_shufflevector(FW[s][f][0], FW[s][f][1], 0, 1, 2, 3, 4, 5, 6, 7);
      mf((f & 1) ? aU[f >> 1][i] : aG[f >> 1][i], wf, FX[s][i], first);
    } else mf(aA[i][f - 2 * NC], FX[s][i], FD[s][f - 2 * NC], first);
  };
  auto seg = [&](int s, bool first, int m0, int m1, int rk, int buf, int ks) __attribute__((always_inline)) {
    int r = 0;
#pragma unroll
    for (int m = m0; m < m1; ++m) {
      mm1(s, m, first);
      if (rk == 2 && r == 0) r = 2;
      if (rk == 2 && r == 6) r = 4 + 4 * NC;
      const int rend = rk == 0 ? 0 : rk == 3 ? 4 + 4 * NC : NR;
      if (r < rend && (TG ? (m % 5 != 4) : (rk == 3 || m % 3 != 2))) { rd1(s ^ 1, buf, ks, r); ++r; }
      PIN();
    }
  };
  BAR();
#pragma unroll
  for (int r = 0; r < NR; ++r) rd1(0, 0, 0, r);
  PIN();
  constexpr int KJ = TG ? 10 : 7;
  seg(0, true, 0, NM, 1, 0, 1);
  WAIT_L0(); BAR(); PIN();
  seg(1, false, 0, NM, 1, 1, 0);
  wait_v<0>();
  if (lane == 0) st_flag(FLAG(C_F0 + (mt * 16 + ns) * 4 + w), tag);
  WAIT_L0(); PIN();
#pragma unroll 1
  for (int it = 0; it < (KJ - 1) / 3; ++it) {
    sfor<1, 4>([&](auto K) __attribute__((always_inline)) {
      constexpr int k = decltype(K)::v;
      seg(0, false, 0, NM, 1, k % 3, 1);
      WAIT_L0();
      if (!TG || k < 3 || it < 2) BAR();
      PIN();
      seg(1, false, 0, NM, 1, (k + 1) % 3, 0);
      WAIT_L0(); PIN();
    });
  }
  if (!TG) sfor<7, 10>([&](auto K) __attribute__((always_inline)) {
    constexpr int k = decltype(K)::v;
    seg(0, false, 8 * NC, NM, 2, k % 3, 1);
    if (k < 9) { WAIT_L0(); BAR(); PIN(); }
    seg(1, false, 8 * NC, NM, k < 9 ? 2 : 3, (k + 1) % 3, 0);
    WAIT_L0(); PIN();
  });
  if (!TG) {
    MFMA_DONE();
    after_mfma(aA);
    asm volatile("" : "+v"(lane)); q = lane >> 4; rho = lane & 15;
    const float sgn = (n8 >= 4) ? -1.0f : 1.0f;
    float cs[5];
#pragma unroll
    for (int j = 0; j < 5; ++j) cs[j] = *(LAS float*)(smem + T_CD + (80 * wn + 16 * j + rho) * 4);
    const int kt = (n8 >= 4 ? 32 : 0) + mt * 2 + wm;
#pragma unroll
    for (int I = 0; I < 2; ++I) {
      f32x4 r4[2], m4[2];
#pragma unroll
      for (int t = 0; t < 2; ++t) {
        const int row = 64 * wm + 32 * I + 8 * q + 4 * t;
        r4[t] = *(LAS f32x4*)(smem + T_RS + row * 4) * sgn; m4[t] = *(LAS f32x4*)(smem + T_RM + row * 4);
      }
      const unsigned dst = WS_AB + ((((n8 & 3) * 64 + kt) * 2 + I) * 10 + 5 * wn) * 1024 + lane * 16;
#pragma unroll
      for (int j = 0; j < 5; ++j) { aA[2 * I][j] -= m4[0] * cs[j]; aA[2 * I + 1][j] -= m4[1] * cs[j]; }
      PIN();
#pragma unroll
      for (int j = 0; j < 5; ++j) { aA[2 * I][j] *= r4[0]; aA[2 * I + 1][j] *= r4[1]; }
      PIN();
#pragma unroll
      for (int j = 0; j < 5; ++j) {
        const f32x4 v0 = aA[2 * I][j], v1 = aA[2 * I + 1][j];
        const u32x4 o = {pk_bf16(v0[0], v0[1]), pk_bf16(v0[2], v0[3]), pk_bf16(v1[0], v1[1]), pk_bf16(v1[2], v1[3])};
        st16c(rw, dst + j * 1024, o);
      }
    }
    WAIT_L0(); PIN();
#pragma unroll
    for (int k = KJ; k < 10; ++k) {
      seg(0, false, 0, 8 * NC, 3, k % 3, 1);
      WAIT_L0(); PIN();
      if (k == 9) {
        wait_v<0>();
        if (lane == 0) st_flag(FLAG(C_FAB + ((n8 & 3) * 64 + kt) * 2 + wn), tag);
        PIN();
      }
      seg(1, false, 0, 8 * NC, k < 9 ? 3 : 0, (k + 1) % 3, 0);
      WAIT_L0(); PIN();
    }
  }
  MFMA_DONE();
  after_mfma(aG); after_mfma(aU);
  asm volatile("" : "+v"(lane)); q = lane >> 4; rho = lane & 15;
  BAR();
  {
    const float K0 = -2.0f * 0.7978845608028654f * 1.4426950408889634f, K1 = K0 * 0.044715f;
    float mu[4], c0[4], c1[4], v2[4];
#pragma unroll
    for (int i = 0; i < 4; ++i) {
      const int row = TG ? 64 * wm + 16 * i + rho : 64 * wm + 32 * (i >> 1) + 8 * (rho >> 2) + 4 * (i & 1) + (rho & 3);
      const float rs = *(LAS float*)(smem + T_RS + row * 4);
      mu[i] = *(LAS float*)(smem + T_RM + row * 4);
      c0[i] = K0 * rs; c1[i] = K1 * rs * rs * rs; v2[i] = __builtin_amdgcn_rcpf(rs * rs);
    }
#pragma unroll
    for (int J = 0; J < NC / 2; ++J) {
      f32x4 cg[2], cu[2];
#pragma unroll
      for (int t = 0; t < 2; ++t) {
        const int c = 2 * J + t;
        const int o = TG ? (wn * 256 + c * 64 + q * 16) : ((2 * wn + c) * 64 + q * 16);
        cg[t] = *(LAS f32x4*)(smem + T_CS + o);
        cu[t] = *(LAS f32x4*)(smem + T_CS + (TG ? 512 : 256) + o);
      }
#pragma unroll
      for (int ip = 0; ip < 2; ++ip) {
        f32x4 g[4], z[4], p[4];
#define EACH(stmt) _Pragma("unroll") for (int n = 0; n < 4; ++n) { const int i = 2 * ip + (n >> 1), t = n & 1; (void)i; (void)t; stmt; } PIN()
        EACH(g[n] = aG[2 * J + t][i] - cg[t] * mu[i]);
        EACH(z[n] = g[n] * g[n]);
        EACH(z[n] = z[n] * c1[i] + c0[i]);
        EACH(z[n] = z[n] * g[n]);
        EACH(p[n] = aU[2 * J + t][i] - cu[t] * mu[i]; z[n][0] = __builtin_amdgcn_exp2f(z[n][0]); z[n][1] = __builtin_amdgcn_exp2f(z[n][1]);
             z[n][2] = __builtin_amdgcn_exp2f(z[n][2]); z[n][3] = __builtin_amdgcn_exp2f(z[n][3]));
        EACH(z[n] = z[n] * v2[i] + v2[i]);
        EACH(p[n] = p[n] * g[n]; z[n][0] = __builtin_amdgcn_rcpf(z[n][0]); z[n][1] = __builtin_amdgcn_rcpf(z[n][1]);
             z[n][2] = __builtin_amdgcn_rcpf(z[n][2]); z[n][3] = __builtin_amdgcn_rcpf(z[n][3]));
        EACH(p[n] = p[n] * z[n]);
#undef EACH
#pragma unroll
        for (int u = 0; u < 2; ++u) {
          const int i = 2 * ip + u;
          const u32x4 o = {pk_bf16(p[2 * u][0], p[2 * u][1]), pk_bf16(p[2 * u][2], p[2 * u][3]), pk_bf16(p[2 * u + 1][0], p[2 * u + 1][1]), pk_bf16(p[2 * u + 1][2], p[2 * u + 1][3])};
          const int f0 = hcol0 + (TG ? wn * 64 + J * 32 : wn * 32);
          const int rb = TG ? 4 * wm + i : 4 * wm + 2 * (i >> 1) + (rho >> 3), pos = TG ? lane : 8 * ((rho >> 2) & 1) + 4 * (i & 1) + (rho & 3) + 16 * q;
          st16c(rw, WS_H + (((mt * 24 + (f0 >> 6)) * 8 + rb) * 2 + ((f0 >> 5) & 1)) * 1024 + pos * 16, o);
        }
      }
    }
  }
}

DI void mfma_p2(const Params& P, LAS char* smem, int lane, int w, int split, int mt, int nt, int mt1, int ns, u64 tag) {
  asm volatile("" : "+v"(lane));
  const int wm = w & 1, wn = w >> 1, q = lane >> 4, r15 = lane & 15;
  unsigned vY0 = wm * 8192 + r15 * 128 + ((q ^ (r15 & 7)) * 16), vY1 = vY0 ^ 64;
  unsigned vB0 = L_B + wn * 5120 + lane * 16, vB2 = vB0 + 2 * B_ST;
  asm volatile("" : "+v"(vY0), "+v"(vY1), "+v"(vB0), "+v"(vB2));
  const __amdgpu_buffer_rsrc_t rw = __builtin_amdgcn_make_buffer_rsrc(P.ws, 0, WS_END, 0x00020000);
  const int tile = mt * 4 + nt;
  f32x4 acc[4][5];
  bf16x8 FA[2][4], FB[2][5];
  auto rdy = [&](int s, int k, int ks, int r) {
    const int buf = k & 3;
    if (r >= 1 && r < 5) FA[s][r - 1] = *(LAS bf16x8*)(smem + (ks ? vY1 : vY0) + buf * A_ST + (r - 1) * 2048);
    else { const int j = r ? r - 4 : 0; FB[s][j] = *(LAS bf16x8*)(smem + (buf >= 2 ? vB2 : vB0) + (buf & 1) * B_ST + ks * 10240 + j * 1024); }
  };
  auto seg = [&](auto K, auto HF) __attribute__((always_inline)) {
    constexpr int k = decltype(K)::v, hf = decltype(HF)::v, rk = k + hf, rh = hf ^ 1;
    int r = 0;
#pragma unroll
    for (int m = 0; m < 20; ++m) {
      const int i = (m & 4) ? 3 - (m & 3) : (m & 3);
      mf(acc[i][m >> 2], FB[hf][m >> 2], FA[hf][i], false);
      if (r < 9 && m % 3 != 2) { rdy(rh, rk, rh, r); ++r; }
      PIN();
    }
  };
  auto publish = [&](int ip) {
    u32x4 o[5];
#pragma unroll
    for (int t = 0; t < 2; ++t) {
      const int i = 2 * ip + t;
#pragma unroll
      for (int j = 0; j < 5; ++j) {
        const int n = j < 4 ? 2 * t + (j >> 1) : 4, e = j < 4 ? (j & 1) * 2 : 2 * t;
        o[n][e] = pk_bf16(acc[i][j][0], acc[i][j][1]); o[n][e + 1] = pk_bf16(acc[i][j][2], acc[i][j][3]);
      }
    }
    if (w == split) {
#pragma unroll
      for (int n = 0; n < 5; ++n) *(LAS u32x4*)(smem + F_Y + (n < 4 ? ip * 4096 + n * 1024 : 8192 + ip * 1024) + lane * 16) = o[n];
    } else {
      const unsigned dst = WS_P + ((tile * 4 + w) * 3 + (split ^ w) - 1) * 10240 + lane * 16;
#pragma unroll
      for (int n = 0; n < 5; ++n) st16c(rw, dst + (n < 4 ? ip * 4096 + n * 1024 : 8192 + ip * 1024), o[n]);
    }
  };
#pragma unroll
  for (int i = 0; i < 4; ++i)
#pragma unroll
    for (int j = 0; j < 5; ++j) acc[i][j] = (f32x4){0, 0, 0, 0};
  after_mfma(acc);
  BAR();
#pragma unroll
  for (int r = 0; r < 9; ++r) rdy(0, 0, 0, r);
  WAIT_L0(); PIN();
#pragma unroll 1
  for (int it = 0; it < NY / 4; ++it) {
    sfor<0, 4>([&](auto K) __attribute__((always_inline)) {
      constexpr int k = decltype(K)::v;
      seg(K, IC<0>{});
      if (k == 2 && it == 0) {
        wait_v<0>();
        if (lane == 0) st_flag(FLAG(C_FH + mt1 * 64 + ns * 4 + w), tag);
      }
      WAIT_L0();
      if (k < 3 || it < NY / 4 - 1) BAR();
      PIN();
      seg(K, IC<1>{});
      WAIT_L0(); PIN();
    });
  }
  MFMA_DONE();
  after_mfma(acc);
  __builtin_amdgcn_s_setprio(3);
  publish(0); publish(1);
  if (w != split) {
    wait_v<0>();
    if (lane == 0) st_flag(FLAG(C_FP + (tile * 4 + w) * 4 + (split ^ w) - 1), tag);
  }
  const int orow = mt * 128 + (split & 1) * 64 + w * 16, ocol = nt * 160 + (split >> 1) * 80;
  u32x4 xv[3];
  unsigned tr[3], oo[3];
#pragma unroll
  for (int i = 0; i < 3; ++i) {
    const int n = min(i * 64 + lane, 159);
    const unsigned off = ((orow + n / 10) * D + ocol + (n % 10) * 8) * 2;
    xv[i] = *(const u32x4*)((const char*)P.x + off);
    tr[i] = F_T + w * (16 * FPITCH) + (n / 10) * FPITCH + (n % 10) * 16;
    oo[i] = i * 64 + lane < 160 ? off : 0xfffffff0u;
  }
  if (w == split) {
    const u64* fp = FLAG(C_FP + (tile * 4 + split) * 4 + min(lane, 2));
    __builtin_amdgcn_s_sleep(60);
#pragma unroll 1
    for (int it = 0; it < 40000; ++it) {
      if (__all(ld_flag(fp) == tag)) break;
      __builtin_amdgcn_s_sleep(8);
    }
    WAIT_L0();
    if (lane == 0) *(volatile LAS unsigned*)(smem + T_GO) = 2;
  } else {
    __builtin_amdgcn_s_setprio(0);
#pragma unroll 1
    for (int it = 0; it < 4000000; ++it) {
      if (*(volatile LAS unsigned*)(smem + T_GO) == 2) break;
      __builtin_amdgcn_s_sleep(4);
    }
    __builtin_amdgcn_s_setprio(3);
  }
  asm volatile("" ::: "memory");
  f32x4 ys[5];
  {
    u32x4 pa[3][2]; u32x2 pb[3];
#pragma unroll
    for (int s = 0; s < 3; ++s) {
      const unsigned src = WS_P + ((tile * 4 + split) * 3 + s) * 10240 + lane * 16;
      pa[s][0] = ld16c(rw, src + w * 2048); pa[s][1] = ld16c(rw, src + w * 2048 + 1024);
      pb[s] = __builtin_amdgcn_raw_buffer_load_b64(rw, src + 8192 + (w >> 1) * 1024 + (w & 1) * 8, 0, 16);
    }
#pragma unroll
    for (int j = 0; j < 5; ++j) {
#pragma unroll
      for (int s = 0; s < 3; ++s) {
        const unsigned lo = j < 4 ? pa[s][j >> 1][(j & 1) * 2] : pb[s][0], hi = j < 4 ? pa[s][j >> 1][(j & 1) * 2 + 1] : pb[s][1];
        const f32x4 v = {bflo(lo), bfhi(lo), bflo(hi), bfhi(hi)};
        ys[j] = s ? ys[j] + v : v;
      }
      asm volatile("" : "+v"(ys[j]));
    }
  }
  {
    LAS char* po = smem + F_Y + lane * 16;
    const u32x4 oa0 = *(LAS u32x4*)(po + w * 2048), oa1 = *(LAS u32x4*)(po + w * 2048 + 1024);
    const u32x2 ob = *(LAS u32x2*)(po + 8192 + (w >> 1) * 1024 + (w & 1) * 8);
#pragma unroll
    for (int j = 0; j < 5; ++j) {
      const unsigned l0 = j < 2 ? oa0[(j & 1) * 2] : j < 4 ? oa1[(j & 1) * 2] : ob[0], h0 = j < 2 ? oa0[(j & 1) * 2 + 1] : j < 4 ? oa1[(j & 1) * 2 + 1] : ob[1];
      ys[j] += (f32x4){bflo(l0), bfhi(l0), bflo(h0), bfhi(h0)};
      asm volatile("" : "+v"(ys[j]));
    }
  }
  unsigned tw = F_T + w * (16 * FPITCH) + r15 * FPITCH + q * 8, rd0 = w * 5120 + lane * 16, rd2 = rd0 + L_B;
  asm volatile("" : "+v"(tw), "+v"(rd0), "+v"(rd2), "+v"(tr[0]), "+v"(tr[1]), "+v"(tr[2]), "+v"(oo[0]), "+v"(oo[1]), "+v"(oo[2]));
  BAR();
  f32x4 dd[4][5];
#pragma unroll
  for (int j = 0; j < 5; ++j)
#pragma unroll
    for (int u = 0; u < 4; ++u) dd[u][j] = *(LAS f32x4*)(smem + ((u & 2) ? rd2 : rd0) + (u & 1) * 20480 + j * 1024);
#pragma unroll
  for (int j = 0; j < 5; ++j) {
    const f32x4 d = (dd[0][j] + dd[1][j]) + (dd[2][j] + dd[3][j]);
    f32x4 e = d * -1.4426950408889634f;
#pragma unroll
    for (int r = 0; r < 4; ++r) e[r] = __builtin_amdgcn_exp2f(e[r]);
    e = e + 1.0f;
#pragma unroll
    for (int r = 0; r < 4; ++r) e[r] = __builtin_amdgcn_rcpf(e[r]);
    e = e * d * ys[j];
    *(LAS u32x2*)(smem + tw + j * 32) = (u32x2){pk_bf16(e[0], e[1]), pk_bf16(e[2], e[3])};
  }
  WAIT_L0();
  const __amdgpu_buffer_rsrc_t ro = __builtin_amdgcn_make_buffer_rsrc(P.out, 0, S * D * 2, 0x00020000);
#pragma unroll
  for (int i = 0; i < 3; ++i) {
    const u32x4 v = *(LAS u32x4*)(smem + tr[i]);
    u32x4 o;
#pragma unroll
    for (int d = 0; d < 4; ++d) o[d] = pk_bf16(bflo(v[d]) + bflo(xv[i][d]), bfhi(v[d]) + bfhi(xv[i][d]));
    __builtin_amdgcn_raw_buffer_store_b128(o, ro, oo[i], 0, 0);
  }
}

__global__ __launch_bounds__(512, 2)
void fused(Params P) {
  extern __shared__ __attribute__((aligned(16))) char smem_[];
  LAS char* smem = (LAS char*)smem_;
  const int tid = threadIdx.x, lane = tid & 63, w = __builtin_amdgcn_readfirstlane(tid >> 6);
  const int bid = blockIdx.x, xcd = bid & 7, idx = bid >> 3;
  u64 tag;
  if (w == 3 && (idx & 8)) {
    u64 pc; unsigned len;
    asm volatile("s_getpc_b64 %0\n.Lcodepf:\n\ts_mov_b32 %1, code_end_marker-.Lcodepf" : "=s"(pc), "=s"(len));
    const unsigned off = ((idx & 7) * 64 + lane) * 128;
    if (off + 4 <= len) touch_lines((unsigned)(size_t)smem + T_PF, off, (const char*)pc);
  }
  const int mt1 = (xcd & 1) * 8 + (idx & 7), j = idx >> 3, ns = (j & 1) * 8 + (xcd >> 1) * 2 + (j >> 1);
  const int split = xcd >> 1, mt2 = (xcd & 1) * 8 + (idx >> 2), nt = idx & 3;
  if (w < 4) {
    if (j & 1) mfma_p1<false>(P, smem, lane, w, mt1, ns, tag); else mfma_p1<true>(P, smem, lane, w, mt1, ns, tag);
    mfma_p2(P, smem, lane, w, split, mt2, nt, mt1, ns, tag);
  } else {
    helper(P, smem, lane, w & 3, mt1, ns, split, mt2, nt);
    __builtin_amdgcn_s_waitcnt(0);
  }
}

extern "C" __global__ void code_end_marker() {}

extern "C" void kernel_launch(void* const* d_in, const int* in_sizes, int n_in, void* d_out, int out_size, void* d_ws, size_t ws_size, hipStream_t stream) {
  Params P;
  P.x = (const bf16_t*)d_in[0]; P.wsr = (const bf16_t*)d_in[1]; P.wsi = (const bf16_t*)d_in[2];
  P.wdr = (const bf16_t*)d_in[3]; P.wdi = (const bf16_t*)d_in[4];
  P.wg = (const bf16_t*)d_in[6]; P.wu = (const bf16_t*)d_in[7]; P.wdn = (const bf16_t*)d_in[8];
  P.out = (bf16_t*)d_out; P.ws = (char*)d_ws;
  (void)hipFuncSetAttribute((const void*)fused, hipFuncAttributeMaxDynamicSharedMemorySize, LDS_BYTES);
  fused<<<256, 512, LDS_BYTES, stream>>>(P);
}
```

```cpp
#include <hip/hip_runtime.h>
#include <hip/hip_bf16.h>
typedef unsigned short bf16_t;
typedef unsigned long long u64;
typedef short bf16x8 __attribute__((ext_vector_type(8)));
typedef short s16x4 __attribute__((ext_vector_type(4)));
typedef float f32x4 __attribute__((ext_vector_type(4)));
typedef float f32x2 __attribute__((ext_vector_type(2)));
typedef unsigned u32x4 __attribute__((ext_vector_type(4)));
typedef unsigned u32x2 __attribute__((ext_vector_type(2)));
typedef __bf16 bf16x2_t __attribute__((ext_vector_type(2)));
#define LAS __attribute__((address_space(3)))
#define GAS __attribute__((address_space(1)))
#define DI __device__ __forceinline__

#define STAMP 0
#define NPRE 2
#define ROT2 0
#define YFL 4
#define FABSL 1
#define AFL 2
#define HFL 2
#define PSPL 1
#define XG 2
#define XY 1
#define XD 1
#define YSB 2
#define WK 2
#define FABM 0
#define SK 5
#define HOLD 0
#define PD0 60
#define PD1 8
#define DPF 0
#define DFK 8
#define DP0 4
#define DPS 2
#define NOPUB 0
#define DPRIO 2
#define CPK 99
#define S0E 2
#define TPRIO 3
#define B0E 0
#define PFG 1
#define PFM 3
#define PH0 0
#define PHS 4
#define PFS 10
#define CPF 1
#define P0M 0
#define PFQ 1
#define HD 0
#define DE 0
#define NAP 2
#define YPL 8
#define XROT 0
#define HPRIO 0
#define PFN 4

constexpr int S = 2048, D = 640, F = 1536;
constexpr unsigned WS_AB = 0, WS_H = WS_AB + 2 * S * D * 2, WS_P = WS_H + S * F * 2, WS_WD = WS_P + 3 * S * D * 2, WS_RS = WS_WD + F * D * 2,
                   WS_CS = WS_RS + S * 8, WS_CD = WS_CS + 16 * 4 * 16 * 64 * 4, WS_CTRL = WS_CD + 8192, WS_END = WS_CTRL + 5120 * 128;
constexpr int C_E = 0, C_F0 = 1, C_FAB = C_F0 + 1024, C_FH = C_FAB + 512, C_FW = C_FH + 1024, C_FP = C_FW + 512;
#define FLAG(i) ((u64*)(P.ws + WS_CTRL) + (i) * 16)

DI unsigned pk_bf16(float lo, float hi) { f32x2 v = {lo, hi}; return __builtin_bit_cast(unsigned, __builtin_convertvector(v, bf16x2_t)); }
DI float rbf(float f) { return __uint_as_float(pk_bf16(f, f) << 16); }
DI float bflo(unsigned u) { return __uint_as_float(u << 16); }
DI float bfhi(unsigned u) { return __uint_as_float(u & 0xffff0000u); }
DI float dot2(unsigned a, unsigned b, float c) { return __builtin_amdgcn_fdot2_f32_bf16(__builtin_bit_cast(bf16x2_t, a), __builtin_bit_cast(bf16x2_t, b), c, false); }
#define GLDS4(POL) asm volatile("s_mov_b32 m0, %0\n\ts_nop 0\n\tglobal_load_lds_dwordx4 %2, %1" POL "\n\tglobal_load_lds_dwordx4 %3, %1 offset:1024" POL \
    "\n\tglobal_load_lds_dwordx4 %4, %1 offset:2048" POL "\n\tglobal_load_lds_dwordx4 %5, %1 offset:3072" POL \
    :: "s"(lds), "s"(base), "v"(v0), "v"(v1), "v"(v2), "v"(v3) : "memory")
#define GLDS5(POL) asm volatile("s_mov_b32 m0, %0\n\ts_nop 0\n\tglobal_load_lds_dwordx4 %2, %1" POL "\n\tglobal_load_lds_dwordx4 %3, %1 offset:1024" POL \
    "\n\tglobal_load_lds_dwordx4 %4, %1 offset:2048" POL "\n\tglobal_load_lds_dwordx4 %5, %1 offset:3072" POL \
    "\n\ts_add_u32 m0, %0, 0x1000\n\ts_nop 0\n\tglobal_load_lds_dwordx4 %6, %1" POL \
    :: "s"(lds), "s"(base), "v"(v0), "v"(v1), "v"(v2), "v"(v3), "v"(v4) : "memory", "scc")
template <bool DEV> DI void glds4(unsigned lds, const char* base, unsigned v0, unsigned v1, unsigned v2, unsigned v3) {
  if (DEV) GLDS4(" sc1"); else GLDS4("");
}
template <bool DEV> DI void glds5(unsigned lds, const char* base, unsigned v0, unsigned v1, unsigned v2, unsigned v3, unsigned v4) {
  if (DEV) GLDS5(" sc1"); else GLDS5("");
}
template <bool DEV = false> DI void touch_lines(unsigned lds, unsigned voff, const char* base) {
  if (DEV) asm volatile("s_mov_b32 m0, %0\n\ts_nop 0\n\tglobal_load_lds_dword %1, %2 sc1" :: "s"(lds), "v"(voff), "s"(base) : "memory");
  else asm volatile("s_mov_b32 m0, %0\n\ts_nop 0\n\tglobal_load_lds_dword %1, %2" :: "s"(lds), "v"(voff), "s"(base) : "memory");
}
template <int N> DI void wait_v() { asm volatile("s_waitcnt vmcnt(%0)" :: "i"(N) : "memory"); }
#define WAIT_L0() do { __builtin_amdgcn_s_waitcnt(0xC07F); asm volatile("" ::: "memory"); } while (0)
#define BAR() do { asm volatile("" ::: "memory"); __builtin_amdgcn_s_barrier(); asm volatile("" ::: "memory"); } while (0)
#define SGB(m, n) __builtin_amdgcn_sched_group_barrier(m, n, 0)
#define PIN() __builtin_amdgcn_sched_barrier(0)
template <int V> struct IC { static constexpr int v = V; };
template <int I, int N, class Fn> DI void sfor(Fn&& f) { if constexpr (I < N) { f(IC<I>{}); sfor<I + 1, N>(f); } }
DI void mf(f32x4& c, const bf16x8& a, const bf16x8& b, bool first) {
  if (first) asm volatile("v_mfma_f32_16x16x32_bf16 %0, %1, %2, 0" : "=&v"(c) : "v"(a), "v"(b));
  else asm volatile("v_mfma_f32_16x16x32_bf16 %0, %1, %2, %0" : "+v"(c) : "v"(a), "v"(b));
}
#define MFMA_DONE() asm volatile("s_nop 15")
template <int A, int B> DI void after_mfma(f32x4 (&c)[A][B]) {
#pragma unroll
  for (int i = 0; i < A; ++i)
#pragma unroll
    for (int j = 0; j < B; ++j) asm volatile("" : "+v"(c[i][j]));
}
#define STAMP_AT(i)

template <int CTRL> DI float dpp_add(float v) { return v + __builtin_bit_cast(float, __builtin_amdgcn_update_dpp(0, __builtin_bit_cast(int, v), CTRL, 0xf, 0xf, true)); }
DI float sum8(float v) { v = dpp_add<0xB1>(v); v = dpp_add<0x4E>(v); return dpp_add<0x141>(v); }
DI float sum16(float v) { return dpp_add<0x140>(sum8(v)); }
DI float xchg16(float keep_lo, float keep_hi) {
  const auto r = __builtin_amdgcn_permlane16_swap(__float_as_uint(keep_lo), __float_as_uint(keep_hi), false, false);
  return __uint_as_float(r[0]) + __uint_as_float(r[1]);
}
DI float xchg32(float keep_lo, float keep_hi) {
  const auto r = __builtin_amdgcn_permlane32_swap(__float_as_uint(keep_lo), __float_as_uint(keep_hi), false, false);
  return __uint_as_float(r[0]) + __uint_as_float(r[1]);
}
#define TIC(v)
#define ACC(a, x, y)
#define PUT(slot, a)
DI u64 ld_flag(const u64* p) { return __hip_atomic_load((const GAS u64*)p, __ATOMIC_RELAXED, __HIP_MEMORY_SCOPE_AGENT); }
DI void st_flag(u64* p, u64 v) { __hip_atomic_store((GAS u64*)p, v, __ATOMIC_RELAXED, __HIP_MEMORY_SCOPE_AGENT); }
DI void wait_flags(const u64* p, u64 tag) {
#pragma unroll 1
  for (int it = 0; it < 60000; ++it) {
    if (__all(ld_flag(p) == tag)) break;
    __builtin_amdgcn_s_sleep(4);
  }
  asm volatile("" ::: "memory");
}
DI void wait_flags2(const u64* p, const u64* p2, u64 tag) {
#pragma unroll 1
  for (int it = 0; it < 60000; ++it) {
    const u64 a = ld_flag(p), b = ld_flag(p2);
    if (__all(a == tag && b == tag)) break;
    __builtin_amdgcn_s_sleep(4);
  }
  asm volatile("" ::: "memory");
}
DI u64 read_tag(const char* ws) {
  u64 e;
  asm volatile("s_load_dwordx2 %0, %1, 0x0\n\ts_waitcnt lgkmcnt(0)" : "=s"(e) : "s"(ws + WS_CTRL) : "memory");
  return e * 6364136223846793005ull + 1442695040888963407ull;
}
DI void st16c(__amdgpu_buffer_rsrc_t r, unsigned off, u32x4 v) { __builtin_amdgcn_raw_buffer_store_b128(v, r, off, 0, 16); }
DI u32x4 ld16c(__amdgpu_buffer_rsrc_t r, unsigned off) { return __builtin_amdgcn_raw_buffer_load_b128(r, off, 0, 16); }

struct Params {
  const bf16_t *x, *wsr, *wsi, *wdr, *wdi, *wg, *wu, *wdn;
  bf16_t* out; char* ws;
};

constexpr int L_W = 49152, L_D = 98304, K1_TAB = 159744, LDS_BYTES = K1_TAB + 4096;
constexpr int T_RS = K1_TAB, T_RM = K1_TAB + 512, T_CS = K1_TAB + 1024, T_CD = K1_TAB + 2048, T_GO = K1_TAB + 3584, T_PF = K1_TAB + 3072;
constexpr int A_ST = 16384, B_ST = 20480, L_B = 4 * A_ST;

constexpr int NY = 16;
constexpr int F_Y = 147456, F_T = 106496, FPITCH = 176;

DI int f_red(int lw) { return (lw >> 1) * L_B + (lw & 1) * 20480; }

DI void helper(const Params& P, LAS char* smem, int lane, int lw, int mt, int ns, int split, int mt2, int nt) {
  constexpr int NL = 12;
  const bool TG = ns < 8;
  const int n8 = ns & 7, hcol0 = TG ? 128 * n8 : 1024 + 64 * n8;
  const int rr = lane >> 3, c8 = lane & 7;
  const unsigned lds0 = (unsigned)(size_t)smem;
  unsigned vX[4], vW[4], vD[5];
#pragma unroll
  for (int j = 0; j < 4; ++j) vX[j] = (32 * lw + 8 * j + rr) * 1280 + ((c8 ^ (TG ? rr : ((rr & 2) | ((j & 2) << 1)))) * 16) - j * 1024;
  const char* bx = (const char*)P.x + (size_t)mt * 128 * 1280;
  const unsigned ldsX = __builtin_amdgcn_readfirstlane(lds0 + lw * 4096);
  if (HD && TG) __builtin_amdgcn_s_sleep(HD);
  glds4<false>(ldsX, bx, vX[0], vX[1], vX[2], vX[3]);
  PIN();
#pragma unroll
  for (int j = 0; j < 4; ++j) vW[j] = (8 * j + rr) * 3072 + ((c8 ^ ((rr & 2) | ((j & 1) << 2))) * 16) - j * 1024;
#pragma unroll
  for (int j = 0; j < 5; ++j) vD[j] = (40 * lw + 8 * j + rr) * 1280 + ((c8 ^ rr) * 16) - (j & 3) * 1024;
  const char* bw = (const char*)((lw >> 1) ? P.wu : P.wg) + hcol0 * 2 + (TG ? (lw & 1) * 128 : (lw & 1) * (32 * 3072));
  const char* bd = (const char*)(n8 >= 4 ? P.wdi : P.wdr) + (size_t)(n8 & 3) * 160 * 1280;
  const unsigned ldsW = __builtin_amdgcn_readfirstlane(lds0 + L_W + (TG ? (lw & 1) * 49152 + (lw >> 1) * 8192 : lw * 4096));
  const unsigned ldsD = __builtin_amdgcn_readfirstlane(lds0 + L_D + lw * 5120);
  auto issue_a = [&](int kt) {
    const int buf = kt % 3;
    if (kt) glds4<false>(ldsX + buf * 16384, bx + kt * 128, vX[0], vX[1], vX[2], vX[3]);
    glds4<false>(ldsW + buf * 16384, bw + kt * (64 * 3072), vW[0], vW[1], vW[2], vW[3]);
  };
  auto issue_b = [&](int kt) {
    const int buf = kt % 3;
    if (TG) glds4<false>(ldsW + buf * 16384 + 4096, bw + kt * (64 * 3072) + 32 * 3072, vW[0], vW[1], vW[2], vW[3]);
    else glds5<false>(ldsD + buf * 20480, bd + kt * 128, vD[0], vD[1], vD[2], vD[3], vD[4]);
  };
  auto issue = [&](int kt) { issue_a(kt); issue_b(kt); };
  issue(0);
  issue_a(1);
  u64 tag;
  if (!S0E) issue_b(1);
  const char* pfb = (const char*)(split < 2 ? P.wsr : P.wsi) + (size_t)(mt2 * 128 + nt * 32) * 4096 + (split & 1) * 2048;
  const unsigned pfv = (lane & 31) * 4096 + (lw * 2 + (lane >> 5)) * 128;
  const int fcol = ((lane & 15) * 16 + ((P0M & 2) ? ns : n8)) * 4 + lw;
  const u64* fa = FLAG(C_F0 + ((TG && (lw > 0 || lane >= 32)) ? fcol : lw == 0 ? mt * 64 + (P0M & 1) * 32 + (lane & 31) : (((lane >> 1) & 15) * 16 + (P0M & 2) * 4 + n8) * 4 + 2 + (lane & 1)));
  u64 fav = 0;
  u32x4 cs[4];
#pragma unroll
  for (int e = 0; e < 4; ++e) asm volatile("" : "=v"(cs[e]));
  auto put_cs = [&]() {
    f32x4 c = (__builtin_bit_cast(f32x4, cs[0]) + __builtin_bit_cast(f32x4, cs[1])) + (__builtin_bit_cast(f32x4, cs[2]) + __builtin_bit_cast(f32x4, cs[3]));
#pragma unroll
    for (int e = 0; e < 4; ++e) { c[e] = xchg16(c[e], c[e]); c[e] = xchg32(c[e], c[e]); }
    if (lane < 16) *(LAS f32x4*)(smem + T_CS + lw * 256 + lane * 16) = c;
  };
  const int xg = ((mt2 ^ nt) & 1) * XG;
  const unsigned vL = lane * 16, vL4 = vL + 4096;
  const char* baseWD = P.ws + WS_WD + ((nt * 48 + lw) * 10 + (split >> 1) * 5) * 1024;
  const int wtask = (mt * 8 + n8) * 4 + lw;
  const bool wdo = TG && wtask < 480;
  unsigned wv[16];
#pragma unroll
  for (int e = 0; e < 16; ++e) asm volatile("" : "=v"(wv[e]));
  sfor<0, 8>([&](auto K) __attribute__((always_inline)) {
    constexpr int k = decltype(K)::v;
    if (S0E && k == 0) wait_v<8>(); else if (S0E == 2 && k == 1) wait_v<8>(); else if (k == WK + 1 && wdo) wait_v<12 + 16>(); else if (k == WK + 3 && wdo) wait_v<12 + 4>(); else if (TG) wait_v<12>(); else wait_v<13>();
    BAR();
    if (S0E && k == 0) issue_b(1);
    if (S0E == 2 && k == 1) issue_b(2);
    if (TG || lw < 2) {
      if (k == SK) asm volatile("global_load_dwordx2 %0, %1, off sc1" : "+v"(fav) : "v"(fa) : "memory");
      if (k == SK + 1) {
        asm volatile("" : "+v"(fav));
        if (!__all(fav == tag)) wait_flags(fa, tag);
        if (lw < (TG ? 1 : 2)) {
          const char* src = P.ws + (lw == 0 ? WS_RS + mt * 1024 : WS_CD + n8 * 640);
          asm volatile("s_mov_b32 m0, %0\n\ts_nop 0\n\tglobal_load_lds_dwordx4 %1, %2 sc1" :: "s"(lds0 + (lw == 0 ? T_RS : T_CD)), "v"(vL), "s"(src) : "memory");
        }
        if (TG) asm volatile("global_load_dwordx4 %0, %4, %5 sc1\n\tglobal_load_dwordx4 %1, %4, %5 offset:1024 sc1\n\tglobal_load_dwordx4 %2, %4, %5 offset:2048 sc1\n\t"
                             "global_load_dwordx4 %3, %4, %5 offset:3072 sc1"
                             : "+v"(cs[0]), "+v"(cs[1]), "+v"(cs[2]), "+v"(cs[3]) : "v"(vL), "s"(P.ws + WS_CS + (ns * 4 + lw) * 4096) : "memory");
      }
    }
    if (S0E == 2 && k == 0) issue_a(2); else issue(k + 2);
    if (k == WK && wdo) {
#pragma unroll
      for (int u = 0; u < 4; ++u) {
        const int task = wtask * 2 + (u >> 1);
        const unsigned od = (((task % 240) / 5) * 32 + (lane >> 4) * 4) * 1280 + ((task / 240) * 160 + (task % 5) * 32) * 2 + (lane & 15) * 4;
        const char* pd = (const char*)P.wdn + (u & 1) * (16 * 1280);
        asm volatile("global_load_dword %0, %4, %5\n\tglobal_load_dword %1, %4, %5 offset:1280\n\tglobal_load_dword %2, %4, %5 offset:2560\n\tglobal_load_dword %3, %4, %5 offset:3840"
                     : "+v"(wv[4 * u]), "+v"(wv[4 * u + 1]), "+v"(wv[4 * u + 2]), "+v"(wv[4 * u + 3]) : "v"(od), "s"(pd) : "memory");
      }
    }
    if (k == WK + 2 && wdo) {
      const __amdgpu_buffer_rsrc_t rw = __builtin_amdgcn_make_buffer_rsrc(P.ws, 0, WS_END, 0x00020000);
      const bool up = lane & 8;
#pragma unroll
      for (int u = 0; u < 2; ++u) {
        u32x4 wo[2];
#pragma unroll
        for (int e = 0; e < 4; ++e) {
          asm volatile("" : "+v"(wv[8 * u + 2 * e]), "+v"(wv[8 * u + 2 * e + 1]));
          const unsigned lo = __builtin_amdgcn_perm(wv[8 * u + 2 * e + 1], wv[8 * u + 2 * e], 0x05040100u), hi = __builtin_amdgcn_perm(wv[8 * u + 2 * e + 1], wv[8 * u + 2 * e], 0x07060302u);
          const unsigned t = __builtin_amdgcn_update_dpp(0, up ? lo : hi, 0x128, 0xf, 0xf, true);
          wo[0][e] = up ? t : lo;
          wo[1][e] = up ? hi : t;
        }
        const int task = wtask * 2 + u;
        const unsigned dst = WS_WD + (((task / 240) * 48 + (task % 240) / 5) * 10 + (task % 5) * 2) * 1024 + (2 * (lane & 7) + ((lane >> 3) & 1) + (lane & 48)) * 16;
        st16c(rw, dst, wo[0]); st16c(rw, dst + 1024, wo[1]);
      }
    }
    if (k == 0) tag = read_tag(P.ws);
    if ((TG || PFM == 0) && k == PH0 + PHS * (lw >> 1) + (mt & 1)) touch_lines(lds0 + T_PF, pfv, pfb);
    if (k == 7) touch_lines(lds0 + T_PF, pfv, pfb + ((TG || PFM == 0) ? 1024 : 0));
  });
  wait_v<NL>();
  BAR();
  wait_v<1>();
  BAR();
  if (wdo && lane == 0) st_flag(FLAG(C_FW + wtask), tag);
  if (TG) {
    asm volatile("" : "+v"(cs[0]), "+v"(cs[1]), "+v"(cs[2]), "+v"(cs[3]));
    put_cs();
  } else if (lw < 2) {
    wait_flags(FLAG(C_F0 + fcol), tag);
    const __amdgpu_buffer_rsrc_t rw = __builtin_amdgcn_make_buffer_rsrc(P.ws, 0, WS_END, 0x00020000);
#pragma unroll
    for (int i = 0; i < 4; ++i) cs[i] = ld16c(rw, WS_CS + (ns * 4 + lw) * 4096 + i * 1024 + lane * 16);
    put_cs();
  }
  if (lw == 0 && lane == 0) *(volatile LAS unsigned*)(smem + T_GO) = 0;
  WAIT_L0();
  if (!TG && PFM == 1) { if (lw < 2) __builtin_amdgcn_s_sleep(PFS); else __builtin_amdgcn_s_sleep(PFS + 24); touch_lines(lds0 + T_PF, pfv, pfb + 1024); }
  if (blockIdx.x == 64 && lw == 3) {
    wait_flags2(FLAG(C_F0 + lane * 4), FLAG(C_F0 + (64 + lane) * 4), tag);
    wait_flags2(FLAG(C_F0 + (128 + lane) * 4), FLAG(C_F0 + (192 + lane) * 4), tag);
    if (lane == 0) st_flag(FLAG(C_E), tag);
  }
  const u64* fab = FLAG(C_FAB + (nt * 64 + split * 16) * 2 + (lane & 31));
  unsigned vY[4];
#pragma unroll
  for (int j = 0; j < 4; ++j) vY[j] = (32 * lw + 8 * j + rr) * 4096 + ((c8 ^ rr) * 16) - j * 1024;
  const char* baseY = (const char*)(split < 2 ? P.wsr : P.wsi) + (size_t)mt2 * 128 * 4096 + (split & 1) * 2048;
  const char* baseAB = P.ws + WS_AB + (nt * 64 + split * 16) * B_ST + lw * 5120;
  const char* baseH = P.ws + WS_H + ((mt2 * 24 + (lw >> 1)) * 16 + (split & 1) * 8 + (lw & 1)) * 1024;
  const unsigned ldsA = ldsX, ldsB = __builtin_amdgcn_readfirstlane(lds0 + L_B + lw * 5120);
  auto issueYA = [&](int k, int yi) { glds4<false>(ldsA + (k & 3) * A_ST, baseY + (yi ^ xg) * 128, vY[0], vY[1], vY[2], vY[3]); };
  auto issueYB = [&](int k, int yi) { glds5<true>(ldsB + (k & 3) * B_ST, baseAB + (yi ^ xg) * B_ST, vL, vL, vL, vL, vL4); };
  BAR();
  __builtin_amdgcn_s_setprio(TPRIO);
  STAMP_AT(8);
  u64 fabv;
  fabv = ~tag;
  if (FABM == 1 || (FABM == 2 && lw == 0)) asm volatile("global_load_dwordx2 %0, %1, off sc1" : "+v"(fabv) : "v"(fab) : "memory");
  issueYA(0, 0); issueYA(1, 1);
  if (FABM) wait_v<8>();
  asm volatile("" : "+v"(fabv));
  const bool ab_seen = __all(fabv == tag);
  if (NAP == 4) { issueYA(2, 2); issueYA(3, 3); }
  if (lw == 0) {
    if (!ab_seen) wait_flags(fab, tag);
    if (lane == 0) *(volatile LAS unsigned*)(smem + T_GO) = 1;
  } else if (!ab_seen) {
#pragma unroll 1
    for (int it = 0; it < 4000000; ++it) {
      if (*(volatile LAS unsigned*)(smem + T_GO) != 0) break;
      __builtin_amdgcn_s_sleep(1);
    }
  }
  asm volatile("" ::: "memory");
  STAMP_AT(9);
  const u64* fh = FLAG(C_FH + mt2 * 64 + (lane & 31) * 2 + (split & 1));
  const int l36 = min(lane, 35);
  const int wt = (nt * 240 + ((l36 / 3) * 4 + lw) * 5 + (split >> 1) * 2 + l36 % 3) >> 1;
  const u64* fw = FLAG(C_FW + wt);
  u64 fhv = 0, fwv = 0;
  issueYB(0, 0);
  if (B0E) { wait_v<0>(); BAR(); issueYB(1, 1); } else { issueYB(1, 1); wait_v<5>(); BAR(); }
  if (NAP != 4) issueYA(2, 2);
  issueYB(2, 2);
  wait_v<(NAP == 4 ? 5 : 9)>(); BAR();
  __builtin_amdgcn_s_setprio(0);
  if (NAP != 4) issueYA(3, 3);
  issueYB(3, 3); issueYA(4, 4); issueYB(4, 4);
  const int ph = nt * 32 + lane, pw = (mt2 & 7) * 20 + min(max(lane - 32, 0), 19);
  const unsigned dstr = lane < 32 ? 2 * A_ST : 2 * B_ST;
  unsigned dpf = (lane < 32 ? WS_H + ((mt2 * 24 + (ph >> 6)) * 16 + (split & 1) * 8) * 1024 + (ph & 63) * 128
                            : WS_WD + ((nt * 48 + pw / 40) * 10 + (split >> 1) * 5) * 1024 + (pw % 40) * 128) + (DP0 + lw) * dstr;
#pragma unroll 1
  for (int k = 2; k < NY - 3; ++k) {
    if (k == DFK + 1) {
      wait_v<9>();
      asm volatile("" : "+v"(fhv), "+v"(fwv));
      if (!__all(fhv == tag && fwv == tag)) wait_flags2(fh, fw, tag);
    } else if (NAP == 4 && k == 2) wait_v<14>(); else wait_v<18>();
    BAR();
    if (k == DFK) asm volatile("global_load_dwordx2 %0, %2, off sc1\n\tglobal_load_dwordx2 %1, %3, off sc1" : "+v"(fhv), "+v"(fwv) : "v"(fh), "v"(fw) : "memory");
    issueYA(k + 3, k + 3); issueYB(k + 3, k + 3);
    if (PFM == 3 && !TG && k == 2 + (lw >> PFQ)) touch_lines(lds0 + T_PF, pfv, pfb + 1024);
    if (DPF) {
      const int n0 = (k - (DFK + 1)) * DPS;
      const int n = n0 + ((lw - n0) & 3);
      if (n0 >= 0 && n < n0 + DPS && DP0 + n < 12) { touch_lines<true>(lds0 + T_PF, dpf, P.ws); dpf += 4 * dstr; }
    }
  }
  bf16x8 GA[4][4], GB[4][5];
  f32x4 dac[4][5];
  auto loadD = [&](int i) {
    const int s = i & 3;
    const char* pa = baseH + (i ^ xg) * (2 * A_ST);
    const char* pb = baseWD + (i ^ xg) * (2 * B_ST);
    asm volatile("global_load_dwordx4 %0, %9, %12 sc1\n\tglobal_load_dwordx4 %4, %9, %11 sc1\n\tglobal_load_dwordx4 %5, %9, %11 offset:2048 sc1\n\t"
                 "global_load_dwordx4 %6, %10, %11 sc1\n\tglobal_load_dwordx4 %7, %10, %11 offset:2048 sc1\n\t"
                 "global_load_dwordx4 %1, %9, %12 offset:1024 sc1\n\tglobal_load_dwordx4 %2, %9, %12 offset:2048 sc1\n\t"
                 "global_load_dwordx4 %3, %9, %12 offset:3072 sc1\n\tglobal_load_dwordx4 %8, %10, %12 sc1"
                 : "=&v"(GB[s][0]), "=&v"(GB[s][1]), "=&v"(GB[s][2]), "=&v"(GB[s][3]), "=&v"(GA[s][0]), "=&v"(GA[s][1]), "=&v"(GA[s][2]), "=&v"(GA[s][3]), "=&v"(GB[s][4])
                 : "v"(vL), "v"(vL4), "s"(pa), "s"(pb) : "memory");
  };
  auto load1 = [&](int i, int n) {
    const int s = i & 3;
    const char* pa = baseH + (i ^ xg) * (2 * A_ST);
    const char* pb = baseWD + (i ^ xg) * (2 * B_ST);
    if (n < 4) asm volatile("global_load_dwordx4 %0, %1, %2 offset:%3 sc1" : "=&v"(GA[s][n]) : "v"(n < 2 ? vL : vL4), "s"(pa), "i"((n & 1) * 2048) : "memory");
    else asm volatile("global_load_dwordx4 %0, %1, %2 offset:%3 sc1" : "=&v"(GB[s][n - 4]) : "v"(n < 8 ? vL : vL4), "s"(pb), "i"(((n - 4) & 3) * 1024) : "memory");
  };
  auto group = [&](auto I) __attribute__((always_inline)) {
    constexpr int i = decltype(I)::v, s = i & 3;
    wait_v<(i == 0 ? 27 : i < 10 ? 18 : (11 - i) * 9)>();
    asm volatile("" : "+v"(GA[s][0]), "+v"(GA[s][1]), "+v"(GA[s][2]), "+v"(GA[s][3]), "+v"(GB[s][0]), "+v"(GB[s][1]), "+v"(GB[s][2]), "+v"(GB[s][3]), "+v"(GB[s][4]));
#pragma unroll
    for (int m = 0; m < 20; ++m) {
      const int r = (m & 4) ? 3 - (m & 3) : (m & 3);
      mf(dac[r][m >> 2], GB[s][m >> 2], GA[s][r], i == 0);
      if (i >= 1 && i + 3 < 12 && (m & 1) == 1 && m < 18) load1(i + 3, m >> 1);
    }
  };
  loadD(0);
  wait_v<27>(); BAR();
  loadD(1);
  wait_v<27>(); BAR();
  loadD(2);
  wait_v<27>(); BAR();
  loadD(3);
  __builtin_amdgcn_s_setprio(DPRIO);
  sfor<0, 12>(group);
  MFMA_DONE();
  after_mfma(dac);
#pragma unroll
  for (int r = 0; r < 4; ++r)
#pragma unroll
    for (int j = 0; j < 5; ++j) *(LAS f32x4*)(smem + f_red(lw) + (r * 5 + j) * 1024 + lane * 16) = dac[r][j];
  WAIT_L0();
  STAMP_AT(11);
  BAR();
  STAMP_AT(10);
}

template <bool TG>
DI void mfma_p1(const Params& P, LAS char* smem, int lane, int w, int mt, int ns, u64& tag) {
  const int n8 = ns & 7, hcol0 = TG ? 128 * n8 : 1024 + 64 * n8;
  const __amdgpu_buffer_rsrc_t rw = __builtin_amdgcn_make_buffer_rsrc(P.ws, 0, WS_END, 0x00020000);
  int q = lane >> 4, rho = lane & 15;
  const int rr = lane >> 3, c8 = lane & 7;
  u32x4 vx[5], vw[5], vm[10];
  constexpr bool DO_R = TG != ((P0M & 1) != 0), DO_W = TG, DO_M = TG != ((P0M & 2) != 0);
  if (DO_R) {
    const char* px = (const char*)P.x + (size_t)(mt * 128 + n8 * 16 + 4 * w + q) * 1280 + rho * 16;
#pragma unroll
    for (int i = 0; i < 5; ++i) vx[i] = *(const u32x4*)(px + i * 256);
  }
  if (DO_W) {
    const char* pw = (const char*)((w >> 1) ? P.wu : P.wg) + (size_t)(40 * mt + rr) * 3072 + (hcol0 + (w & 1) * 64) * 2 + c8 * 16;
#pragma unroll
    for (int i = 0; i < 5; ++i) vw[i] = *(const u32x4*)(pw + i * (8 * 3072));
  }
  if (DO_M) {
    if (w < 2) {
      const char* pm = (const char*)(w ? P.wu : P.wg) + (size_t)(40 * mt + rr) * 3072 + (1024 + 64 * n8) * 2 + c8 * 16;
#pragma unroll
      for (int i = 0; i < 5; ++i) vm[i] = *(const u32x4*)(pm + i * (8 * 3072));
    } else {
      const int e = (n8 & 3) * 160 + 10 * mt + 5 * (w - 2) + min(rr, 4);
      const char* pd = (const char*)(n8 >= 4 ? P.wdi : P.wdr) + (size_t)e * 1280 + c8 * 16;
#pragma unroll
      for (int i = 0; i < 10; ++i) vm[i] = *(const u32x4*)(pd + i * 128);
    }
  }
  tag = read_tag(P.ws);
  const int wm = w & 1, wn = w >> 1;
  constexpr int NC = TG ? 4 : 2;
  unsigned vX0 = TG ? wm * 8192 + rho * 128 + ((q ^ (rho & 7)) * 16)
                          : wm * 8192 + (8 * (rho >> 2) + (rho & 3)) * 128 + ((q ^ ((rho & 2) | ((rho >> 1) & 4))) * 16);
  unsigned vX1 = vX0 ^ 64;
  unsigned vW[NC];
  {
    const int qq = rho >> 2, p = lane & 3, swz = (qq & 2) | ((q & 1) << 2);
#pragma unroll
    for (int c = 0; c < NC; ++c) {
      const int cc = TG ? c : (2 * wn + c);
      vW[c] = L_W + (TG ? wn * 49152 : 0) + (8 * q + qq) * 128 + ((((2 * cc) ^ swz) + (p >> 1)) * 16) + 8 * (p & 1);
    }
  }
  unsigned vD0 = L_D + (80 * wn + rho) * 128 + ((q ^ (rho & 7)) * 16), vD1 = vD0 ^ 64;
  asm volatile("" : "+v"(vX0), "+v"(vX1));
  if (!TG) asm volatile("" : "+v"(vD0), "+v"(vD1));
#pragma unroll
  for (int c = 0; c < NC; ++c) asm volatile("" : "+v"(vW[c]));
  f32x4 aG[NC][4], aU[NC][4], aA[4][5];
  if (DO_R || DO_W || DO_M) {
    unsigned ONE2 = 0x3f803f80u, SLO = 0x00003f80u, SHI = 0x3f800000u;
    asm volatile("" : "+v"(ONE2), "+v"(SLO), "+v"(SHI));
    float rso = 0, c1 = 0, c2 = 0; unsigned off2 = 0;
    if (DO_R) {
      float sx[2] = {0, 0}, sq[2] = {0, 0};
#pragma unroll
      for (int i = 0; i < 5; ++i)
#pragma unroll
        for (int e = 0; e < 4; ++e) { sx[e & 1] = dot2(vx[i][e], ONE2, sx[e & 1]); sq[e & 1] = dot2(vx[i][e], vx[i][e], sq[e & 1]); }
      const float mean = sum16(sx[0] + sx[1]) * (1.0f / D);
      const float var = fmaxf(sum16(sq[0] + sq[1]) * (1.0f / D) - mean * mean, 0.f);
      const float sd = rbf(__builtin_amdgcn_sqrtf(rbf(rbf(var) + 1e-5f)));
      rso = (lane & 1) ? rbf(mean) : __builtin_amdgcn_rcpf(sd);
    }
    auto colsum = [&](const u32x4* v) -> float {
      float c[8] = {0, 0, 0, 0, 0, 0, 0, 0};
#pragma unroll
      for (int i = 0; i < 5; ++i)
#pragma unroll
        for (int e = 0; e < 4; ++e) { c[2 * e] = dot2(v[i][e], SLO, c[2 * e]); c[2 * e + 1] = dot2(v[i][e], SHI, c[2 * e + 1]); }
      float d4[4], d2[2];
#pragma unroll
      for (int e = 0; e < 4; ++e) d4[e] = xchg32(c[e], c[4 + e]);
#pragma unroll
      for (int e = 0; e < 2; ++e) d2[e] = xchg16(d4[e], d4[2 + e]);
      d2[0] = dpp_add<0x128>(d2[0]); d2[1] = dpp_add<0x128>(d2[1]);
      return (lane & 8) ? d2[1] : d2[0];
    };
    if (DO_W) c1 = colsum(vw);
    if (DO_M) {
      if (w < 2) {
        c2 = colsum(vm);
        off2 = WS_CS + ((((8 + n8) * 4 + w) * 16 + mt) * 64 + 8 * c8 + rr) * 4;
      } else {
        float a = 0, b = 0;
#pragma unroll
        for (int i = 0; i < 10; ++i) { a = dot2(vm[i][0], ONE2, a); b = dot2(vm[i][1], ONE2, b); a = dot2(vm[i][2], ONE2, a); b = dot2(vm[i][3], ONE2, b); }
        c2 = sum8(a + b);
        off2 = (c8 == 0 && rr < 5) ? WS_CD + (n8 * 160 + 10 * mt + 5 * (w - 2) + rr) * 4 : 0xfffffff0u;
      }
    }
    asm volatile("" : "+v"(c1), "+v"(c2), "+v"(rso));
    if (DO_W) __builtin_amdgcn_raw_buffer_store_b32(__float_as_uint(c1), rw, WS_CS + (((n8 * 4 + w) * 16 + mt) * 64 + 8 * c8 + rr) * 4, 0, 16);
    if (DO_M) __builtin_amdgcn_raw_buffer_store_b32(__float_as_uint(c2), rw, off2, 0, 16);
    if (DO_R) __builtin_amdgcn_raw_buffer_store_b32(__float_as_uint(rso), rw, rho < 2 ? WS_RS + mt * 1024 + rho * 512 + (n8 * 16 + 4 * w + q) * 4 : 0xfffffff0u, 0, 16);
  }
  bf16x8 FX[2][4], FD[2][5];
  s16x4 FW[2][2 * NC][2];
  constexpr int NR = TG ? 20 : 17, NM = TG ? 32 : 36;
  auto rd1 = [&](int s, int buf, int ks, int r) {
    if (r >= 2 && r < 6) {
      const int i = r - 2;
      FX[s][i] = *(LAS bf16x8*)(smem + (ks ? vX1 : vX0) + buf * 16384 + (TG ? i * 2048 : (i >> 1) * 4096 + (i & 1) * 512));
    } else if (r < 4 + 4 * NC) {
      const int h = r < 2 ? r : r - 4, f = h >> 1;
      FW[s][f][h & 1] = __builtin_amdgcn_ds_read_tr16_b64_v4i16((LAS s16x4*)(smem + vW[f >> 1] + buf * 16384 + ks * 4096 + (f & 1) * 8192 + (h & 1) * 512));
    } else {
      const int jj = r - (4 + 4 * NC);
      FD[s][jj] = *(LAS bf16x8*)(smem + (ks ? vD1 : vD0) + buf * 20480 + jj * 2048);
    }
  };
  auto mm1 = [&](int s, int m, bool first) {
    const int f = m >> 2, i = (f & 1) ? 3 - (m & 3) : (m & 3);
    if (f < 2 * NC) {
      const bf16x8 wf = __builtin_shufflevector(FW[s][f][0], FW[s][f][1], 0, 1, 2, 3, 4, 5, 6, 7);
      mf((f & 1) ? aU[f >> 1][i] : aG[f >> 1][i], wf, FX[s][i], first);
    } else mf(aA[i][f - 2 * NC], FX[s][i], FD[s][f - 2 * NC], first);
  };
  auto seg = [&](int s, bool first, int m0, int m1, int rk, int buf, int ks) __attribute__((always_inline)) {
    int r = 0;
#pragma unroll
    for (int m = m0; m < m1; ++m) {
      mm1(s, m, first);
      if (rk == 2 && r == 0) r = 2;
      if (rk == 2 && r == 6) r = 4 + 4 * NC;
      const int rend = rk == 0 ? 0 : rk == 3 ? 4 + 4 * NC : NR;
      if (r < rend && (TG ? (m % 5 != 4) : (rk == 3 || m % 3 != 2))) { rd1(s ^ 1, buf, ks, r); ++r; }
      PIN();
    }
  };
  BAR();
  STAMP_AT(1);
#pragma unroll
  for (int r = 0; r < NR; ++r) rd1(0, 0, 0, r);
  PIN();
  constexpr int KJ = TG ? 10 : 7;
  seg(0, true, 0, NM, 1, 0, 1);
  WAIT_L0(); BAR(); PIN();
  seg(1, false, 0, NM, 1, 1, 0);
  wait_v<0>();
  if (lane == 0) st_flag(FLAG(C_F0 + (mt * 16 + ns) * 4 + w), tag);
  WAIT_L0(); PIN();
#pragma unroll 1
  for (int it = 0; it < (KJ - 1) / 3; ++it) {
    sfor<1, 4>([&](auto K) __attribute__((always_inline)) {
      constexpr int k = decltype(K)::v;
      seg(0, false, 0, NM, 1, k % 3, 1);
      WAIT_L0();
      if (!TG || k < 3 || it < 2) BAR();
      PIN();
      seg(1, false, 0, NM, 1, (k + 1) % 3, 0);
      WAIT_L0(); PIN();
    });
  }
  if (!TG) sfor<7, 10>([&](auto K) __attribute__((always_inline)) {
    constexpr int k = decltype(K)::v;
    seg(0, false, 8 * NC, NM, 2, k % 3, 1);
    if (k < 9) { WAIT_L0(); BAR(); PIN(); }
    seg(1, false, 8 * NC, NM, k < 9 ? 2 : 3, (k + 1) % 3, 0);
    WAIT_L0(); PIN();
  });
  if (!TG) {
    MFMA_DONE();
    after_mfma(aA);
    asm volatile("" : "+v"(lane)); q = lane >> 4; rho = lane & 15;
    STAMP_AT(4);
    const float sgn = (n8 >= 4) ? -1.0f : 1.0f;
    float cs[5];
#pragma unroll
    for (int j = 0; j < 5; ++j) cs[j] = *(LAS float*)(smem + T_CD + (80 * wn + 16 * j + rho) * 4);
    const int kt = (n8 >= 4 ? 32 : 0) + mt * 2 + wm;
#pragma unroll
    for (int I = 0; I < 2; ++I) {
      f32x4 r4[2], m4[2];
#pragma unroll
      for (int t = 0; t < 2; ++t) {
        const int row = 64 * wm + 32 * I + 8 * q + 4 * t;
        r4[t] = *(LAS f32x4*)(smem + T_RS + row * 4) * sgn; m4[t] = *(LAS f32x4*)(smem + T_RM + row * 4);
      }
      const unsigned dst = WS_AB + ((((n8 & 3) * 64 + kt) * 2 + I) * 10 + 5 * wn) * 1024 + lane * 16;
#pragma unroll
      for (int j = 0; j < 5; ++j) { aA[2 * I][j] -= m4[0] * cs[j]; aA[2 * I + 1][j] -= m4[1] * cs[j]; }
      PIN();
#pragma unroll
      for (int j = 0; j < 5; ++j) { aA[2 * I][j] *= r4[0]; aA[2 * I + 1][j] *= r4[1]; }
      PIN();
#pragma unroll
      for (int j = 0; j < 5; ++j) {
        const f32x4 v0 = aA[2 * I][j], v1 = aA[2 * I + 1][j];
        const u32x4 o = {pk_bf16(v0[0], v0[1]), pk_bf16(v0[2], v0[3]), pk_bf16(v1[0], v1[1]), pk_bf16(v1[2], v1[3])};
        st16c(rw, dst + j * 1024, o);
      }
    }
    STAMP_AT(7);
    WAIT_L0(); PIN();
#pragma unroll
    for (int k = KJ; k < 10; ++k) {
      seg(0, false, 0, 8 * NC, 3, k % 3, 1);
      WAIT_L0(); PIN();
      if (k == KJ + AFL) {
        wait_v<0>();
        if (lane == 0) st_flag(FLAG(C_FAB + ((n8 & 3) * 64 + kt) * 2 + wn), tag);
        PIN();
      }
      seg(1, false, 0, 8 * NC, k < 9 ? 3 : 0, (k + 1) % 3, 0);
      WAIT_L0(); PIN();
    }
  }
  MFMA_DONE();
  after_mfma(aG); after_mfma(aU);
  asm volatile("" : "+v"(lane)); q = lane >> 4; rho = lane & 15;
  BAR();
  STAMP_AT(2);
  {
    const float K0 = -2.0f * 0.7978845608028654f * 1.4426950408889634f, K1 = K0 * 0.044715f;
    float mu[4], c0[4], c1[4], v2[4];
#pragma unroll
    for (int i = 0; i < 4; ++i) {
      const int row = TG ? 64 * wm + 16 * i + rho : 64 * wm + 32 * (i >> 1) + 8 * (rho >> 2) + 4 * (i & 1) + (rho & 3);
      const float rs = *(LAS float*)(smem + T_RS + row * 4);
      mu[i] = *(LAS float*)(smem + T_RM + row * 4);
      c0[i] = K0 * rs; c1[i] = K1 * rs * rs * rs; v2[i] = __builtin_amdgcn_rcpf(rs * rs);
    }
#pragma unroll
    for (int J = 0; J < NC / 2; ++J) {
      f32x4 cg[2], cu[2];
#pragma unroll
      for (int t = 0; t < 2; ++t) {
        const int c = 2 * J + t;
        const int o = TG ? (wn * 256 + c * 64 + q * 16) : ((2 * wn + c) * 64 + q * 16);
        cg[t] = *(LAS f32x4*)(smem + T_CS + o);
        cu[t] = *(LAS f32x4*)(smem + T_CS + (TG ? 512 : 256) + o);
      }
#pragma unroll
      for (int ip = 0; ip < 2; ++ip) {
        f32x4 g[4], z[4], p[4];
#define EACH(stmt) _Pragma("unroll") for (int n = 0; n < 4; ++n) { const int i = 2 * ip + (n >> 1), t = n & 1; (void)i; (void)t; stmt; } PIN()
        EACH(g[n] = aG[2 * J + t][i] - cg[t] * mu[i]);
        EACH(z[n] = g[n] * g[n]);
        EACH(z[n] = z[n] * c1[i] + c0[i]);
        EACH(z[n] = z[n] * g[n]);
        EACH(p[n] = aU[2 * J + t][i] - cu[t] * mu[i]; z[n][0] = __builtin_amdgcn_exp2f(z[n][0]); z[n][1] = __builtin_amdgcn_exp2f(z[n][1]);
             z[n][2] = __builtin_amdgcn_exp2f(z[n][2]); z[n][3] = __builtin_amdgcn_exp2f(z[n][3]));
        EACH(z[n] = z[n] * v2[i] + v2[i]);
        EACH(p[n] = p[n] * g[n]; z[n][0] = __builtin_amdgcn_rcpf(z[n][0]); z[n][1] = __builtin_amdgcn_rcpf(z[n][1]);
             z[n][2] = __builtin_amdgcn_rcpf(z[n][2]); z[n][3] = __builtin_amdgcn_rcpf(z[n][3]));
        EACH(p[n] = p[n] * z[n]);
#undef EACH
#pragma unroll
        for (int u = 0; u < 2; ++u) {
          const int i = 2 * ip + u;
          const u32x4 o = {pk_bf16(p[2 * u][0], p[2 * u][1]), pk_bf16(p[2 * u][2], p[2 * u][3]), pk_bf16(p[2 * u + 1][0], p[2 * u + 1][1]), pk_bf16(p[2 * u + 1][2], p[2 * u + 1][3])};
          const int f0 = hcol0 + (TG ? wn * 64 + J * 32 : wn * 32);
          const int rb = TG ? 4 * wm + i : 4 * wm + 2 * (i >> 1) + (rho >> 3), pos = TG ? lane : 8 * ((rho >> 2) & 1) + 4 * (i & 1) + (rho & 3) + 16 * q;
          st16c(rw, WS_H + (((mt * 24 + (f0 >> 6)) * 8 + rb) * 2 + ((f0 >> 5) & 1)) * 1024 + pos * 16, o);
        }
      }
    }
  }
  STAMP_AT(3);
}

DI void mfma_p2(const Params& P, LAS char* smem, int lane, int w, int split, int mt, int nt, int mt1, int ns, u64 tag) {
  asm volatile("" : "+v"(lane));
  const int wm = w & 1, wn = w >> 1, q = lane >> 4, r15 = lane & 15;
  unsigned vY0 = wm * 8192 + r15 * 128 + ((q ^ (r15 & 7)) * 16), vY1 = vY0 ^ 64;
  unsigned vB0 = L_B + wn * 5120 + lane * 16, vB2 = vB0 + 2 * B_ST;
  asm volatile("" : "+v"(vY0), "+v"(vY1), "+v"(vB0), "+v"(vB2));
  const __amdgpu_buffer_rsrc_t rw = __builtin_amdgcn_make_buffer_rsrc(P.ws, 0, WS_END, 0x00020000);
  const int tile = mt * 4 + nt;
  f32x4 acc[4][5];
  bf16x8 FA[2][4], FB[2][5];
  auto rdy = [&](int s, int k, int ks, int r) {
    const int buf = k & 3;
    if (r >= 1 && r < 5) FA[s][r - 1] = *(LAS bf16x8*)(smem + (ks ? vY1 : vY0) + buf * A_ST + (r - 1) * 2048);
    else { const int j = r ? r - 4 : 0; FB[s][j] = *(LAS bf16x8*)(smem + (buf >= 2 ? vB2 : vB0) + (buf & 1) * B_ST + ks * 10240 + j * 1024); }
  };
  auto seg = [&](auto K, auto HF) __attribute__((always_inline)) {
    constexpr int k = decltype(K)::v, hf = decltype(HF)::v, rk = k + hf, rh = hf ^ 1;
    int r = 0;
#pragma unroll
    for (int m = 0; m < 20; ++m) {
      const int i = (m & 4) ? 3 - (m & 3) : (m & 3);
      mf(acc[i][m >> 2], FB[hf][m >> 2], FA[hf][i], k == 0 && hf == 0);
      if (rk < NY && r < 9 && m % 3 != 2) { rdy(rh, rk, rh, r); ++r; }
      PIN();
    }
  };
  auto publish = [&](int ip) {
    u32x4 o[5];
#pragma unroll
    for (int t = 0; t < 2; ++t) {
      const int i = 2 * ip + t;
#pragma unroll
      for (int j = 0; j < 5; ++j) {
        const int n = j < 4 ? 2 * t + (j >> 1) : 4, e = j < 4 ? (j & 1) * 2 : 2 * t;
        o[n][e] = pk_bf16(acc[i][j][0], acc[i][j][1]); o[n][e + 1] = pk_bf16(acc[i][j][2], acc[i][j][3]);
      }
    }
    if (w == split) {
#pragma unroll
      for (int n = 0; n < 5; ++n) *(LAS u32x4*)(smem + F_Y + (n < 4 ? ip * 4096 + n * 1024 : 8192 + ip * 1024) + lane * 16) = o[n];
    } else {
      const unsigned dst = WS_P + ((tile * 4 + w) * 3 + (split ^ w) - 1) * 10240 + lane * 16;
#pragma unroll
      for (int n = 0; n < 5; ++n) if (!NOPUB) st16c(rw, dst + (n < 4 ? ip * 4096 + n * 1024 : 8192 + ip * 1024), o[n]);
    }
  };
#pragma unroll
  for (int i = 0; i < 4; ++i)
#pragma unroll
    for (int j = 0; j < 5; ++j) acc[i][j] = (f32x4){0, 0, 0, 0};
  after_mfma(acc);
  BAR();
  STAMP_AT(5);
#pragma unroll
  for (int r = 0; r < 9; ++r) rdy(0, 0, 0, r);
  WAIT_L0(); PIN();
#pragma unroll 1
  for (int it = 0; it < NY / 4; ++it) {
    sfor<4, 8>([&](auto K) __attribute__((always_inline)) {
      constexpr int k = decltype(K)::v;
      seg(K, IC<0>{});
      if (k == 4 + HFL && it == 0) {
        wait_v<0>();
        if (lane == 0) st_flag(FLAG(C_FH + mt1 * 64 + ns * 4 + w), tag);
      }
      WAIT_L0();
      if (k < (DE == 3 ? 6 : 7) || it < NY / 4 - 1) BAR();
      PIN();
      seg(K, IC<1>{});
      WAIT_L0(); PIN();
    });
  }
  MFMA_DONE();
  after_mfma(acc);
  __builtin_amdgcn_s_setprio(3);
  publish(0); publish(1);
  if (HOLD) BAR();
  if (w != split) {
    wait_v<0>();
    if (lane == 0) st_flag(FLAG(C_FP + (tile * 4 + w) * 4 + (split ^ w) - 1), tag);
  }
  const int orow = mt * 128 + (split & 1) * 64 + w * 16, ocol = nt * 160 + (split >> 1) * 80;
  u32x4 xv[3];
  unsigned tr[3], oo[3];
#pragma unroll
  for (int i = 0; i < 3; ++i) {
    const int n = min(i * 64 + lane, 159);
    const unsigned off = ((orow + n / 10) * D + ocol + (n % 10) * 8) * 2;
    xv[i] = *(const u32x4*)((const char*)P.x + off);
    tr[i] = F_T + w * (16 * FPITCH) + (n / 10) * FPITCH + (n % 10) * 16;
    oo[i] = i * 64 + lane < 160 ? off : 0xfffffff0u;
  }
  if (w == split) {
    const u64* fp = FLAG(C_FP + (tile * 4 + split) * 4 + min(lane, 2));
    __builtin_amdgcn_s_sleep(PD0);
#pragma unroll 1
    for (int it = 0; it < 40000; ++it) {
      if (__all(ld_flag(fp) == tag)) break;
      __builtin_amdgcn_s_sleep(PD1);
    }
    WAIT_L0();
    if (lane == 0) *(volatile LAS unsigned*)(smem + T_GO) = 2;
  } else {
    __builtin_amdgcn_s_setprio(0);
#pragma unroll 1
    for (int it = 0; it < 4000000; ++it) {
      if (*(volatile LAS unsigned*)(smem + T_GO) == 2) break;
      __builtin_amdgcn_s_sleep(4);
    }
    __builtin_amdgcn_s_setprio(3);
  }
  asm volatile("" ::: "memory");
  f32x4 ys[5];
  {
    u32x4 pa[3][2]; u32x2 pb[3];
#pragma unroll
    for (int s = 0; s < 3; ++s) {
      const unsigned src = WS_P + ((tile * 4 + split) * 3 + s) * 10240 + lane * 16;
      pa[s][0] = ld16c(rw, src + w * 2048); pa[s][1] = ld16c(rw, src + w * 2048 + 1024);
      pb[s] = __builtin_amdgcn_raw_buffer_load_b64(rw, src + 8192 + (w >> 1) * 1024 + (w & 1) * 8, 0, 16);
    }
#pragma unroll
    for (int j = 0; j < 5; ++j) {
#pragma unroll
      for (int s = 0; s < 3; ++s) {
        const unsigned lo = j < 4 ? pa[s][j >> 1][(j & 1) * 2] : pb[s][0], hi = j < 4 ? pa[s][j >> 1][(j & 1) * 2 + 1] : pb[s][1];
        const f32x4 v = {bflo(lo), bfhi(lo), bflo(hi), bfhi(hi)};
        ys[j] = s ? ys[j] + v : v;
      }
      asm volatile("" : "+v"(ys[j]));
    }
  }
  {
    LAS char* po = smem + F_Y + lane * 16;
    const u32x4 oa0 = *(LAS u32x4*)(po + w * 2048), oa1 = *(LAS u32x4*)(po + w * 2048 + 1024);
    const u32x2 ob = *(LAS u32x2*)(po + 8192 + (w >> 1) * 1024 + (w & 1) * 8);
#pragma unroll
    for (int j = 0; j < 5; ++j) {
      const unsigned l0 = j < 2 ? oa0[(j & 1) * 2] : j < 4 ? oa1[(j & 1) * 2] : ob[0], h0 = j < 2 ? oa0[(j & 1) * 2 + 1] : j < 4 ? oa1[(j & 1) * 2 + 1] : ob[1];
      ys[j] += (f32x4){bflo(l0), bfhi(l0), bflo(h0), bfhi(h0)};
      asm volatile("" : "+v"(ys[j]));
    }
  }
  unsigned tw = F_T + w * (16 * FPITCH) + r15 * FPITCH + q * 8, rd0 = w * 5120 + lane * 16, rd2 = rd0 + L_B;
  asm volatile("" : "+v"(tw), "+v"(rd0), "+v"(rd2), "+v"(tr[0]), "+v"(tr[1]), "+v"(tr[2]), "+v"(oo[0]), "+v"(oo[1]), "+v"(oo[2]));
  STAMP_AT(6);
  BAR();
  f32x4 dd[4][5];
#pragma unroll
  for (int j = 0; j < 5; ++j)
#pragma unroll
    for (int u = 0; u < 4; ++u) dd[u][j] = *(LAS f32x4*)(smem + ((u & 2) ? rd2 : rd0) + (u & 1) * 20480 + j * 1024);
#pragma unroll
  for (int j = 0; j < 5; ++j) {
    const f32x4 d = (dd[0][j] + dd[1][j]) + (dd[2][j] + dd[3][j]);
    f32x4 e = d * -1.4426950408889634f;
#pragma unroll
    for (int r = 0; r < 4; ++r) e[r] = __builtin_amdgcn_exp2f(e[r]);
    e = e + 1.0f;
#pragma unroll
    for (int r = 0; r < 4; ++r) e[r] = __builtin_amdgcn_rcpf(e[r]);
    e = e * d * ys[j];
    *(LAS u32x2*)(smem + tw + j * 32) = (u32x2){pk_bf16(e[0], e[1]), pk_bf16(e[2], e[3])};
  }
  WAIT_L0();
  const __amdgpu_buffer_rsrc_t ro = __builtin_amdgcn_make_buffer_rsrc(P.out, 0, S * D * 2, 0x00020000);
#pragma unroll
  for (int i = 0; i < 3; ++i) {
    const u32x4 v = *(LAS u32x4*)(smem + tr[i]);
    u32x4 o;
#pragma unroll
    for (int d = 0; d < 4; ++d) o[d] = pk_bf16(bflo(v[d]) + bflo(xv[i][d]), bfhi(v[d]) + bfhi(xv[i][d]));
    __builtin_amdgcn_raw_buffer_store_b128(o, ro, oo[i], 0, 0);
  }
}

__global__ __launch_bounds__(512, 2)
void fused(Params P) {
  extern __shared__ __attribute__((aligned(16))) char smem_[];
  LAS char* smem = (LAS char*)smem_;
  const int tid = threadIdx.x, lane = tid & 63, w = __builtin_amdgcn_readfirstlane(tid >> 6);
  const int bid = blockIdx.x, xcd = bid & 7, idx = bid >> 3;
  u64 tag;
  STAMP_AT(0);
  if (CPF && w == 3 && (idx & 8)) {
    u64 pc; unsigned len;
    asm volatile("s_getpc_b64 %0\n.Lcodepf:\n\ts_mov_b32 %1, code_end_marker-.Lcodepf" : "=s"(pc), "=s"(len));
    const unsigned off = ((idx & 7) * 64 + lane) * (CPF == 2 ? 64 : 128) + (CPF == 2 ? (idx >> 4) * 32768 : 0);
    if (off + 4 <= len) touch_lines((unsigned)(size_t)smem + T_PF, off, (const char*)pc);
  }
  const int mt1 = (xcd & 1) * 8 + (idx & 7), j = idx >> 3, ns = (j & 1) * 8 + (xcd >> 1) * 2 + (j >> 1);
  const int split = xcd >> 1, mt2 = (xcd & 1) * 8 + (idx >> 2), nt = idx & 3;
  if (w < 4) {
    if (j & 1) mfma_p1<false>(P, smem, lane, w, mt1, ns, tag); else mfma_p1<true>(P, smem, lane, w, mt1, ns, tag);
    mfma_p2(P, smem, lane, w, split, mt2, nt, mt1, ns, tag);
  } else {
    helper(P, smem, lane, w & 3, mt1, ns, split, mt2, nt);
    __builtin_amdgcn_s_waitcnt(0);
  }
}

extern "C" __global__ void code_end_marker() {}
extern "C" void kernel_launch(void* const* d_in, const int* in_sizes, int n_in, void* d_out, int out_size, void* d_ws, size_t ws_size, hipStream_t stream) {
  Params P;
  P.x = (const bf16_t*)d_in[0]; P.wsr = (const bf16_t*)d_in[1]; P.wsi = (const bf16_t*)d_in[2];
  P.wdr = (const bf16_t*)d_in[3]; P.wdi = (const bf16_t*)d_in[4];
  P.wg = (const bf16_t*)d_in[6]; P.wu = (const bf16_t*)d_in[7]; P.wdn = (const bf16_t*)d_in[8];
  P.out = (bf16_t*)d_out; P.ws = (char*)d_ws;
  (void)hipFuncSetAttribute((const void*)fused, hipFuncAttributeMaxDynamicSharedMemorySize, LDS_BYTES);
  fused<<<256, 512, LDS_BYTES, stream>>>(P);
}
```

```cpp
#include <hip/hip_runtime.h>
#include <hip/hip_bf16.h>
typedef unsigned short bf16_t;
typedef unsigned long long u64;
typedef short bf16x8 __attribute__((ext_vector_type(8)));
typedef short s16x4 __attribute__((ext_vector_type(4)));
typedef float f32x4 __attribute__((ext_vector_type(4)));
typedef float f32x2 __attribute__((ext_vector_type(2)));
typedef unsigned u32x4 __attribute__((ext_vector_type(4)));
typedef unsigned u32x2 __attribute__((ext_vector_type(2)));
typedef __bf16 bf16x2_t __attribute__((ext_vector_type(2)));
#define LAS __attribute__((address_space(3)))
#define GAS __attribute__((address_space(1)))
#define DI __device__ __forceinline__

constexpr int S = 2048, D = 640, F = 1536;
constexpr unsigned WS_AB = 0, WS_H = WS_AB + 2 * S * D * 2, WS_P = WS_H + S * F * 2, WS_WD = WS_P + 3 * S * D * 2, WS_RS = WS_WD + F * D * 2,
                   WS_CS = WS_RS + S * 8, WS_CD = WS_CS + 16 * 4 * 16 * 64 * 4, WS_CTRL = WS_CD + 8192, WS_END = WS_CTRL + 5120 * 128;
constexpr int C_E = 0, C_F0 = 1, C_FAB = C_F0 + 1024, C_FH = C_FAB + 512, C_FW = C_FH + 1024, C_FP = C_FW + 512;
#define FLAG(i) ((u64*)(P.ws + WS_CTRL) + (i) * 16)

DI unsigned pk_bf16(float lo, float hi) { f32x2 v = {lo, hi}; return __builtin_bit_cast(unsigned, __builtin_convertvector(v, bf16x2_t)); }
DI float rbf(float f) { return __uint_as_float(pk_bf16(f, f) << 16); }
DI float bflo(unsigned u) { return __uint_as_float(u << 16); }
DI float bfhi(unsigned u) { return __uint_as_float(u & 0xffff0000u); }
DI float dot2(unsigned a, unsigned b, float c) { return __builtin_amdgcn_fdot2_f32_bf16(__builtin_bit_cast(bf16x2_t, a), __builtin_bit_cast(bf16x2_t, b), c, false); }
#define GLDS4(POL) asm volatile("s_mov_b32 m0, %0\n\ts_nop 0\n\tglobal_load_lds_dwordx4 %2, %1" POL "\n\tglobal_load_lds_dwordx4 %3, %1 offset:1024" POL \
    "\n\tglobal_load_lds_dwordx4 %4, %1 offset:2048" POL "\n\tglobal_load_lds_dwordx4 %5, %1 offset:3072" POL \
    :: "s"(lds), "s"(base), "v"(v0), "v"(v1), "v"(v2), "v"(v3) : "memory")
#define GLDS5(POL) asm volatile("s_mov_b32 m0, %0\n\ts_nop 0\n\tglobal_load_lds_dwordx4 %2, %1" POL "\n\tglobal_load_lds_dwordx4 %3, %1 offset:1024" POL \
    "\n\tglobal_load_lds_dwordx4 %4, %1 offset:2048" POL "\n\tglobal_load_lds_dwordx4 %5, %1 offset:3072" POL \
    "\n\ts_add_u32 m0, %0, 0x1000\n\ts_nop 0\n\tglobal_load_lds_dwordx4 %6, %1" POL \
    :: "s"(lds), "s"(base), "v"(v0), "v"(v1), "v"(v2), "v"(v3), "v"(v4) : "memory", "scc")
template <bool DEV> DI void glds4(unsigned lds, const char* base, unsigned v0, unsigned v1, unsigned v2, unsigned v3) {
  if (DEV) GLDS4(" sc1"); else GLDS4("");
}
template <bool DEV> DI void glds5(unsigned lds, const char* base, unsigned v0, unsigned v1, unsigned v2, unsigned v3, unsigned v4) {
  if (DEV) GLDS5(" sc1"); else GLDS5("");
}
DI void touch_lines(unsigned lds, unsigned voff, const char* base) {
  asm volatile("s_mov_b32 m0, %0\n\ts_nop 0\n\tglobal_load_lds_dword %1, %2" :: "s"(lds), "v"(voff), "s"(base) : "memory");
}
template <int N> DI void wait_v() { asm volatile("s_waitcnt vmcnt(%0)" :: "i"(N) : "memory"); }
#define WAIT_L0() do { __builtin_amdgcn_s_waitcnt(0xC07F); asm volatile("" ::: "memory"); } while (0)
#define BAR() do { asm volatile("" ::: "memory"); __builtin_amdgcn_s_barrier(); asm volatile("" ::: "memory"); } while (0)
#define PIN() __builtin_amdgcn_sched_barrier(0)
template <int V> struct IC { static constexpr int v = V; };
template <int I, int N, class Fn> DI void sfor(Fn&& f) { if constexpr (I < N) { f(IC<I>{}); sfor<I + 1, N>(f); } }
DI void mf(f32x4& c, const bf16x8& a, const bf16x8& b, bool first) {
  if (first) asm volatile("v_mfma_f32_16x16x32_bf16 %0, %1, %2, 0" : "=&v"(c) : "v"(a), "v"(b));
  else asm volatile("v_mfma_f32_16x16x32_bf16 %0, %1, %2, %0" : "+v"(c) : "v"(a), "v"(b));
}
#define MFMA_DONE() asm volatile("s_nop 15")
template <int A, int B> DI void after_mfma(f32x4 (&c)[A][B]) {
#pragma unroll
  for (int i = 0; i < A; ++i)
#pragma unroll
    for (int j = 0; j < B; ++j) asm volatile("" : "+v"(c[i][j]));
}

template <int CTRL> DI float dpp_add(float v) { return v + __builtin_bit_cast(float, __builtin_amdgcn_update_dpp(0, __builtin_bit_cast(int, v), CTRL, 0xf, 0xf, true)); }
DI float sum8(float v) { v = dpp_add<0xB1>(v); v = dpp_add<0x4E>(v); return dpp_add<0x141>(v); }
DI float sum16(float v) { return dpp_add<0x140>(sum8(v)); }
DI float xchg16(float keep_lo, float keep_hi) {
  const auto r = __builtin_amdgcn_permlane16_swap(__float_as_uint(keep_lo), __float_as_uint(keep_hi), false, false);
  return __uint_as_float(r[0]) + __uint_as_float(r[1]);
}
DI float xchg32(float keep_lo, float keep_hi) {
  const auto r = __builtin_amdgcn_permlane32_swap(__float_as_uint(keep_lo), __float_as_uint(keep_hi), false, false);
  return __uint_as_float(r[0]) + __uint_as_float(r[1]);
}
DI u64 ld_flag(const u64* p) { return __hip_atomic_load((const GAS u64*)p, __ATOMIC_RELAXED, __HIP_MEMORY_SCOPE_AGENT); }
DI void st_flag(u64* p, u64 v) { __hip_atomic_store((GAS u64*)p, v, __ATOMIC_RELAXED, __HIP_MEMORY_SCOPE_AGENT); }
DI void wait_flags(const u64* p, u64 tag) {
#pragma unroll 1
  for (int it = 0; it < 60000; ++it) {
    if (__all(ld_flag(p) == tag)) break;
    __builtin_amdgcn_s_sleep(4);
  }
  asm volatile("" ::: "memory");
}
DI void wait_flags2(const u64* p, const u64* p2, u64 tag) {
#pragma unroll 1
  for (int it = 0; it < 60000; ++it) {
    const u64 a = ld_flag(p), b = ld_flag(p2);
    if (__all(a == tag && b == tag)) break;
    __builtin_amdgcn_s_sleep(4);
  }
  asm volatile("" ::: "memory");
}
DI u64 read_tag(const char* ws) {
  u64 e;
  asm volatile("s_load_dwordx2 %0, %1, 0x0\n\ts_waitcnt lgkmcnt(0)" : "=s"(e) : "s"(ws + WS_CTRL) : "memory");
  return e * 6364136223846793005ull + 1442695040888963407ull;
}
DI void st16c(__amdgpu_buffer_rsrc_t r, unsigned off, u32x4 v) { __builtin_amdgcn_raw_buffer_store_b128(v, r, off, 0, 16); }
DI u32x4 ld16c(__amdgpu_buffer_rsrc_t r, unsigned off) { return __builtin_amdgcn_raw_buffer_load_b128(r, off, 0, 16); }

struct Params {
  const bf16_t *x, *wsr, *wsi, *wdr, *wdi, *wg, *wu, *wdn;
  bf16_t* out; char* ws;
};

constexpr int L_W = 49152, L_D = 98304, K1_TAB = 159744, LDS_BYTES = K1_TAB + 4096;
constexpr int T_RS = K1_TAB, T_RM = K1_TAB + 512, T_CS = K1_TAB + 1024, T_CD = K1_TAB + 2048, T_GO = K1_TAB + 3584, T_PF = K1_TAB + 3072;
constexpr int A_ST = 16384, B_ST = 20480, L_B = 4 * A_ST;

constexpr int NY = 16;
constexpr int F_Y = 147456, F_T = 106496, FPITCH = 176;

DI int f_red(int lw) { return (lw >> 1) * L_B + (lw & 1) * 20480; }

DI void helper(const Params& P, LAS char* smem, int lane, int lw, int mt, int ns, int split, int mt2, int nt) {
  const bool TG = ns < 8;
  const int n8 = ns & 7, hcol0 = TG ? 128 * n8 : 1024 + 64 * n8;
  const int rr = lane >> 3, c8 = lane & 7;
  const unsigned lds0 = (unsigned)(size_t)smem;
  unsigned vX[4], vW[4], vD[5];
#pragma unroll
  for (int j = 0; j < 4; ++j) vX[j] = (32 * lw + 8 * j + rr) * 1280 + ((c8 ^ (TG ? rr : ((rr & 2) | ((j & 2) << 1)))) * 16) - j * 1024;
  const char* bx = (const char*)P.x + (size_t)mt * 128 * 1280;
  const unsigned ldsX = __builtin_amdgcn_readfirstlane(lds0 + lw * 4096);
  glds4<false>(ldsX, bx, vX[0], vX[1], vX[2], vX[3]);
  PIN();
#pragma unroll
  for (int j = 0; j < 4; ++j) vW[j] = (8 * j + rr) * 3072 + ((c8 ^ ((rr & 2) | ((j & 1) << 2))) * 16) - j * 1024;
#pragma unroll
  for (int j = 0; j < 5; ++j) vD[j] = (40 * lw + 8 * j + rr) * 1280 + ((c8 ^ rr) * 16) - (j & 3) * 1024;
  const char* bw = (const char*)((lw >> 1) ? P.wu : P.wg) + hcol0 * 2 + (TG ? (lw & 1) * 128 : (lw & 1) * (32 * 3072));
  const char* bd = (const char*)(n8 >= 4 ? P.wdi : P.wdr) + (size_t)(n8 & 3) * 160 * 1280;
  const unsigned ldsW = __builtin_amdgcn_readfirstlane(lds0 + L_W + (TG ? (lw & 1) * 49152 + (lw >> 1) * 8192 : lw * 4096));
  const unsigned ldsD = __builtin_amdgcn_readfirstlane(lds0 + L_D + lw * 5120);
  auto issue_a = [&](int kt) {
    const int buf = kt % 3;
    if (kt) glds4<false>(ldsX + buf * 16384, bx + kt * 128, vX[0], vX[1], vX[2], vX[3]);
    glds4<false>(ldsW + buf * 16384, bw + kt * (64 * 3072), vW[0], vW[1], vW[2], vW[3]);
  };
  auto issue_b = [&](int kt) {
    const int buf = kt % 3;
    if (TG) glds4<false>(ldsW + buf * 16384 + 4096, bw + kt * (64 * 3072) + 32 * 3072, vW[0], vW[1], vW[2], vW[3]);
    else glds5<false>(ldsD + buf * 20480, bd + kt * 128, vD[0], vD[1], vD[2], vD[3], vD[4]);
  };
  auto issue = [&](int kt) { issue_a(kt); issue_b(kt); };
  issue(0);
  issue_a(1);
  u64 tag;
  const char* pfb = (const char*)(split < 2 ? P.wsr : P.wsi) + (size_t)(mt2 * 128 + nt * 32) * 4096 + (split & 1) * 2048;
  const unsigned pfv = (lane & 31) * 4096 + (lw * 2 + (lane >> 5)) * 128;
  const int fcol = ((lane & 15) * 16 + n8) * 4 + lw;
  const u64* fa = FLAG(C_F0 + ((TG && (lw > 0 || lane >= 32)) ? fcol : lw == 0 ? mt * 64 + (lane & 31) : (((lane >> 1) & 15) * 16 + n8) * 4 + 2 + (lane & 1)));
  u64 fav = 0;
  u32x4 cs[4];
#pragma unroll
  for (int e = 0; e < 4; ++e) asm volatile("" : "=v"(cs[e]));
  auto put_cs = [&]() {
    f32x4 c = (__builtin_bit_cast(f32x4, cs[0]) + __builtin_bit_cast(f32x4, cs[1])) + (__builtin_bit_cast(f32x4, cs[2]) + __builtin_bit_cast(f32x4, cs[3]));
#pragma unroll
    for (int e = 0; e < 4; ++e) { c[e] = xchg16(c[e], c[e]); c[e] = xchg32(c[e], c[e]); }
    if (lane < 16) *(LAS f32x4*)(smem + T_CS + lw * 256 + lane * 16) = c;
  };
  const int xg = ((mt2 ^ nt) & 1) * 2;
  const unsigned vL = lane * 16, vL4 = vL + 4096;
  const char* baseWD = P.ws + WS_WD + ((nt * 48 + lw) * 10 + (split >> 1) * 5) * 1024;
  const int wtask = (mt * 8 + n8) * 4 + lw;
  const bool wdo = TG && wtask < 480;
  unsigned wv[16];
#pragma unroll
  for (int e = 0; e < 16; ++e) asm volatile("" : "=v"(wv[e]));
  constexpr int SK = 5, WK = 2;
  sfor<0, 8>([&](auto K) __attribute__((always_inline)) {
    constexpr int k = decltype(K)::v;
    if (k < 2) wait_v<8>(); else if (k == WK + 1 && wdo) wait_v<12 + 16>(); else if (TG) wait_v<12>(); else wait_v<13>();
    BAR();
    if (k < 2) issue_b(k + 1);
    if (TG || lw < 2) {
      if (k == SK) asm volatile("global_load_dwordx2 %0, %1, off sc1" : "+v"(fav) : "v"(fa) : "memory");
      if (k == SK + 1) {
        asm volatile("" : "+v"(fav));
        if (!__all(fav == tag)) wait_flags(fa, tag);
        if (lw < (TG ? 1 : 2)) {
          const char* src = P.ws + (lw == 0 ? WS_RS + mt * 1024 : WS_CD + n8 * 640);
          asm volatile("s_mov_b32 m0, %0\n\ts_nop 0\n\tglobal_load_lds_dwordx4 %1, %2 sc1" :: "s"(lds0 + (lw == 0 ? T_RS : T_CD)), "v"(lw == 0 ? vL : min(vL, 624u)), "s"(src) : "memory");
        }
        if (TG) asm volatile("global_load_dwordx4 %0, %4, %5 sc1\n\tglobal_load_dwordx4 %1, %4, %5 offset:1024 sc1\n\tglobal_load_dwordx4 %2, %4, %5 offset:2048 sc1\n\t"
                             "global_load_dwordx4 %3, %4, %5 offset:3072 sc1"
                             : "+v"(cs[0]), "+v"(cs[1]), "+v"(cs[2]), "+v"(cs[3]) : "v"(vL), "s"(P.ws + WS_CS + (ns * 4 + lw) * 4096) : "memory");
      }
    }
    if (k == 0) issue_a(2); else issue(k + 2);
    if (k == WK && wdo) {
#pragma unroll
      for (int u = 0; u < 4; ++u) {
        const int task = wtask * 2 + (u >> 1);
        const unsigned od = (((task % 240) / 5) * 32 + (lane >> 4) * 4) * 1280 + ((task / 240) * 160 + (task % 5) * 32) * 2 + (lane & 15) * 4;
        const char* pd = (const char*)P.wdn + (u & 1) * (16 * 1280);
        asm volatile("global_load_dword %0, %4, %5\n\tglobal_load_dword %1, %4, %5 offset:1280\n\tglobal_load_dword %2, %4, %5 offset:2560\n\tglobal_load_dword %3, %4, %5 offset:3840"
                     : "+v"(wv[4 * u]), "+v"(wv[4 * u + 1]), "+v"(wv[4 * u + 2]), "+v"(wv[4 * u + 3]) : "v"(od), "s"(pd) : "memory");
      }
    }
    if (k == WK + 2 && wdo) {
      const __amdgpu_buffer_rsrc_t rw = __builtin_amdgcn_make_buffer_rsrc(P.ws, 0, WS_END, 0x00020000);
      const bool up = lane & 8;
#pragma unroll
      for (int u = 0; u < 2; ++u) {
        u32x4 wo[2];
#pragma unroll
        for (int e = 0; e < 4; ++e) {
          asm volatile("" : "+v"(wv[8 * u + 2 * e]), "+v"(wv[8 * u + 2 * e + 1]));
          const unsigned lo = __builtin_amdgcn_perm(wv[8 * u + 2 * e + 1], wv[8 * u + 2 * e], 0x05040100u), hi = __builtin_amdgcn_perm(wv[8 * u + 2 * e + 1], wv[8 * u + 2 * e], 0x07060302u);
          const unsigned t = __builtin_amdgcn_update_dpp(0, up ? lo : hi, 0x128, 0xf, 0xf, true);
          wo[0][e] = up ? t : lo;
          wo[1][e] = up ? hi : t;
        }
        const int task = wtask * 2 + u;
        const unsigned dst = WS_WD + (((task / 240) * 48 + (task % 240) / 5) * 10 + (task % 5) * 2) * 1024 + (2 * (lane & 7) + ((lane >> 3) & 1) + (lane & 48)) * 16;
        st16c(rw, dst, wo[0]); st16c(rw, dst + 1024, wo[1]);
      }
    }
    if (k == 0) tag = read_tag(P.ws);
    if (TG && k == 4 * (lw >> 1) + (mt & 1)) touch_lines(lds0 + T_PF, pfv, pfb);
    if (k == 7) touch_lines(lds0 + T_PF, pfv, pfb + (TG ? 1024 : 0));
  });
  wait_v<12>();
  BAR();
  wait_v<1>();
  BAR();
  if (TG) {
    asm volatile("" : "+v"(cs[0]), "+v"(cs[1]), "+v"(cs[2]), "+v"(cs[3]));
    put_cs();
  } else if (lw < 2) {
    wait_flags(FLAG(C_F0 + fcol), tag);
    const __amdgpu_buffer_rsrc_t rw = __builtin_amdgcn_make_buffer_rsrc(P.ws, 0, WS_END, 0x00020000);
#pragma unroll
    for (int i = 0; i < 4; ++i) cs[i] = ld16c(rw, WS_CS + (ns * 4 + lw) * 4096 + i * 1024 + lane * 16);
    put_cs();
  }
  if (lw == 0 && lane == 0) *(volatile LAS unsigned*)(smem + T_GO) = 0;
  WAIT_L0();
  if (blockIdx.x == 64 && lw == 3) {
    wait_flags2(FLAG(C_F0 + lane * 4), FLAG(C_F0 + (64 + lane) * 4), tag);
    wait_flags2(FLAG(C_F0 + (128 + lane) * 4), FLAG(C_F0 + (192 + lane) * 4), tag);
    if (lane == 0) st_flag(FLAG(C_E), tag);
  }
  const u64* fab = FLAG(C_FAB + (nt * 64 + split * 16) * 2 + (lane & 31));
  unsigned vY[4];
#pragma unroll
  for (int j = 0; j < 4; ++j) vY[j] = (32 * lw + 8 * j + rr) * 4096 + ((c8 ^ rr) * 16) - j * 1024;
  const char* baseY = (const char*)(split < 2 ? P.wsr : P.wsi) + (size_t)mt2 * 128 * 4096 + (split & 1) * 2048;
  const char* baseAB = P.ws + WS_AB + (nt * 64 + split * 16) * B_ST + lw * 5120;
  const char* baseH = P.ws + WS_H + ((mt2 * 24 + (lw >> 1)) * 16 + (split & 1) * 8 + (lw & 1)) * 1024;
  const unsigned ldsA = ldsX, ldsB = __builtin_amdgcn_readfirstlane(lds0 + L_B + lw * 5120);
  auto issueYA = [&](int k, int yi) { glds4<false>(ldsA + (k & 3) * A_ST, baseY + (yi ^ xg) * 128, vY[0], vY[1], vY[2], vY[3]); };
  auto issueYB = [&](int k, int yi) { glds5<true>(ldsB + (k & 3) * B_ST, baseAB + (yi ^ xg) * B_ST, vL, vL, vL, vL, vL4); };
  BAR();
  __builtin_amdgcn_s_setprio(3);
  issueYA(0, 0); issueYA(1, 1);
  if (lw == 0) {
    wait_flags(fab, tag);
    if (lane == 0) *(volatile LAS unsigned*)(smem + T_GO) = 1;
  } else {
#pragma unroll 1
    for (int it = 0; it < 4000000; ++it) {
      if (*(volatile LAS unsigned*)(smem + T_GO) != 0) break;
      __builtin_amdgcn_s_sleep(1);
    }
  }
  if (wdo) wait_v<0>();
  asm volatile("" ::: "memory");
  const u64* fh = FLAG(C_FH + mt2 * 64 + (lane & 31) * 2 + (split & 1));
  const int l36 = min(lane, 35);
  const int wt = (nt * 240 + ((l36 / 3) * 4 + lw) * 5 + (split >> 1) * 2 + l36 % 3) >> 1;
  const u64* fw = FLAG(C_FW + wt);
  u64 fhv = 0, fwv = 0;
  issueYB(0, 0); issueYB(1, 1);
  wait_v<5>(); BAR();
  issueYA(2, 2); issueYB(2, 2);
  wait_v<9>(); BAR();
  __builtin_amdgcn_s_setprio(0);
  issueYA(3, 3); issueYB(3, 3); issueYA(4, 4); issueYB(4, 4);
  if (wdo && lane == 0) st_flag(FLAG(C_FW + wtask), tag);
#pragma unroll 1
  for (int k = 2; k < NY - 3; ++k) {
    wait_v<18>();
    BAR();
    if (k == 9) asm volatile("global_load_dwordx2 %0, %2, off sc1\n\tglobal_load_dwordx2 %1, %3, off sc1" : "+v"(fhv), "+v"(fwv) : "v"(fh), "v"(fw) : "memory");
    issueYA(k + 3, k + 3); issueYB(k + 3, k + 3);
    if (!TG && k == 2 + (lw >> 1)) touch_lines(lds0 + T_PF, pfv, pfb + 1024);
  }
  asm volatile("" : "+v"(fhv), "+v"(fwv));
  if (!__all(fhv == tag && fwv == tag)) wait_flags2(fh, fw, tag);
  bf16x8 GA[4][4], GB[4][5];
  f32x4 dac[4][5];
  auto load1 = [&](int i, int n) {
    const int s = i & 3;
    const char* pa = baseH + (i ^ xg) * (2 * A_ST);
    const char* pb = baseWD + (i ^ xg) * (2 * B_ST);
    if (n < 4) asm volatile("global_load_dwordx4 %0, %1, %2 offset:%3 sc1" : "=&v"(GA[s][n]) : "v"(n < 2 ? vL : vL4), "s"(pa), "i"((n & 1) * 2048) : "memory");
    else asm volatile("global_load_dwordx4 %0, %1, %2 offset:%3 sc1" : "=&v"(GB[s][n - 4]) : "v"(n < 8 ? vL : vL4), "s"(pb), "i"(((n - 4) & 3) * 1024) : "memory");
  };
  auto loadD = [&](int i) {
#pragma unroll
    for (int n = 0; n < 9; ++n) load1(i, n);
  };
  auto group = [&](auto I) __attribute__((always_inline)) {
    constexpr int i = decltype(I)::v, s = i & 3;
    wait_v<(i == 0 ? 27 : i < 10 ? 18 : (11 - i) * 9)>();
    asm volatile("" : "+v"(GA[s][0]), "+v"(GA[s][1]), "+v"(GA[s][2]), "+v"(GA[s][3]), "+v"(GB[s][0]), "+v"(GB[s][1]), "+v"(GB[s][2]), "+v"(GB[s][3]), "+v"(GB[s][4]));
#pragma unroll
    for (int m = 0; m < 20; ++m) {
      const int r = (m & 4) ? 3 - (m & 3) : (m & 3);
      mf(dac[r][m >> 2], GB[s][m >> 2], GA[s][r], i == 0);
      if (i >= 1 && i + 3 < 12 && (m & 1) == 1 && m < 18) load1(i + 3, m >> 1);
    }
  };
  loadD(0);
  wait_v<27>(); BAR();
  loadD(1);
  wait_v<27>(); BAR();
  loadD(2);
  wait_v<27>(); BAR();
  loadD(3);
  __builtin_amdgcn_s_setprio(2);
  sfor<0, 12>(group);
  MFMA_DONE();
  after_mfma(dac);
#pragma unroll
  for (int r = 0; r < 4; ++r)
#pragma unroll
    for (int j = 0; j < 5; ++j) *(LAS f32x4*)(smem + f_red(lw) + (r * 5 + j) * 1024 + lane * 16) = dac[r][j];
  WAIT_L0();
  BAR();
}

template <bool TG>
DI void mfma_p1(const Params& P, LAS char* smem, int lane, int w, int mt, int ns, u64& tag) {
  const int n8 = ns & 7, hcol0 = TG ? 128 * n8 : 1024 + 64 * n8;
  const __amdgpu_buffer_rsrc_t rw = __builtin_amdgcn_make_buffer_rsrc(P.ws, 0, WS_END, 0x00020000);
  int q = lane >> 4, rho = lane & 15;
  const int rr = lane >> 3, c8 = lane & 7;
  u32x4 vx[5], vw[5], vm[10];
  if (TG) {
    const char* px = (const char*)P.x + (size_t)(mt * 128 + n8 * 16 + 4 * w + q) * 1280 + rho * 16;
#pragma unroll
    for (int i = 0; i < 5; ++i) vx[i] = *(const u32x4*)(px + i * 256);
    const char* pw = (const char*)((w >> 1) ? P.wu : P.wg) + (size_t)(40 * mt + rr) * 3072 + (hcol0 + (w & 1) * 64) * 2 + c8 * 16;
#pragma unroll
    for (int i = 0; i < 5; ++i) vw[i] = *(const u32x4*)(pw + i * (8 * 3072));
    if (w < 2) {
      const char* pm = (const char*)(w ? P.wu : P.wg) + (size_t)(40 * mt + rr) * 3072 + (1024 + 64 * n8) * 2 + c8 * 16;
#pragma unroll
      for (int i = 0; i < 5; ++i) vm[i] = *(const u32x4*)(pm + i * (8 * 3072));
    } else {
      const int e = (n8 & 3) * 160 + 10 * mt + 5 * (w - 2) + min(rr, 4);
      const char* pd = (const char*)(n8 >= 4 ? P.wdi : P.wdr) + (size_t)e * 1280 + c8 * 16;
#pragma unroll
      for (int i = 0; i < 10; ++i) vm[i] = *(const u32x4*)(pd + i * 128);
    }
  }
  tag = read_tag(P.ws);
  const int wm = w & 1, wn = w >> 1;
  constexpr int NC = TG ? 4 : 2;
  unsigned vX0 = TG ? wm * 8192 + rho * 128 + ((q ^ (rho & 7)) * 16)
                          : wm * 8192 + (8 * (rho >> 2) + (rho & 3)) * 128 + ((q ^ ((rho & 2) | ((rho >> 1) & 4))) * 16);
  unsigned vX1 = vX0 ^ 64;
  unsigned vW[NC];
  {
    const int qq = rho >> 2, p = lane & 3, swz = (qq & 2) | ((q & 1) << 2);
#pragma unroll
    for (int c = 0; c < NC; ++c) {
      const int cc = TG ? c : (2 * wn + c);
      vW[c] = L_W + (TG ? wn * 49152 : 0) + (8 * q + qq) * 128 + ((((2 * cc) ^ swz) + (p >> 1)) * 16) + 8 * (p & 1);
    }
  }
  unsigned vD0 = L_D + (80 * wn + rho) * 128 + ((q ^ (rho & 7)) * 16), vD1 = vD0 ^ 64;
  asm volatile("" : "+v"(vX0), "+v"(vX1));
  if (!TG) asm volatile("" : "+v"(vD0), "+v"(vD1));
#pragma unroll
  for (int c = 0; c < NC; ++c) asm volatile("" : "+v"(vW[c]));
  f32x4 aG[NC][4], aU[NC][4], aA[4][5];
  if (TG) {
    unsigned ONE2 = 0x3f803f80u, SLO = 0x00003f80u, SHI = 0x3f800000u;
    asm volatile("" : "+v"(ONE2), "+v"(SLO), "+v"(SHI));
    float rso = 0, c1 = 0, c2 = 0; unsigned off2 = 0;
    {
      float sx[2] = {0, 0}, sq[2] = {0, 0};
#pragma unroll
      for (int i = 0; i < 5; ++i)
#pragma unroll
        for (int e = 0; e < 4; ++e) { sx[e & 1] = dot2(vx[i][e], ONE2, sx[e & 1]); sq[e & 1] = dot2(vx[i][e], vx[i][e], sq[e & 1]); }
      const float mean = sum16(sx[0] + sx[1]) * (1.0f / D);
      const float var = fmaxf(sum16(sq[0] + sq[1]) * (1.0f / D) - mean * mean, 0.f);
      const float sd = rbf(__builtin_amdgcn_sqrtf(rbf(rbf(var) + 1e-5f)));
      rso = (lane & 1) ? rbf(mean) : __builtin_amdgcn_rcpf(sd);
    }
    auto colsum = [&](const u32x4* v) -> float {
      float c[8] = {0, 0, 0, 0, 0, 0, 0, 0};
#pragma unroll
      for (int i = 0; i < 5; ++i)
#pragma unroll
        for (int e = 0; e < 4; ++e) { c[2 * e] = dot2(v[i][e], SLO, c[2 * e]); c[2 * e + 1] = dot2(v[i][e], SHI, c[2 * e + 1]); }
      float d4[4], d2[2];
#pragma unroll
      for (int e = 0; e < 4; ++e) d4[e] = xchg32(c[e], c[4 + e]);
#pragma unroll
      for (int e = 0; e < 2; ++e) d2[e] = xchg16(d4[e], d4[2 + e]);
      d2[0] = dpp_add<0x128>(d2[0]); d2[1] = dpp_add<0x128>(d2[1]);
      return (lane & 8) ? d2[1] : d2[0];
    };
    c1 = colsum(vw);
    {
      if (w < 2) {
        c2 = colsum(vm);
        off2 = WS_CS + ((((8 + n8) * 4 + w) * 16 + mt) * 64 + 8 * c8 + rr) * 4;
      } else {
        float a = 0, b = 0;
#pragma unroll
        for (int i = 0; i < 10; ++i) { a = dot2(vm[i][0], ONE2, a); b = dot2(vm[i][1], ONE2, b); a = dot2(vm[i][2], ONE2, a); b = dot2(vm[i][3], ONE2, b); }
        c2 = sum8(a + b);
        off2 = (c8 == 0 && rr < 5) ? WS_CD + (n8 * 160 + 10 * mt + 5 * (w - 2) + rr) * 4 : 0xfffffff0u;
      }
    }
    asm volatile("" : "+v"(c1), "+v"(c2), "+v"(rso));
    __builtin_amdgcn_raw_buffer_store_b32(__float_as_uint(c1), rw, WS_CS + (((n8 * 4 + w) * 16 + mt) * 64 + 8 * c8 + rr) * 4, 0, 16);
    __builtin_amdgcn_raw_buffer_store_b32(__float_as_uint(c2), rw, off2, 0, 16);
    __builtin_amdgcn_raw_buffer_store_b32(__float_as_uint(rso), rw, rho < 2 ? WS_RS + mt * 1024 + rho * 512 + (n8 * 16 + 4 * w + q) * 4 : 0xfffffff0u, 0, 16);
  }
  bf16x8 FX[2][4], FD[2][5];
  s16x4 FW[2][2 * NC][2];
  constexpr int NR = TG ? 20 : 17, NM = TG ? 32 : 36;
  auto rd1 = [&](int s, int buf, int ks, int r) {
    if (r >= 2 && r < 6) {
      const int i = r - 2;
      FX[s][i] = *(LAS bf16x8*)(smem + (ks ? vX1 : vX0) + buf * 16384 + (TG ? i * 2048 : (i >> 1) * 4096 + (i & 1) * 512));
    } else if (r < 4 + 4 * NC) {
      const int h = r < 2 ? r : r - 4, f = h >> 1;
      FW[s][f][h & 1] = __builtin_amdgcn_ds_read_tr16_b64_v4i16((LAS s16x4*)(smem + vW[f >> 1] + buf * 16384 + ks * 4096 + (f & 1) * 8192 + (h & 1) * 512));
    } else {
      const int jj = r - (4 + 4 * NC);
      FD[s][jj] = *(LAS bf16x8*)(smem + (ks ? vD1 : vD0) + buf * 20480 + jj * 2048);
    }
  };
  auto mm1 = [&](int s, int m, bool first) {
    const int f = m >> 2, i = (f & 1) ? 3 - (m & 3) : (m & 3);
    if (f < 2 * NC) {
      const bf16x8 wf = __builtin_shufflevector(FW[s][f][0], FW[s][f][1], 0, 1, 2, 3, 4, 5, 6, 7);
      mf((f & 1) ? aU[f >> 1][i] : aG[f >> 1][i], wf, FX[s][i], first);
    } else mf(aA[i][f - 2 * NC], FX[s][i], FD[s][f - 2 * NC], first);
  };
  auto seg = [&](int s, bool first, int m0, int m1, int rk, int buf, int ks) __attribute__((always_inline)) {
    int r = 0;
#pragma unroll
    for (int m = m0; m < m1; ++m) {
      mm1(s, m, first);
      if (rk == 2 && r == 0) r = 2;
      if (rk == 2 && r == 6) r = 4 + 4 * NC;
      const int rend = rk == 0 ? 0 : rk == 3 ? 4 + 4 * NC : NR;
      if (r < rend && (TG ? (m % 5 != 4) : (rk == 3 || m % 3 != 2))) { rd1(s ^ 1, buf, ks, r); ++r; }
      PIN();
    }
  };
  BAR();
#pragma unroll
  for (int r = 0; r < NR; ++r) rd1(0, 0, 0, r);
  PIN();
  constexpr int KJ = TG ? 10 : 7;
  seg(0, true, 0, NM, 1, 0, 1);
  WAIT_L0(); BAR(); PIN();
  seg(1, false, 0, NM, 1, 1, 0);
  wait_v<0>();
  if (lane == 0) st_flag(FLAG(C_F0 + (mt * 16 + ns) * 4 + w), tag);
  WAIT_L0(); PIN();
#pragma unroll 1
  for (int it = 0; it < (KJ - 1) / 3; ++it) {
    sfor<1, 4>([&](auto K) __attribute__((always_inline)) {
      constexpr int k = decltype(K)::v;
      seg(0, false, 0, NM, 1, k % 3, 1);
      WAIT_L0();
      if (!TG || k < 3 || it < 2) BAR();
      PIN();
      seg(1, false, 0, NM, 1, (k + 1) % 3, 0);
      WAIT_L0(); PIN();
    });
  }
  if (!TG) sfor<7, 10>([&](auto K) __attribute__((always_inline)) {
    constexpr int k = decltype(K)::v;
    seg(0, false, 8 * NC, NM, 2, k % 3, 1);
    if (k < 9) { WAIT_L0(); BAR(); PIN(); }
    seg(1, false, 8 * NC, NM, k < 9 ? 2 : 3, (k + 1) % 3, 0);
    WAIT_L0(); PIN();
  });
  if (!TG) {
    MFMA_DONE();
    after_mfma(aA);
    asm volatile("" : "+v"(lane)); q = lane >> 4; rho = lane & 15;
    const float sgn = (n8 >= 4) ? -1.0f : 1.0f;
    float cs[5];
#pragma unroll
    for (int j = 0; j < 5; ++j) cs[j] = *(LAS float*)(smem + T_CD + (80 * wn + 16 * j + rho) * 4);
    const int kt = (n8 >= 4 ? 32 : 0) + mt * 2 + wm;
#pragma unroll
    for (int I = 0; I < 2; ++I) {
      f32x4 r4[2], m4[2];
#pragma unroll
      for (int t = 0; t < 2; ++t) {
        const int row = 64 * wm + 32 * I + 8 * q + 4 * t;
        r4[t] = *(LAS f32x4*)(smem + T_RS + row * 4) * sgn; m4[t] = *(LAS f32x4*)(smem + T_RM + row * 4);
      }
      const unsigned dst = WS_AB + ((((n8 & 3) * 64 + kt) * 2 + I) * 10 + 5 * wn) * 1024 + lane * 16;
#pragma unroll
      for (int j = 0; j < 5; ++j) { aA[2 * I][j] -= m4[0] * cs[j]; aA[2 * I + 1][j] -= m4[1] * cs[j]; }
      PIN();
#pragma unroll
      for (int j = 0; j < 5; ++j) { aA[2 * I][j] *= r4[0]; aA[2 * I + 1][j] *= r4[1]; }
      PIN();
#pragma unroll
      for (int j = 0; j < 5; ++j) {
        const f32x4 v0 = aA[2 * I][j], v1 = aA[2 * I + 1][j];
        const u32x4 o = {pk_bf16(v0[0], v0[1]), pk_bf16(v0[2], v0[3]), pk_bf16(v1[0], v1[1]), pk_bf16(v1[2], v1[3])};
        st16c(rw, dst + j * 1024, o);
      }
    }
    WAIT_L0(); PIN();
#pragma unroll
    for (int k = KJ; k < 10; ++k) {
      seg(0, false, 0, 8 * NC, 3, k % 3, 1);
      WAIT_L0(); PIN();
      if (k == 9) {
        wait_v<0>();
        if (lane == 0) st_flag(FLAG(C_FAB + ((n8 & 3) * 64 + kt) * 2 + wn), tag);
        PIN();
      }
      seg(1, false, 0, 8 * NC, k < 9 ? 3 : 0, (k + 1) % 3, 0);
      WAIT_L0(); PIN();
    }
  }
  MFMA_DONE();
  after_mfma(aG); after_mfma(aU);
  asm volatile("" : "+v"(lane)); q = lane >> 4; rho = lane & 15;
  BAR();
  {
    const float K0 = -2.0f * 0.7978845608028654f * 1.4426950408889634f, K1 = K0 * 0.044715f;
    float mu[4], c0[4], c1[4], v2[4];
#pragma unroll
    for (int i = 0; i < 4; ++i) {
      const int row = TG ? 64 * wm + 16 * i + rho : 64 * wm + 32 * (i >> 1) + 8 * (rho >> 2) + 4 * (i & 1) + (rho & 3);
      const float rs = *(LAS float*)(smem + T_RS + row * 4);
      mu[i] = *(LAS float*)(smem + T_RM + row * 4);
      c0[i] = K0 * rs; c1[i] = K1 * rs * rs * rs; v2[i] = __builtin_amdgcn_rcpf(rs * rs);
    }
#pragma unroll
    for (int J = 0; J < NC / 2; ++J) {
      f32x4 cg[2], cu[2];
#pragma unroll
      for (int t = 0; t < 2; ++t) {
        const int c = 2 * J + t;
        const int o = TG ? (wn * 256 + c * 64 + q * 16) : ((2 * wn + c) * 64 + q * 16);
        cg[t] = *(LAS f32x4*)(smem + T_CS + o);
        cu[t] = *(LAS f32x4*)(smem + T_CS + (TG ? 512 : 256) + o);
      }
#pragma unroll
      for (int ip = 0; ip < 2; ++ip) {
        f32x4 g[4], z[4], p[4];
#define EACH(stmt) _Pragma("unroll") for (int n = 0; n < 4; ++n) { const int i = 2 * ip + (n >> 1), t = n & 1; (void)i; (void)t; stmt; } PIN()
        EACH(g[n] = aG[2 * J + t][i] - cg[t] * mu[i]);
        EACH(z[n] = g[n] * g[n]);
        EACH(z[n] = z[n] * c1[i] + c0[i]);
        EACH(z[n] = z[n] * g[n]);
        EACH(p[n] = aU[2 * J + t][i] - cu[t] * mu[i]; z[n][0] = __builtin_amdgcn_exp2f(z[n][0]); z[n][1] = __builtin_amdgcn_exp2f(z[n][1]);
             z[n][2] = __builtin_amdgcn_exp2f(z[n][2]); z[n][3] = __builtin_amdgcn_exp2f(z[n][3]));
        EACH(z[n] = z[n] * v2[i] + v2[i]);
        EACH(p[n] = p[n] * g[n]; z[n][0] = __builtin_amdgcn_rcpf(z[n][0]); z[n][1] = __builtin_amdgcn_rcpf(z[n][1]);
             z[n][2] = __builtin_amdgcn_rcpf(z[n][2]); z[n][3] = __builtin_amdgcn_rcpf(z[n][3]));
        EACH(p[n] = p[n] * z[n]);
#undef EACH
#pragma unroll
        for (int u = 0; u < 2; ++u) {
          const int i = 2 * ip + u;
          const u32x4 o = {pk_bf16(p[2 * u][0], p[2 * u][1]), pk_bf16(p[2 * u][2], p[2 * u][3]), pk_bf16(p[2 * u + 1][0], p[2 * u + 1][1]), pk_bf16(p[2 * u + 1][2], p[2 * u + 1][3])};
          const int f0 = hcol0 + (TG ? wn * 64 + J * 32 : wn * 32);
          const int rb = TG ? 4 * wm + i : 4 * wm + 2 * (i >> 1) + (rho >> 3), pos = TG ? lane : 8 * ((rho >> 2) & 1) + 4 * (i & 1) + (rho & 3) + 16 * q;
          st16c(rw, WS_H + (((mt * 24 + (f0 >> 6)) * 8 + rb) * 2 + ((f0 >> 5) & 1)) * 1024 + pos * 16, o);
        }
      }
    }
  }
}

DI void mfma_p2(const Params& P, LAS char* smem, int lane, int w, int split, int mt, int nt, int mt1, int ns, u64 tag) {
  asm volatile("" : "+v"(lane));
  const int wm = w & 1, wn = w >> 1, q = lane >> 4, r15 = lane & 15;
  unsigned vY0 = wm * 8192 + r15 * 128 + ((q ^ (r15 & 7)) * 16), vY1 = vY0 ^ 64;
  unsigned vB0 = L_B + wn * 5120 + lane * 16, vB2 = vB0 + 2 * B_ST;
  asm volatile("" : "+v"(vY0), "+v"(vY1), "+v"(vB0), "+v"(vB2));
  const __amdgpu_buffer_rsrc_t rw = __builtin_amdgcn_make_buffer_rsrc(P.ws, 0, WS_END, 0x00020000);
  const int tile = mt * 4 + nt;
  f32x4 acc[4][5];
  bf16x8 FA[2][4], FB[2][5];
  auto rdy = [&](int s, int k, int ks, int r) {
    const int buf = k & 3;
    if (r >= 1 && r < 5) FA[s][r - 1] = *(LAS bf16x8*)(smem + (ks ? vY1 : vY0) + buf * A_ST + (r - 1) * 2048);
    else { const int j = r ? r - 4 : 0; FB[s][j] = *(LAS bf16x8*)(smem + (buf >= 2 ? vB2 : vB0) + (buf & 1) * B_ST + ks * 10240 + j * 1024); }
  };
  auto seg = [&](auto K, auto HF) __attribute__((always_inline)) {
    constexpr int k = decltype(K)::v, hf = decltype(HF)::v, rk = k + hf, rh = hf ^ 1;
    int r = 0;
#pragma unroll
    for (int m = 0; m < 20; ++m) {
      const int i = (m & 4) ? 3 - (m & 3) : (m & 3);
      mf(acc[i][m >> 2], FB[hf][m >> 2], FA[hf][i], false);
      if (r < 9 && m % 3 != 2) { rdy(rh, rk, rh, r); ++r; }
      PIN();
    }
  };
  auto publish = [&](int ip) {
    u32x4 o[5];
#pragma unroll
    for (int t = 0; t < 2; ++t) {
      const int i = 2 * ip + t;
#pragma unroll
      for (int j = 0; j < 5; ++j) {
        const int n = j < 4 ? 2 * t + (j >> 1) : 4, e = j < 4 ? (j & 1) * 2 : 2 * t;
        o[n][e] = pk_bf16(acc[i][j][0], acc[i][j][1]); o[n][e + 1] = pk_bf16(acc[i][j][2], acc[i][j][3]);
      }
    }
    if (w == split) {
#pragma unroll
      for (int n = 0; n < 5; ++n) *(LAS u32x4*)(smem + F_Y + (n < 4 ? ip * 4096 + n * 1024 : 8192 + ip * 1024) + lane * 16) = o[n];
    } else {
      const unsigned dst = WS_P + ((tile * 4 + w) * 3 + (split ^ w) - 1) * 10240 + lane * 16;
#pragma unroll
      for (int n = 0; n < 5; ++n) st16c(rw, dst + (n < 4 ? ip * 4096 + n * 1024 : 8192 + ip * 1024), o[n]);
    }
  };
#pragma unroll
  for (int i = 0; i < 4; ++i)
#pragma unroll
    for (int j = 0; j < 5; ++j) acc[i][j] = (f32x4){0, 0, 0, 0};
  after_mfma(acc);
  BAR();
#pragma unroll
  for (int r = 0; r < 9; ++r) rdy(0, 0, 0, r);
  WAIT_L0(); PIN();
#pragma unroll 1
  for (int it = 0; it < NY / 4; ++it) {
    sfor<0, 4>([&](auto K) __attribute__((always_inline)) {
      constexpr int k = decltype(K)::v;
      seg(K, IC<0>{});
      if (k == 2 && it == 0) {
        wait_v<0>();
        if (lane == 0) st_flag(FLAG(C_FH + mt1 * 64 + ns * 4 + w), tag);
      }
      WAIT_L0();
      if (k < 3 || it < NY / 4 - 1) BAR();
      PIN();
      seg(K, IC<1>{});
      WAIT_L0(); PIN();
    });
  }
  MFMA_DONE();
  after_mfma(acc);
  __builtin_amdgcn_s_setprio(3);
  publish(0); publish(1);
  if (w != split) {
    wait_v<0>();
    if (lane == 0) st_flag(FLAG(C_FP + (tile * 4 + w) * 4 + (split ^ w) - 1), tag);
  }
  const int orow = mt * 128 + (split & 1) * 64 + w * 16, ocol = nt * 160 + (split >> 1) * 80;
  u32x4 xv[3];
  unsigned tr[3], oo[3];
#pragma unroll
  for (int i = 0; i < 3; ++i) {
    const int n = min(i * 64 + lane, 159);
    const unsigned off = ((orow + n / 10) * D + ocol + (n % 10) * 8) * 2;
    xv[i] = *(const u32x4*)((const char*)P.x + off);
    tr[i] = F_T + w * (16 * FPITCH) + (n / 10) * FPITCH + (n % 10) * 16;
    oo[i] = i * 64 + lane < 160 ? off : 0xfffffff0u;
  }
  if (w == split) {
    const u64* fp = FLAG(C_FP + (tile * 4 + split) * 4 + min(lane, 2));
    __builtin_amdgcn_s_sleep(60);
#pragma unroll 1
    for (int it = 0; it < 40000; ++it) {
      if (__all(ld_flag(fp) == tag)) break;
      __builtin_amdgcn_s_sleep(8);
    }
    WAIT_L0();
    if (lane == 0) *(volatile LAS unsigned*)(smem + T_GO) = 2;
  } else {
    __builtin_amdgcn_s_setprio(0);
#pragma unroll 1
    for (int it = 0; it < 4000000; ++it) {
      if (*(volatile LAS unsigned*)(smem + T_GO) == 2) break;
      __builtin_amdgcn_s_sleep(4);
    }
    __builtin_amdgcn_s_setprio(3);
  }
  asm volatile("" ::: "memory");
  f32x4 ys[5];
  {
    u32x4 pa[3][2]; u32x2 pb[3];
#pragma unroll
    for (int s = 0; s < 3; ++s) {
      const unsigned src = WS_P + ((tile * 4 + split) * 3 + s) * 10240 + lane * 16;
      pa[s][0] = ld16c(rw, src + w * 2048); pa[s][1] = ld16c(rw, src + w * 2048 + 1024);
      pb[s] = __builtin_amdgcn_raw_buffer_load_b64(rw, src + 8192 + (w >> 1) * 1024 + (w & 1) * 8, 0, 16);
    }
#pragma unroll
    for (int j = 0; j < 5; ++j) {
#pragma unroll
      for (int s = 0; s < 3; ++s) {
        const unsigned lo = j < 4 ? pa[s][j >> 1][(j & 1) * 2] : pb[s][0], hi = j < 4 ? pa[s][j >> 1][(j & 1) * 2 + 1] : pb[s][1];
        const f32x4 v = {bflo(lo), bfhi(lo), bflo(hi), bfhi(hi)};
        ys[j] = s ? ys[j] + v : v;
      }
      asm volatile("" : "+v"(ys[j]));
    }
  }
  {
    LAS char* po = smem + F_Y + lane * 16;
    const u32x4 oa0 = *(LAS u32x4*)(po + w * 2048), oa1 = *(LAS u32x4*)(po + w * 2048 + 1024);
    const u32x2 ob = *(LAS u32x2*)(po + 8192 + (w >> 1) * 1024 + (w & 1) * 8);
#pragma unroll
    for (int j = 0; j < 5; ++j) {
      const unsigned l0 = j < 2 ? oa0[(j & 1) * 2] : j < 4 ? oa1[(j & 1) * 2] : ob[0], h0 = j < 2 ? oa0[(j & 1) * 2 + 1] : j < 4 ? oa1[(j & 1) * 2 + 1] : ob[1];
      ys[j] += (f32x4){bflo(l0), bfhi(l0), bflo(h0), bfhi(h0)};
      asm volatile("" : "+v"(ys[j]));
    }
  }
  unsigned tw = F_T + w * (16 * FPITCH) + r15 * FPITCH + q * 8, rd0 = w * 5120 + lane * 16, rd2 = rd0 + L_B;
  asm volatile("" : "+v"(tw), "+v"(rd0), "+v"(rd2), "+v"(tr[0]), "+v"(tr[1]), "+v"(tr[2]), "+v"(oo[0]), "+v"(oo[1]), "+v"(oo[2]));
  BAR();
  f32x4 dd[4][5];
#pragma unroll
  for (int j = 0; j < 5; ++j)
#pragma unroll
    for (int u = 0; u < 4; ++u) dd[u][j] = *(LAS f32x4*)(smem + ((u & 2) ? rd2 : rd0) + (u & 1) * 20480 + j * 1024);
#pragma unroll
  for (int j = 0; j < 5; ++j) {
    const f32x4 d = (dd[0][j] + dd[1][j]) + (dd[2][j] + dd[3][j]);
    f32x4 e = d * -1.4426950408889634f;
#pragma unroll
    for (int r = 0; r < 4; ++r) e[r] = __builtin_amdgcn_exp2f(e[r]);
    e = e + 1.0f;
#pragma unroll
    for (int r = 0; r < 4; ++r) e[r] = __builtin_amdgcn_rcpf(e[r]);
    e = e * d * ys[j];
    *(LAS u32x2*)(smem + tw + j * 32) = (u32x2){pk_bf16(e[0], e[1]), pk_bf16(e[2], e[3])};
  }
  WAIT_L0();
  const __amdgpu_buffer_rsrc_t ro = __builtin_amdgcn_make_buffer_rsrc(P.out, 0, S * D * 2, 0x00020000);
#pragma unroll
  for (int i = 0; i < 3; ++i) {
    const u32x4 v = *(LAS u32x4*)(smem + tr[i]);
    u32x4 o;
#pragma unroll
    for (int d = 0; d < 4; ++d) o[d] = pk_bf16(bflo(v[d]) + bflo(xv[i][d]), bfhi(v[d]) + bfhi(xv[i][d]));
    __builtin_amdgcn_raw_buffer_store_b128(o, ro, oo[i], 0, 0);
  }
}

__global__ __launch_bounds__(512, 2)
void fused(Params P) {
  extern __shared__ __attribute__((aligned(16))) char smem_[];
  LAS char* smem = (LAS char*)smem_;
  const int tid = threadIdx.x, lane = tid & 63, w = __builtin_amdgcn_readfirstlane(tid >> 6);
  const int bid = blockIdx.x, xcd = bid & 7, idx = bid >> 3;
  u64 tag;
  if (w == 3 && (idx & 8)) {
    u64 pc; unsigned len;
    asm volatile("s_getpc_b64 %0\n.Lcodepf:\n\ts_mov_b32 %1, code_end_marker-.Lcodepf" : "=s"(pc), "=s"(len));
    const unsigned off = ((idx & 7) * 64 + lane) * 128;
    if (off + 4 <= len) touch_lines((unsigned)(size_t)smem + T_PF, off, (const char*)pc);
  }
  const int mt1 = (xcd & 1) * 8 + (idx & 7), j = idx >> 3, ns = (j & 1) * 8 + (xcd >> 1) * 2 + (j >> 1);
  const int split = xcd >> 1, mt2 = (xcd & 1) * 8 + (idx >> 2), nt = idx & 3;
  if (w < 4) {
    if (j & 1) mfma_p1<false>(P, smem, lane, w, mt1, ns, tag); else mfma_p1<true>(P, smem, lane, w, mt1, ns, tag);
    mfma_p2(P, smem, lane, w, split, mt2, nt, mt1, ns, tag);
  } else {
    helper(P, smem, lane, w & 3, mt1, ns, split, mt2, nt);
    __builtin_amdgcn_s_waitcnt(0);
  }
}

extern "C" __global__ void code_end_marker() {}

extern "C" void kernel_launch(void* const* d_in, const int* in_sizes, int n_in, void* d_out, int out_size, void* d_ws, size_t ws_size, hipStream_t stream) {
  Params P;
  P.x = (const bf16_t*)d_in[0]; P.wsr = (const bf16_t*)d_in[1]; P.wsi = (const bf16_t*)d_in[2];
  P.wdr = (const bf16_t*)d_in[3]; P.wdi = (const bf16_t*)d_in[4];
  P.wg = (const bf16_t*)d_in[6]; P.wu = (const bf16_t*)d_in[7]; P.wdn = (const bf16_t*)d_in[8];
  P.out = (bf16_t*)d_out; P.ws = (char*)d_ws;
  (void)hipFuncSetAttribute((const void*)fused, hipFuncAttributeMaxDynamicSharedMemorySize, LDS_BYTES);
  fused<<<256, 512, LDS_BYTES, stream>>>(P);
}
```
